```python
import torch

def _softmax(x, axis=-1):
    return torch.softmax(x, dim=axis)
def _gelu(x):
    return torch.nn.functional.gelu(x, approximate="tanh")
def _silu(x):
    return torch.nn.functional.silu(x)
def _sigmoid(x):
    return torch.sigmoid(x)
def _layernorm(x, axis=-1, eps=1e-5):
    m = x.mean(dim=axis, keepdim=True)
    v = x.var(dim=axis, keepdim=True, unbiased=False)
    return (x - m) / torch.sqrt(v + eps)
def _scatter_add(tgt, idx, src, axis=0):
    return tgt.clone().index_add_(axis, idx, src)
def _segment_sum(src, seg, n):
    out = torch.zeros((n,) + src.shape[1:], dtype=src.dtype)
    return out.index_add_(0, seg, src)
def _pad(x, pw):
    flat = tuple(v for pair in reversed(pw) for v in pair)
    return torch.nn.functional.pad(x, flat)

kernel_name = 'sequential_preLN__sliding_local_dilated__glu_gated_reglu__prompt'

def setup_inputs():

    _w = (lambda a: (lambda t: t.to(torch.bfloat16) if t.is_floating_point() else t)(torch.as_tensor(a)))
    def _A_setup():
        B, S, D = 1, 2048, 512
        return {
            "x": _w((lambda *s: torch.randn(*s, dtype=torch.bfloat16) * 0.05)(B, S, D)),
            "wqkv": _w((lambda *s: torch.randn(*s, dtype=torch.bfloat16) * 0.05)(D, 3 * D)),
            "wo": _w((lambda *s: torch.randn(*s, dtype=torch.bfloat16) * 0.05)(D, D)),
        }
    _A_in = _A_setup()

    def _B_setup():
        B, S, D, F = 1, 2048, 512, 1376
        return {
            "x": _w((lambda *s: torch.randn(*s, dtype=torch.bfloat16) * 0.05)(B, S, D)),
            "W_gate": _w((lambda *s: torch.randn(*s, dtype=torch.bfloat16) * 0.8)(D, F) * (2.0 / D) ** 0.5),
            "W_up": _w((lambda *s: torch.randn(*s, dtype=torch.bfloat16) * 0.8)(D, F) * (2.0 / D) ** 0.5),
            "W_down": _w((lambda *s: torch.randn(*s, dtype=torch.bfloat16) * 0.8)(F, D) * (1.0 / F) ** 0.5),
        }
    _B_in = _B_setup()

    return {
        "A_x": _A_in["x"],
        "A_wqkv": _A_in["wqkv"],
        "A_wo": _A_in["wo"],
        "B_x": _B_in["x"],
        "B_W_gate": _B_in["W_gate"],
        "B_W_up": _B_in["W_up"],
        "B_W_down": _B_in["W_down"],
    }

def reference(A_x, A_wqkv, A_wo, B_x, B_W_gate, B_W_up, B_W_down):
    def _A_ref(x, wqkv, wo):
        B, S, D = x.shape
        H = 32; Dh = D // H
        W, dil = 8, 2
        qkv = (lambda x, *d: x.permute(*d))((x @ wqkv).reshape(B, S, 3, H, Dh), 2, 0, 3, 1, 4)
        q, k, v = qkv[0], qkv[1], qkv[2]
        scale = Dh ** -0.5
        pos = torch.arange(S)
        off = torch.arange(W) * dil
        idx = pos[:, None] - off[None, :]
        mask = idx >= 0
        idx_c = torch.where(mask, idx, 0).reshape(-1)
        kg = k[:, :, idx_c, :].reshape(B, H, S, W, Dh)
        vg = v[:, :, idx_c, :].reshape(B, H, S, W, Dh)
        logits = torch.einsum("bhsd,bhswd->bhsw", q, kg) * scale
        logits = torch.where(mask[None, None, :, :], logits, -1e9)
        attn = _softmax(logits, axis=-1)
        o = torch.einsum("bhsw,bhswd->bhsd", attn, vg)
        return (lambda x, *d: x.permute(*d))(o, 0, 2, 1, 3).reshape(B, S, D) @ wo

    def _B_ref(x, W_gate, W_up, W_down):
        gate = torch.einsum("bsd,df->bsf", x, W_gate)
        up = torch.einsum("bsd,df->bsf", x, W_up)
        gate = gate * (lambda x, dt: x.to(dt))(gate > 0, gate.dtype)
        h = gate * up
        return torch.einsum("bsf,fd->bsd", h, W_down)

    x = A_x
    h = x + _A_ref(_layernorm(x, axis=-1), A_wqkv, A_wo)
    h2 = h + _B_ref(_layernorm(h, axis=-1), B_W_gate, B_W_up, B_W_down)
    return h2
```

```cpp
#include <hip/hip_runtime.h>
#include <hip/hip_bf16.h>
#include <stdint.h>

typedef unsigned short u16;
__device__ __forceinline__ float bf2f(u16 b) { return __uint_as_float(((unsigned)b) << 16); }
__device__ __forceinline__ u16 f2bf(float f) { unsigned u = __float_as_uint(f); u += 0x7FFFu + ((u >> 16) & 1u); return (u16)(u >> 16); }
__device__ __forceinline__ float rbf(float f) { return bf2f(f2bf(f)); }

constexpr int S = 2048, D = 512, F = 1376;

__global__ void k_ln(const u16* x, u16* y) {
    int row = blockIdx.x * 4 + (threadIdx.x >> 6), lane = threadIdx.x & 63;
    float v[8]; float s = 0.f;
    for (int j = 0; j < 8; ++j) { v[j] = bf2f(x[row * D + lane * 8 + j]); s += v[j]; }
    for (int o = 32; o >= 1; o >>= 1) s += __shfl_xor(s, o);
    float mean = s / D; float q = 0.f;
    for (int j = 0; j < 8; ++j) { float d = v[j] - mean; q += d * d; }
    for (int o = 32; o >= 1; o >>= 1) q += __shfl_xor(q, o);
    float var = q / D;
    float mb = rbf(mean), vb = rbf(var);
    float sd = rbf(sqrtf(rbf(vb + 1e-5f)));
    for (int j = 0; j < 8; ++j) y[row * D + lane * 8 + j] = f2bf(rbf(v[j] - mb) / sd);
}

__global__ void k_gemm(const u16* A, const u16* B, u16* C, const u16* res, int M, int N, int K) {
    int n = blockIdx.x * 64 + (threadIdx.x & 63), m = blockIdx.y * 4 + (threadIdx.x >> 6);
    if (n >= N) return;
    float acc = 0.f;
    for (int k = 0; k < K; ++k) acc += bf2f(A[m * K + k]) * bf2f(B[k * N + n]);
    float r = rbf(acc);
    if (res) r = r + bf2f(res[m * N + n]);
    C[m * N + n] = f2bf(r);
}

__global__ void k_attn(const u16* qkv, u16* o) {
    int i = blockIdx.x * 256 + threadIdx.x;
    int t = i >> 5, h = i & 31;
    float q[16];
    for (int d = 0; d < 16; ++d) q[d] = bf2f(qkv[t * 1536 + h * 16 + d]);
    float lg[8]; float mx = -1e30f;
    for (int w = 0; w < 8; ++w) {
        int idx = t - 2 * w;
        if (idx >= 0) { float s = 0.f; for (int d = 0; d < 16; ++d) s += q[d] * bf2f(qkv[idx * 1536 + 512 + h * 16 + d]); lg[w] = rbf(rbf(s) * 0.25f); }
        else lg[w] = rbf(-1e9f);
        mx = fmaxf(mx, lg[w]);
    }
    float sum = 0.f;
    for (int w = 0; w < 8; ++w) { lg[w] = __expf(lg[w] - mx); sum += lg[w]; }
    float ov[16] = {};
    for (int w = 0; w < 8; ++w) {
        int idx = t - 2 * w; if (idx < 0) idx = 0;
        float p = rbf(lg[w] / sum);
        for (int d = 0; d < 16; ++d) ov[d] += p * bf2f(qkv[idx * 1536 + 1024 + h * 16 + d]);
    }
    for (int d = 0; d < 16; ++d) o[t * 512 + h * 16 + d] = f2bf(ov[d]);
}

__global__ void k_glu(const u16* g, const u16* u, u16* h, int n) {
    int i = blockIdx.x * 256 + threadIdx.x; if (i >= n) return;
    float gg = bf2f(g[i]); gg = gg > 0.f ? gg : 0.f * gg;
    h[i] = f2bf(gg * bf2f(u[i]));
}

extern "C" void kernel_launch(void* const* d_in, const int* in_sizes, int n_in, void* d_out, int out_size, void* d_ws, size_t ws_size, hipStream_t stream) {
    const u16* x = (const u16*)d_in[0];
    const u16* wqkv = (const u16*)d_in[1];
    const u16* wo = (const u16*)d_in[2];
    const u16* wg = (const u16*)d_in[4];
    const u16* wu = (const u16*)d_in[5];
    const u16* wd = (const u16*)d_in[6];
    u16* out = (u16*)d_out;
    u16* ws = (u16*)d_ws;
    u16* ln = ws;
    u16* qkv = ln + S * D;
    u16* o = qkv + S * 3 * D;
    u16* h = o + S * D;
    u16* g = h + S * D;
    u16* u = g + S * F;
    u16* hid = u + S * F;
    k_ln<<<S / 4, 256, 0, stream>>>(x, ln);
    k_gemm<<<dim3(1536 / 64, S / 4), 256, 0, stream>>>(ln, wqkv, qkv, nullptr, S, 1536, D);
    k_attn<<<S * 32 / 256, 256, 0, stream>>>(qkv, o);
    k_gemm<<<dim3(512 / 64, S / 4), 256, 0, stream>>>(o, wo, h, x, S, 512, D);
    k_ln<<<S / 4, 256, 0, stream>>>(h, ln);
    k_gemm<<<dim3((F + 63) / 64, S / 4), 256, 0, stream>>>(ln, wg, g, nullptr, S, F, D);
    k_gemm<<<dim3((F + 63) / 64, S / 4), 256, 0, stream>>>(ln, wu, u, nullptr, S, F, D);
    k_glu<<<(S * F + 255) / 256, 256, 0, stream>>>(g, u, hid, S * F);
    k_gemm<<<dim3(512 / 64, S / 4), 256, 0, stream>>>(hid, wd, out, h, S, 512, F);
}
```

```cpp
#include <hip/hip_runtime.h>
#include <hip/hip_bf16.h>
#include <stdint.h>
#pragma clang diagnostic ignored "-Wint-to-pointer-cast"

#define LAS __attribute__((address_space(3)))
#define GAS __attribute__((address_space(1)))
typedef unsigned short u16;
typedef unsigned long long u64;
typedef short s16x4 __attribute__((ext_vector_type(4)));
typedef __bf16 bf16x8 __attribute__((ext_vector_type(8)));
typedef __bf16 bf16x2 __attribute__((ext_vector_type(2)));
typedef float f32x4 __attribute__((ext_vector_type(4)));
typedef float f32x2 __attribute__((ext_vector_type(2)));
typedef unsigned u32x4 __attribute__((ext_vector_type(4)));
typedef unsigned u32x2 __attribute__((ext_vector_type(2)));
typedef GAS u64 gu64;
typedef __amdgpu_buffer_rsrc_t rsrc_t;

namespace {

constexpr int SEQ = 2048, DM = 512, FF = 1376;
constexpr int NKS4 = FF / 32;
constexpr unsigned WS_O = 0;
constexpr unsigned WS_H = 2u << 20;
constexpr unsigned WS_HID = 4u << 20;
constexpr unsigned WS_ST = 10u << 20;
constexpr unsigned WS_FLAG = 12u << 20;
constexpr unsigned WS_BYTES = WS_FLAG + 16 * 64 * 256;
constexpr int FL_GEN = 0, FL_1 = 8, FL_2 = 24, FL_3 = 40;
constexpr int AUX_SC1 = 16;
constexpr unsigned kSpinCap = 1u << 16;

constexpr int LDS_BYTES = 163840;
constexpr int L1_X = 0, L1_H = 65536, L1_W = 81920, L1_WSLOT = 24576, L1_SCR = 155648;
constexpr int L2_WO = 81920, L2_RED = 0;
constexpr int L3_A = 0, L3_B = 49152, L3_BSLOT = 12288, L3_ST = 147456;

__device__ __forceinline__ constexpr int vm_imm(int n) { return (n & 0xF) | ((n >> 4) << 14) | (0x7 << 4) | (0xF << 8); }
#define WAIT_VM(n) asm volatile("s_waitcnt vmcnt(%0)" :: "i"(n) : "memory")
#define BAR() do { asm volatile("s_waitcnt lgkmcnt(0)" ::: "memory"); __builtin_amdgcn_s_barrier(); asm volatile("" ::: "memory"); } while (0)
#define PIN(x) asm volatile("" : "+v"(x))
#define DMA(m0b, ldsoff, voff, rs, sbase, soffimm) do { unsigned t_; asm volatile("s_add_u32 m0, %1, %2\n\ts_add_u32 %0, %5, %6\n\tbuffer_load_dwordx4 %3, %4, %0 offen lds" \
    : "=&s"(t_) : "s"(m0b), "i"(ldsoff), "v"(voff), "s"(rs), "s"(sbase), "i"(soffimm) : "scc", "memory"); } while (0)
#define DMA_SC1(m0b, ldsoff, voff, rs, sbase, soffimm) do { unsigned t_; asm volatile("s_add_u32 m0, %1, %2\n\ts_add_u32 %0, %5, %6\n\tbuffer_load_dwordx4 %3, %4, %0 offen sc1 lds" \
    : "=&s"(t_) : "s"(m0b), "i"(ldsoff), "v"(voff), "s"(rs), "s"(sbase), "i"(soffimm) : "scc", "memory"); } while (0)

__device__ __forceinline__ rsrc_t mk_rsrc(const void* p, unsigned bytes) { return __builtin_amdgcn_make_buffer_rsrc((void*)p, 0, bytes, 0x00020000); }
__device__ __forceinline__ float u2f(unsigned u) { return __builtin_bit_cast(float, u); }
__device__ __forceinline__ unsigned f2u(float f) { return __builtin_bit_cast(unsigned, f); }
__device__ __forceinline__ unsigned pk_bf16(float a, float b) { bf16x2 v; v[0] = (__bf16)a; v[1] = (__bf16)b; return __builtin_bit_cast(unsigned, v); }
__device__ __forceinline__ float bf_lo(unsigned u) { return u2f(u << 16); }
__device__ __forceinline__ float bf_hi(unsigned u) { return u2f(u & 0xffff0000u); }
__device__ __forceinline__ float rbf(float f) { return (float)(__bf16)f; }
__device__ __forceinline__ f32x4 mfma32(u32x4 a, u32x4 b, f32x4 c) {
    return __builtin_amdgcn_mfma_f32_16x16x32_bf16(__builtin_bit_cast(bf16x8, a), __builtin_bit_cast(bf16x8, b), c, 0, 0, 0);
}
__device__ __forceinline__ f32x4 mfma16(u32x2 a, u32x2 b, f32x4 c) {
    return __builtin_amdgcn_mfma_f32_16x16x16bf16_1k(__builtin_bit_cast(s16x4, a), __builtin_bit_cast(s16x4, b), c, 0, 0, 0);
}
__device__ __forceinline__ u32x4 lds128(unsigned a) { return *(LAS u32x4*)a; }
__device__ __forceinline__ f32x4 lds128f(unsigned a) { return *(LAS f32x4*)a; }
__device__ __forceinline__ f32x2 lds64f(unsigned a) { return *(LAS f32x2*)a; }
__device__ __forceinline__ u32x2 tr_read(unsigned a) { return __builtin_bit_cast(u32x2, __builtin_amdgcn_ds_read_tr16_b64_v4i16((LAS s16x4*)a)); }
__device__ __forceinline__ u32x4 tr_frag(unsigned a, int step) { const u32x2 x = tr_read(a), y = tr_read(a + step); return (u32x4){x.x, x.y, y.x, y.y}; }
__device__ __forceinline__ float swap16_lo(float v) {
    const unsigned a = f2u(v); auto r = __builtin_amdgcn_permlane16_swap(a, a, false, false); const unsigned x = r[0]; return u2f(x);
}
__device__ __forceinline__ float xmax16_32(float v) {
    unsigned a = f2u(v);
    auto r = __builtin_amdgcn_permlane16_swap(a, a, false, false);
    const unsigned r0 = r[0], r1 = r[1];
    v = __builtin_fmaxf(u2f(r0), u2f(r1));
    a = f2u(v);
    auto s = __builtin_amdgcn_permlane32_swap(a, a, false, false);
    const unsigned s0 = s[0], s1 = s[1];
    return __builtin_fmaxf(u2f(s0), u2f(s1));
}
__device__ __forceinline__ float bcast_row0(float v) { return __builtin_amdgcn_update_dpp(0.f, v, 0x150, 0xf, 0xf, false); }
__device__ __forceinline__ float diag4(f32x4 d, unsigned lane) { const float a = (lane & 1) ? d[1] : d[0], b = (lane & 1) ? d[3] : d[2]; return (lane & 2) ? b : a; }
__device__ __forceinline__ f32x2 ln_stats(float s, float q) {
    const float mean = s * (1.0f / DM);
    const float var = __builtin_fmaxf(q * (1.0f / DM) - mean * mean, 0.0f);
    const float sd = rbf(__builtin_amdgcn_sqrtf(rbf(rbf(var) + 1e-5f)));
    return (f32x2){rbf(mean), __builtin_amdgcn_rcpf(sd)};
}
__device__ __forceinline__ void wait_flags(gu64* base, unsigned n, u64 epoch, unsigned lane) {
    for (unsigned spins = 0; spins < kSpinCap; ++spins) {
        u64 v = epoch;
        if (lane < n) v = __hip_atomic_load(base + lane * 32, __ATOMIC_RELAXED, __HIP_MEMORY_SCOPE_AGENT);
        if (__all(v == epoch)) break;
    }
    __builtin_amdgcn_fence(__ATOMIC_ACQUIRE, "wavefront");
}

struct Params { const u16* x; const u16* wqkv; const u16* wo; const u16* wg; const u16* wu; const u16* wd; u16* out; unsigned char* ws; };

#define STAMP(i) do {} while (0)

__global__ void __launch_bounds__(512, 2) __attribute__((amdgpu_waves_per_eu(2, 2))) fused_block(Params P
) {
    const unsigned tid = threadIdx.x, lane = tid & 63;
    const unsigned w = __builtin_amdgcn_readfirstlane(tid >> 6);
    const unsigned b = blockIdx.x, xcd = b & 7, jj = b >> 3;
    const unsigned p = 2 * xcd + (jj >> 4), c = jj & 15;
    const unsigned hf = c >> 3, hg = c & 7;
    const unsigned r16 = lane & 15, g = lane >> 4, l8 = lane >> 3, l7 = lane & 7, q4 = r16 >> 2, p4 = lane & 3;
    STAMP(0);
    const unsigned wk = w * 1024;
    const rsrc_t rs_x = mk_rsrc(P.x, SEQ * DM * 2);
    const rsrc_t rs_w = mk_rsrc(P.wqkv, DM * 3 * DM * 2);
    gu64* const flags = (gu64*)(P.ws + WS_FLAG) + (size_t)p * 64 * 32;
    const unsigned T0 = 128 * p + 64 * hf;
    STAMP(18);
    const u32x4 ONES = {0x3F803F80u, 0x3F803F80u, 0x3F803F80u, 0x3F803F80u};
    const f32x4 Z4 = {0.f, 0.f, 0.f, 0.f};
    const unsigned fsw = ((q4 >> 1) & 1) | ((g & 1) << 1);
    const unsigned wsw = (l7 ^ (((g & 1) | ((w & 1) << 1)) << 1)) * 16;
    u64 gen = 0;

    {
        const unsigned rot = (hg + 2 * (2 * (p & 1) + hf) + xcd) & 7;
        const unsigned hh = w & 3, par = w >> 2;
        const unsigned vh = l8 * 2048 + ((l7 ^ g) * 16);
        const unsigned vxr = vh ^ ((w & 1) * 64);
        const unsigned sxr = (T0 + 32 * ((w >> 1) & 1) + (w >> 2) + 16 * (w & 1)) * 1024;
        const unsigned vw = (8 * w + l8) * 3072 + wsw + hg * 128;
        {
            const unsigned kap = (w + rot) & 7;
            const unsigned sh = (T0 == 0 ? 0 : T0 - 16) * 1024 + kap * 128;
            const unsigned hb = L1_H + w * 2048;
            if (lane >= 8) { DMA(hb, 0, vh, rs_x, sh, 0); const unsigned vh1 = vh ^ 64; DMA(hb, 1024, vh1, rs_x, sh, 1024); }
            else { *(LAS u32x4*)(hb + lane * 16) = ONES; *(LAS u32x4*)(hb + 1024 + lane * 16) = ONES; }
        }
        unsigned kx[8];
#pragma unroll
        for (int q = 0; q < 8; ++q) kx[q] = (q + rot) & 7;
#define ISSUE_X(q) DMA(wk, L1_X + (q) * 8192, vxr, rs_x, sxr + kx[q] * 128, 0)
#define ISSUE_W1(q, mat) DMA(wk, L1_W + ((q) % 3) * L1_WSLOT + (mat) * 8192, vw, rs_w, kx[q] * 196608, (mat) * 1024)
#define ISSUE_W(q) do { ISSUE_W1(q, 0); ISSUE_W1(q, 1); ISSUE_W1(q, 2); } while (0)
        ISSUE_X(0); ISSUE_W(0);
        __builtin_amdgcn_s_barrier();
        ISSUE_X(1); ISSUE_W(1);
        __builtin_amdgcn_s_barrier();
        ISSUE_X(2); ISSUE_W(2);
        __builtin_amdgcn_s_barrier();
        ISSUE_X(3); ISSUE_X(4); ISSUE_X(5); ISSUE_X(6); ISSUE_X(7);
        u64 genv;
        { const gu64* gp = flags + FL_GEN * 32; asm volatile("global_load_dwordx2 %0, %1, off sc1" : "=v"(genv) : "v"(gp) : "memory"); }
        STAMP(17);
        const unsigned xa = (r16 >> 3) * 1024 + (r16 & 7) * 128 + ((g ^ ((r16 >> 1) & 7)) * 16);
        const unsigned xc0 = xa + par * 4096, xc1 = xc0 ^ 64;
        const unsigned xh0 = xa + L1_H, xh1 = xh0 ^ 64;
        const unsigned wa = L1_W + (8 * g + q4) * 128 + ((hh ^ fsw) * 32) + 8 * p4, wa2 = wa + 2 * L1_WSLOT;
        const unsigned sxa = (xa ^ ((hh & 1) * 64));
        const unsigned stc = sxa + par * 4096 + (hh >> 1) * 8192, sth = sxa + L1_H + (hh >> 1) * 2048;
        f32x4 mh, mp, mc;
#pragma unroll
        for (int r = 0; r < 4; ++r) {
            const unsigned kap = 4 * g + r;
            mh[r] = (T0 > 0 && (kap >> 3) == par && (kap & 7) >= r16 + 1) ? 0.f : -1e30f;
            mp[r] = (kap >= r16 + 9) ? 0.f : -1e30f;
            mc[r] = (kap <= r16 && r16 - kap <= 7) ? 0.f : -1e30f;
        }
        PIN(mh); PIN(mp); PIN(mc);

        f32x4 aq[3] = {Z4, Z4, Z4}, ak[3] = {Z4, Z4, Z4}, av[3] = {Z4, Z4, Z4};
        f32x4 gr[3] = {Z4, Z4, Z4}, rs[3] = {Z4, Z4, Z4};
        f32x2 sL[3]; f32x4 sR[3][2];
        const unsigned part = L1_SCR, fin = L1_SCR + 2560 + w * 384;
        u32x4 xf[2][3], wf[2][3], sf[3];
        const rsrc_t rs_wo = mk_rsrc(P.wo, DM * DM * 2);
        const unsigned vwo = (8 * w + l8) * 1024 + wsw + hg * 128;
#define LOAD1(s) do { const int kq_ = (s) >> 1, kk_ = (s) & 1; \
            xf[(s) & 1][0] = lds128((kk_ ? xh1 : xh0) + kq_ * 2048); xf[(s) & 1][1] = lds128((kk_ ? xc1 : xc0) + kq_ * 8192); xf[(s) & 1][2] = lds128((kk_ ? xc1 : xc0) + kq_ * 8192 + 2048); \
            _Pragma("unroll") for (int mat = 0; mat < 3; ++mat) wf[(s) & 1][mat] = tr_frag((kq_ % 3 == 2 ? wa2 : wa + (kq_ % 3) * L1_WSLOT) + mat * 8192 + kk_ * 4096, 512); } while (0)
        WAIT_VM(14);
        BAR();
        STAMP(1);
        LOAD1(0);
#pragma unroll
        for (int s = 0; s < 16; ++s) {
            if (s & 1) {
                if (s == 1) WAIT_VM(10); else if (s == 3) WAIT_VM(9); else if (s < 13) WAIT_VM(3); else if (s == 13) WAIT_VM(2);
                if (s < 15) BAR();
                if (s == 5) { asm volatile("" : "+v"(genv)); gen = ((u64)(unsigned)__builtin_amdgcn_readfirstlane((unsigned)(genv >> 32)) << 32) | (unsigned)__builtin_amdgcn_readfirstlane((unsigned)genv); }
            }
            if (s < 15) LOAD1(s + 1);
            if (s >= 6 && s <= 9) { const int j = s - 6; sf[0] = lds128(sth + j * 4096); sf[1] = lds128(stc + j * 16384); sf[2] = lds128(stc + j * 16384 + 2048); }
            f32x2 pp[4];
            if (s == 11) {
                const unsigned pa = part + ((g == 0 ? 0 : (g == 3 ? 0 : 2 * par + g)) * 64 + r16) * 8;
                pp[0] = lds64f(pa); pp[1] = lds64f(pa + 128); pp[2] = lds64f(pa + 256); pp[3] = lds64f(pa + 384);
            }
            if (s == 13) {
#pragma unroll
                for (int t = 0; t < 3; ++t) { sL[t] = lds64f(fin + (t * 16 + r16) * 8); sR[t][0] = lds128f(fin + (t * 16 + 4 * g) * 8); sR[t][1] = lds128f(fin + (t * 16 + 4 * g) * 8 + 16); }
            }
            __builtin_amdgcn_sched_barrier(0);
#pragma unroll
            for (int t = 0; t < 3; ++t) {
                aq[t] = mfma32(wf[s & 1][0], xf[s & 1][t], aq[t]); ak[t] = mfma32(wf[s & 1][1], xf[s & 1][t], ak[t]); av[t] = mfma32(xf[s & 1][t], wf[s & 1][2], av[t]);
                __builtin_amdgcn_sched_barrier(0);
                if ((s & 1) && s <= 9) ISSUE_W1((s + 5) / 2, t);
                if (s == 11 && t < 2) DMA(wk, L2_WO + (6 + t) * 8192, vwo, rs_wo, 0u, (6 + t) * 65536);
                if (s == 13) DMA(wk, L2_WO + t * 8192, vwo, rs_wo, 0u, t * 65536);
                __builtin_amdgcn_sched_barrier(0);
            }
            if (s >= 6 && s <= 9) {
#pragma unroll
                for (int t = 0; t < 3; ++t) { gr[t] = mfma32(sf[t], sf[t], gr[t]); rs[t] = mfma32(ONES, sf[t], rs[t]); }
            }
            if (s == 10 && g == q4) {
                *(LAS f32x2*)(part + (hh * 16 + r16) * 8) = (f32x2){rs[0][0], diag4(gr[0], lane)};
                *(LAS f32x2*)(part + (((1 + 2 * par) * 4 + hh) * 16 + r16) * 8) = (f32x2){rs[1][0], diag4(gr[1], lane)};
                *(LAS f32x2*)(part + (((2 + 2 * par) * 4 + hh) * 16 + r16) * 8) = (f32x2){rs[2][0], diag4(gr[2], lane)};
            }
            if (s == 11) {
                const f32x2 sm = (pp[0] + pp[1]) + (pp[2] + pp[3]);
                const f32x2 res = ln_stats(sm.x, sm.y);
                if (g < 3) *(LAS f32x2*)(fin + (g * 16 + r16) * 8) = res;
            }
            __builtin_amdgcn_sched_barrier(0);
        }
        STAMP(2);
        f32x4 csq, csk;
#pragma unroll
        for (int r = 0; r < 4; ++r) { csq[r] = bcast_row0(aq[0][r]); csk[r] = bcast_row0(ak[0][r]); }
        const float csv = swap16_lo(av[0][0]);
        u32x2 qb[2], kb[3], vb[3];
#pragma unroll
        for (int t = 0; t < 3; ++t) {
            const f32x4 kv = (ak[t] - sL[t].x * csk) * sL[t].y;
            kb[t] = (u32x2){pk_bf16(kv[0], kv[1]), pk_bf16(kv[2], kv[3])};
            if (t > 0) { const f32x4 qv = (aq[t] - sL[t].x * csq) * sL[t].y; qb[t - 1] = (u32x2){pk_bf16(qv[0], qv[1]), pk_bf16(qv[2], qv[3])}; }
            const f32x4 mn = {sR[t][0][0], sR[t][0][2], sR[t][1][0], sR[t][1][2]}, rd = {sR[t][0][1], sR[t][0][3], sR[t][1][1], sR[t][1][3]};
            const f32x4 vv = (av[t] - mn * csv) * rd;
            vb[t] = (u32x2){pk_bf16(vv[0], vv[1]), pk_bf16(vv[2], vv[3])};
        }
        const float SC = 0.25f * 1.44269504088896f;
        u32x2 ob[2];
#pragma unroll
        for (int t = 0; t < 2; ++t) {
            f32x4 sp = mfma16(kb[t], qb[t], t == 0 ? mh : mp), sc = mfma16(kb[t + 1], qb[t], mc);
            float mx = __builtin_fmaxf(__builtin_fmaxf(__builtin_fmaxf(sp[0], sp[1]), __builtin_fmaxf(sp[2], sp[3])), __builtin_fmaxf(__builtin_fmaxf(sc[0], sc[1]), __builtin_fmaxf(sc[2], sc[3])));
            mx = xmax16_32(mx);
            const float mb = -mx * SC;
#pragma unroll
            for (int r = 0; r < 4; ++r) { sp[r] = __builtin_amdgcn_exp2f(sp[r] * SC + mb); sc[r] = __builtin_amdgcn_exp2f(sc[r] * SC + mb); }
            const u32x4 pf = {pk_bf16(sp[0], sp[1]), pk_bf16(sp[2], sp[3]), pk_bf16(sc[0], sc[1]), pk_bf16(sc[2], sc[3])};
            const u32x4 vf = {vb[t].x, vb[t].y, vb[t + 1].x, vb[t + 1].y};
            const f32x4 sm = mfma32(ONES, pf, Z4);
            const f32x4 o = mfma32(vf, pf, Z4) * __builtin_amdgcn_rcpf(sm[0]);
            ob[t] = (u32x2){pk_bf16(o[0], o[1]), pk_bf16(o[2], o[3])};
        }
        auto s0 = __builtin_amdgcn_permlane16_swap(ob[0].x, ob[1].x, false, false);
        auto s1 = __builtin_amdgcn_permlane16_swap(ob[0].y, ob[1].y, false, false);
        const u32x4 o_frag = {s0[0], s1[0], s0[1], s1[1]};
        const rsrc_t rs_ws1 = mk_rsrc(P.ws, WS_BYTES);
        const unsigned off = ((g & 1) * 16384) + ((2 * (hh & 1) + (g >> 1)) * 16 + r16) * 16;
        const unsigned soff = WS_O + ((p * 8 + 4 * hf + 2 * par) * 16 + 2 * hg + (hh >> 1)) * 1024;
        __builtin_amdgcn_raw_buffer_store_b128(o_frag, rs_ws1, off, soff, AUX_SC1);
        asm volatile("" ::: "memory");
    }
    STAMP(3);
    const rsrc_t rs_ws = mk_rsrc(P.ws, WS_BYTES);
    const u64 epoch = gen + 1;
    const unsigned n3 = w & 3, m3 = w >> 2;
    const rsrc_t rs_gu = mk_rsrc(m3 ? P.wu : P.wg, DM * FF * 2);
    unsigned vgu[3];
#pragma unroll
    for (int i = 0; i < 3; ++i) {
        const unsigned s = (n3 * 3 + i) * 64 + lane;
        const unsigned ks2 = s >= 384 ? 1 : 0, s2 = s - ks2 * 384, row = (s2 * 43691u) >> 19, pos = s2 - row * 12;
        int u = (int)pos - (int)(((row >> 3) & 1) + 2 * m3); if (u < 0) u += 12;
        u = min(u, (int)(c < 15 ? 171 - 12 * c : 0));
        vgu[i] = (ks2 * 32 + row) * (FF * 2) + c * 192 + u * 16;
    }
    unsigned mgu[3];
#pragma unroll
    for (int i = 0; i < 3; ++i) { const unsigned pi = n3 * 3 + i; mgu[i] = L3_B + (pi >= 6 ? L3_BSLOT + (pi - 6) * 1024 : pi * 1024) + m3 * 6144; }
#define ISSUE_B1(pr, i_) DMA(mgu[i_], ((pr) & 3) * 2 * L3_BSLOT, vgu[i_], rs_gu, 0u, (pr) * 64 * FF * 2)
#define ISSUE_B(pr) do { _Pragma("unroll") for (int i_ = 0; i_ < 3; ++i_) DMA(mgu[i_], ((pr) & 3) * 2 * L3_BSLOT, vgu[i_], rs_gu, 0u, (pr) * 64 * FF * 2); } while (0)

    const unsigned rt4 = w & 3, kh = w >> 2;
    u32x4 h_keep;
    const unsigned tok_off = (128 * p + 64 * hf + 32 * (rt4 & 1) + (rt4 >> 1) + 2 * r16) * 1024 + (64 * hg + 32 * kh + 16 * (g & 1) + 8 * (g >> 1)) * 2;
    {
        BAR();
        {
            const rsrc_t rs_wo = mk_rsrc(P.wo, DM * DM * 2);
            const unsigned vwo = (8 * w + l8) * 1024 + wsw + hg * 128;
            DMA(wk, L2_WO + 3 * 8192, vwo, rs_wo, 0u, 3 * 65536); DMA(wk, L2_WO + 4 * 8192, vwo, rs_wo, 0u, 4 * 65536); DMA(wk, L2_WO + 5 * 8192, vwo, rs_wo, 0u, 5 * 65536);
        }
        WAIT_VM(3);
        BAR();
        if (tid == 0) __hip_atomic_store(flags + (FL_1 + c) * 32, epoch, __ATOMIC_RELAXED, __HIP_MEMORY_SCOPE_AGENT);
        STAMP(4);
        ISSUE_B(0);
        u32x4 xr;
        asm volatile("buffer_load_dwordx4 %0, %1, %2, 0 offen" : "=v"(xr) : "v"(tok_off), "s"(rs_x) : "memory");
        const unsigned wb0 = L2_WO + kh * 32768 + (8 * g + q4) * 128 + (fsw * 32) + 8 * p4;
        unsigned abase = lane * 16;
        const unsigned asoff = WS_O + ((p * 8 + 4 * hf + rt4) * 16 + 8 * kh) * 1024;
        PIN(abase);
        WAIT_VM(4);
        BAR();
        u32x4 bf[8][4];
#pragma unroll
        for (int k = 0; k < 8; ++k)
#pragma unroll
            for (int ct = 0; ct < 4; ++ct) bf[k][ct] = tr_frag((wb0 ^ (ct * 32)) + k * 4096, 512);
        __builtin_amdgcn_sched_barrier(0);
        STAMP(19);
        if (w == 0) wait_flags(flags + (FL_1 + 8 * hf) * 32, 8, epoch, lane);
        BAR();
        STAMP(5);
        u32x4 af[8];
#pragma unroll
        for (int k = 0; k < 4; ++k) af[k] = __builtin_amdgcn_raw_buffer_load_b128(rs_ws, abase + (k & 3) * 1024, asoff + (k >> 2) * 4096, AUX_SC1);
        __builtin_amdgcn_sched_barrier(0);
        f32x4 acc[4] = {Z4, Z4, Z4, Z4};
#pragma unroll
        for (int k = 0; k < 8; ++k) {
            if (k + 4 < 8) af[k + 4] = __builtin_amdgcn_raw_buffer_load_b128(rs_ws, abase + (k & 3) * 1024, asoff + 4096, AUX_SC1);
            __builtin_amdgcn_sched_barrier(0);
#pragma unroll
            for (int ct = 0; ct < 4; ++ct) acc[ct] = mfma32(bf[k][ct], af[k], acc[ct]);
            __builtin_amdgcn_sched_barrier(0);
        }
        asm volatile("" : "+v"(xr), "+v"(acc[3]));
        STAMP(6);
        const unsigned red = L2_RED + w * 2048 + lane * 16;
        *(LAS f32x4*)red = kh ? acc[0] : acc[2];
        *(LAS f32x4*)(red + 1024) = kh ? acc[1] : acc[3];
        BAR();
        const f32x4 a0 = (kh ? acc[2] : acc[0]) + lds128f(red ^ 8192);
        const f32x4 a1 = (kh ? acc[3] : acc[1]) + lds128f((red ^ 8192) + 1024);
        auto s0 = __builtin_amdgcn_permlane16_swap(pk_bf16(a0[0], a0[1]), pk_bf16(a1[0], a1[1]), false, false);
        auto s1 = __builtin_amdgcn_permlane16_swap(pk_bf16(a0[2], a0[3]), pk_bf16(a1[2], a1[3]), false, false);
        const u32x4 at = {s0[0], s1[0], s0[1], s1[1]};
#pragma unroll
        for (int i = 0; i < 4; ++i) h_keep[i] = pk_bf16(bf_lo(at[i]) + bf_lo(xr[i]), bf_hi(at[i]) + bf_hi(xr[i]));
        const unsigned hoff = ((2 * (g & 1) + (g >> 1)) * 16 + r16) * 16;
        const unsigned hsoff = WS_H + ((p * 8 + 4 * hf + rt4) * 16 + 2 * hg + kh) * 1024;
        __builtin_amdgcn_raw_buffer_store_b128(h_keep, rs_ws, hoff, hsoff, AUX_SC1);
        const f32x4 gs = mfma32(h_keep, h_keep, Z4), sm = mfma32(ONES, h_keep, Z4);
        const u32x2 stv = {f2u(sm[0]), f2u(diag4(gs, lane))};
        const unsigned stoff = (g == q4) ? r16 * 8 : 0xFFFFFFF0u;
        const unsigned stsoff = WS_ST + p * 16384 + c * 1024 + w * 128;
        __builtin_amdgcn_raw_buffer_store_b64(stv, rs_ws, stoff, stsoff, AUX_SC1);
        asm volatile("" ::: "memory");
    }
    STAMP(7);
    const rsrc_t rs_wd = mk_rsrc(P.wd, FF * DM * 2);
    const unsigned vwd = l8 * 1024 + wsw + hg * 128;
    const unsigned swd = ((w >> 2) * 32 + (w & 3) * 8) * 1024;
#define ISSUE_D2(s0) DMA(wk, (s0) * 4096, vwd, rs_wd, swd, ((s0) < 20 ? (s0) + 2 : (s0) + 3) * 32768)
    {
        const bool active = c < 15;
        if (active) { ISSUE_B(1); ISSUE_B(2); ISSUE_B(3); WAIT_VM(9); } else WAIT_VM(0);
        BAR();
        if (tid == 0) __hip_atomic_store(flags + (FL_2 + c) * 32, epoch, __ATOMIC_RELAXED, __HIP_MEMORY_SCOPE_AGENT);
        STAMP(8);
        unsigned aa0 = L3_A + (4 * m3) * 1024 + lane * 16 + m3 * 8192, aa1 = aa0 ^ 8192;
        unsigned ba0[3], ba1[3];
#pragma unroll
        for (int i = 0; i < 3; ++i) {
            const unsigned mat = p4 >> 1;
            unsigned pos = 3 * n3 + i + (g & 1) + 2 * mat; if (pos >= 12) pos -= 12;
            const unsigned a = L3_B + mat * 6144 + ((8 * g + q4) * 12 + pos) * 16 + 8 * (p4 & 1);
            ba0[i] = a + m3 * L3_BSLOT; ba1[i] = a + (1 - m3) * L3_BSLOT;
            PIN(ba0[i]); PIN(ba1[i]);
        }
        unsigned vl16 = lane * 16;
        const unsigned sa3 = WS_H + (p * 8 + w) * 16384, sst = WS_ST + p * 16384 + w * 2048;
        PIN(aa0); PIN(aa1); PIN(vl16);
        if (w == 0) wait_flags(flags + FL_2 * 32, 16, epoch, lane);
        BAR();
        STAMP(9);
        if (tid == 0 && c == 0) __hip_atomic_store(flags + FL_GEN * 32, epoch, __ATOMIC_RELAXED, __HIP_MEMORY_SCOPE_AGENT);
        if (active) {
#define ISSUE_A1(pr, k2) DMA_SC1(wk, L3_A + ((pr) % 3) * 16384 + (k2) * 8192, vl16, rs_ws, sa3, (pr) * 2048 + (k2) * 1024)
#define ISSUE_A(pr) do { ISSUE_A1(pr, 0); ISSUE_A1(pr, 1); } while (0)
            ISSUE_A(0); ISSUE_A(1); ISSUE_A(2);
            DMA_SC1(wk + wk, L3_ST, vl16, rs_ws, sst, 0); DMA_SC1(wk + wk, L3_ST + 1024, vl16, rs_ws, sst, 1024);
            f32x4 acc[4][3], cs[3] = {Z4, Z4, Z4};
#pragma unroll
            for (int t = 0; t < 4; ++t)
#pragma unroll
                for (int i = 0; i < 3; ++i) acc[t][i] = Z4;
            f32x2 st[4];
            u32x4 af[2][4], bf[2][3];
#define LOAD3(s) do { const int pr_ = (s) >> 1, k2_ = (s) & 1; \
                _Pragma("unroll") for (int i = 0; i < 3; ++i) bf[k2_][i] = tr_frag((k2_ ? ba1[i] : ba0[i]) + (pr_ & 3) * 2 * L3_BSLOT, 4 * 12 * 16); \
                _Pragma("unroll") for (int t = 0; t < 4; ++t) af[k2_][t] = lds128((k2_ ? aa1 : aa0) + (pr_ % 3) * 16384 + t * 1024); } while (0)
            WAIT_VM(6);
            BAR();
            STAMP(10);
            LOAD3(0);
#pragma unroll
            for (int s = 0; s < 16; ++s) {
                const int pr = s >> 1;
                if ((s & 1) && s < 15) {
                    if (s == 1) WAIT_VM(4); else if (s == 3) WAIT_VM(7); else if (s < 13) WAIT_VM(8); else WAIT_VM(6);
                    BAR();
                }
                if (s < 15) LOAD3(s + 1);
                f32x2 pp[4];
                const unsigned row = tid >> 2, sa = L3_ST + (row >> 6) * 8192 + (tid & 3) * 2048 + ((row >> 4) & 3) * 128 + (row & 15) * 8;
                if (s == 6) { pp[0] = lds64f(sa); pp[1] = lds64f(sa + 512); pp[2] = lds64f(sa + 1024); pp[3] = lds64f(sa + 1536); }
                if (s == 12) {
#pragma unroll
                    for (int t = 0; t < 4; ++t) st[t] = lds64f(L3_ST + 512 + m3 * 8192 + t * 128 + r16 * 8);
                }
                __builtin_amdgcn_sched_barrier(0);
#pragma unroll
                for (int i = 0; i < 3; ++i) {
#pragma unroll
                    for (int t = 0; t < 4; ++t) acc[t][i] = mfma32(bf[s & 1][i], af[s & 1][t], acc[t][i]);
                    if ((s & 1) == 0) cs[i] = mfma32(bf[s & 1][i], ONES, cs[i]);
                    __builtin_amdgcn_sched_barrier(0);
                    const int so = (s & 1) ? s : s - 1, po = so >> 1, job = (s & 1) ? i : 3 + i;
                    if (so >= 1 && so < 15 && job < 5) {
                        if (po + 3 < 8) {
                            if (job < 2) ISSUE_A1(po + 3, job);
                            else if (po + 4 < 8) ISSUE_B1(po + 4, job - 2);
                            else ISSUE_D2(12 + 2 * (job - 2));
                        } else if (job < 3) ISSUE_D2(12 + 6 * (po - 4) + 2 * job);
                    }
                    __builtin_amdgcn_sched_barrier(0);
                }
                if (s == 6) {
                    const f32x2 sm = (pp[0] + pp[1]) + (pp[2] + pp[3]);
                    float sx = sm.x, sq = sm.y;
                    sx += __builtin_amdgcn_update_dpp(0.f, sx, 0xB1, 0xf, 0xf, false); sq += __builtin_amdgcn_update_dpp(0.f, sq, 0xB1, 0xf, 0xf, false);
                    sx += __builtin_amdgcn_update_dpp(0.f, sx, 0x4E, 0xf, 0xf, false); sq += __builtin_amdgcn_update_dpp(0.f, sq, 0x4E, 0xf, 0xf, false);
                    if ((lane & 3) == 0) *(LAS f32x2*)(sa + 512) = ln_stats(sx, sq);
                }
                __builtin_amdgcn_sched_barrier(0);
            }
            STAMP(11);
            if (r16 == 0) {
#pragma unroll
                for (int i = 0; i < 3; ++i) *(LAS f32x4*)(L3_ST + 1024 + (3 * w + i) * 64 + g * 16) = cs[i];
            }
            BAR();
            ISSUE_D2(0); ISSUE_D2(2); ISSUE_D2(4);
            const unsigned hi = lane >> 5;
            const float r01 = hi ? st[1].y : st[0].y, r23 = hi ? st[3].y : st[2].y;
            const float rr01 = r01 * r01, rr23 = r23 * r23;
            const unsigned tau = 2 * (g & 1) + (g >> 1);
#pragma unroll
            for (int i = 0; i < 3; ++i) {
                const f32x4 cv = cs[i] + lds128f(L3_ST + 1024 + (3 * (w ^ 4) + i) * 64 + g * 16);
                u32x2 hp[2];
#pragma unroll
                for (int pp = 0; pp < 2; ++pp) {
                    const f32x4 e0 = acc[2 * pp][i] - st[2 * pp].x * cv, e1 = acc[2 * pp + 1][i] - st[2 * pp + 1].x * cv;
                    float hv[4];
#pragma unroll
                    for (int r = 0; r < 4; ++r) {
                        auto sw = __builtin_amdgcn_permlane32_swap(f2u(e0[r]), f2u(e1[r]), false, false);
                        const unsigned x0 = sw[0], x1 = sw[1];
                        hv[r] = __builtin_fmaxf(u2f(x0), 0.0f) * (u2f(x1) * (pp ? rr23 : rr01));
                    }
                    hp[pp] = (u32x2){pk_bf16(hv[0], hv[1]), pk_bf16(hv[2], hv[3])};
                }
                auto s0 = __builtin_amdgcn_permlane16_swap(hp[0].x, hp[1].x, false, false);
                auto s1 = __builtin_amdgcn_permlane16_swap(hp[0].y, hp[1].y, false, false);
                const u32x4 hf4 = {s0[0], s1[0], s0[1], s1[1]};
                const unsigned U = 12 * c + 3 * n3 + i;
                const unsigned off = U < 172 ? tau * (NKS4 * 1024) + r16 * 16 : 0xFFFFFFF0u;
                const unsigned soff = WS_HID + ((p * 8 + 4 * m3) * NKS4 + (U >> 2)) * 1024 + (U & 3) * 256;
                __builtin_amdgcn_raw_buffer_store_b128(hf4, rs_ws, off, soff, AUX_SC1);
            }
            asm volatile("" ::: "memory");
        }
    }
    STAMP(12);
    {
        const bool active = c < 15;
        if (!active) {
            ISSUE_D2(12); ISSUE_D2(14); ISSUE_D2(16); ISSUE_D2(18); ISSUE_D2(20); ISSUE_D2(22); ISSUE_D2(24); ISSUE_D2(26); ISSUE_D2(28);
            ISSUE_D2(0); ISSUE_D2(2); ISSUE_D2(4);
        }
        DMA(wk, 30 * 4096, vwd, rs_wd, swd, 0);
        DMA(wk, 32 * 4096, vwd, rs_wd, swd + (kh ? 35 : 22) * 32768, 0);
        ISSUE_D2(6); ISSUE_D2(8); ISSUE_D2(10);
        WAIT_VM(5);
        BAR();
        if (tid == 0 && active) __hip_atomic_store(flags + (FL_3 + c) * 32, epoch, __ATOMIC_RELAXED, __HIP_MEMORY_SCOPE_AGENT);
        STAMP(13);
        ISSUE_D2(34); ISSUE_D2(36); ISSUE_D2(38);
        WAIT_VM(6);
        BAR();
        const unsigned wb0 = (8 * g + q4) * 128 + (fsw * 32) + 8 * p4;
        u32x4 bq[22][4];
#pragma unroll
        for (int ct = 0; ct < 4; ++ct) {
            const u32x4 t0 = tr_frag((wb0 ^ (ct * 32)) + 30 * 4096, 512);
            bq[1][ct] = tr_frag((wb0 ^ (ct * 32)) + (kh ? 32 : 31) * 4096, 512);
            bq[0][ct] = kh ? (u32x4){0u, 0u, 0u, 0u} : t0;
        }
        BAR();
        ISSUE_D2(30);
        DMA(wk, 32 * 4096, vwd, rs_wd, swd, 35 * 32768);
        unsigned wbk = wb0 + kh * (20 * 4096);
        unsigned abase = lane * 16;
        const unsigned asoff = WS_HID + ((p * 8 + 4 * hf + rt4) * NKS4 + 21 * kh) * 1024;
        PIN(wbk); PIN(abase);
        WAIT_VM(0);
        BAR();
#define LOAD4(j) do { _Pragma("unroll") for (int ct = 0; ct < 4; ++ct) bq[j][ct] = tr_frag((wbk ^ (ct * 32)) + ((j) - 2) * 4096, 512); } while (0)
        LOAD4(2); LOAD4(3); LOAD4(4); LOAD4(5);
        __builtin_amdgcn_sched_barrier(0);
        if (w == 0) wait_flags(flags + FL_3 * 32, 15, epoch, lane);
        BAR();
        STAMP(14);
        u32x4 af[22];
#define LOADA4(k) af[k] = __builtin_amdgcn_raw_buffer_load_b128(rs_ws, abase + ((k) & 3) * 1024, asoff + ((k) >> 2) * 4096, AUX_SC1)
#pragma unroll
        for (int k = 0; k < 6; ++k) LOADA4(k);
        __builtin_amdgcn_sched_barrier(0);
        f32x4 acc[4] = {Z4, Z4, Z4, Z4};
#pragma unroll
        for (int j = 0; j < 22; ++j) {
            if (j + 6 < 22) { LOADA4(j + 6); LOAD4(j + 6); }
            __builtin_amdgcn_sched_barrier(0);
#pragma unroll
            for (int ct = 0; ct < 4; ++ct) acc[ct] = mfma32(bq[j][ct], af[j], acc[ct]);
            __builtin_amdgcn_sched_barrier(0);
        }
        STAMP(15);
        BAR();
        const unsigned red = w * 2048 + lane * 16;
        *(LAS f32x4*)red = kh ? acc[0] : acc[2];
        *(LAS f32x4*)(red + 1024) = kh ? acc[1] : acc[3];
        BAR();
        const f32x4 a0 = (kh ? acc[2] : acc[0]) + lds128f(red ^ 8192);
        const f32x4 a1 = (kh ? acc[3] : acc[1]) + lds128f((red ^ 8192) + 1024);
        auto s0 = __builtin_amdgcn_permlane16_swap(pk_bf16(a0[0], a0[1]), pk_bf16(a1[0], a1[1]), false, false);
        auto s1 = __builtin_amdgcn_permlane16_swap(pk_bf16(a0[2], a0[3]), pk_bf16(a1[2], a1[3]), false, false);
        const u32x4 at = {s0[0], s1[0], s0[1], s1[1]};
        u32x4 o;
#pragma unroll
        for (int i = 0; i < 4; ++i) o[i] = pk_bf16(bf_lo(at[i]) + bf_lo(h_keep[i]), bf_hi(at[i]) + bf_hi(h_keep[i]));
        *(u32x4*)((char*)P.out + tok_off) = o;
    }
    STAMP(16);
}

}

extern "C" void kernel_launch(void* const* d_in, const int* in_sizes, int n_in, void* d_out, int out_size, void* d_ws, size_t ws_size, hipStream_t stream) {
    Params P;
    P.x = (const u16*)d_in[0]; P.wqkv = (const u16*)d_in[1]; P.wo = (const u16*)d_in[2];
    P.wg = (const u16*)d_in[4]; P.wu = (const u16*)d_in[5]; P.wd = (const u16*)d_in[6];
    P.out = (u16*)d_out; P.ws = (unsigned char*)d_ws;
    (void)hipFuncSetAttribute((const void*)fused_block, hipFuncAttributeMaxDynamicSharedMemorySize, LDS_BYTES);
    hipLaunchKernelGGL(fused_block, dim3(256), dim3(512), LDS_BYTES, stream, P);
}
```

```cpp
#include <hip/hip_runtime.h>
#include <hip/hip_bf16.h>
#include <stdint.h>
#pragma clang diagnostic ignored "-Wint-to-pointer-cast"

#define LAS __attribute__((address_space(3)))
#define GAS __attribute__((address_space(1)))
typedef unsigned short u16;
typedef unsigned long long u64;
typedef short s16x4 __attribute__((ext_vector_type(4)));
typedef __bf16 bf16x8 __attribute__((ext_vector_type(8)));
typedef __bf16 bf16x2 __attribute__((ext_vector_type(2)));
typedef float f32x4 __attribute__((ext_vector_type(4)));
typedef float f32x2 __attribute__((ext_vector_type(2)));
typedef unsigned u32x4 __attribute__((ext_vector_type(4)));
typedef unsigned u32x2 __attribute__((ext_vector_type(2)));
typedef GAS u64 gu64;
typedef __amdgpu_buffer_rsrc_t rsrc_t;

namespace {

constexpr int SEQ = 2048, DM = 512, FF = 1376;
constexpr int NKS4 = FF / 32;
constexpr unsigned WS_O = 0;
constexpr unsigned WS_H = 2u << 20;
constexpr unsigned WS_HID = 4u << 20;
constexpr unsigned WS_ST = 10u << 20;
constexpr unsigned WS_FLAG = 12u << 20;
constexpr unsigned WS_BYTES = WS_FLAG + 16 * 64 * 256;
constexpr int FL_GEN = 0, FL_1 = 8, FL_2 = 24, FL_3 = 40;
constexpr int AUX_SC1 = 16;
constexpr unsigned kSpinCap = 1u << 16;

constexpr int LDS_BYTES = 163840;
constexpr int L1_X = 0, L1_H = 65536, L1_W = 81920, L1_WSLOT = 24576, L1_SCR = 155648;
constexpr int L2_WO = 81920, L2_RED = 114688;
constexpr int L3_A = 0, L3_B = 49152, L3_BSLOT = 12288, L3_ST = 147456;

__device__ __forceinline__ constexpr int vm_imm(int n) { return (n & 0xF) | ((n >> 4) << 14) | (0x7 << 4) | (0xF << 8); }
#define WAIT_VM(n) asm volatile("s_waitcnt vmcnt(%0)" :: "i"(n) : "memory")
#define BAR() do { asm volatile("s_waitcnt lgkmcnt(0)" ::: "memory"); __builtin_amdgcn_s_barrier(); asm volatile("" ::: "memory"); } while (0)
#define PIN(x) asm volatile("" : "+v"(x))
#define DMA(m0b, ldsoff, voff, rs, sbase, soffimm) do { unsigned t_; asm volatile("s_add_u32 m0, %1, %2\n\ts_add_u32 %0, %5, %6\n\tbuffer_load_dwordx4 %3, %4, %0 offen lds" \
    : "=&s"(t_) : "s"(m0b), "i"(ldsoff), "v"(voff), "s"(rs), "s"(sbase), "i"(soffimm) : "scc", "memory"); } while (0)
#define DMA_SC1(m0b, ldsoff, voff, rs, sbase, soffimm) do { unsigned t_; asm volatile("s_add_u32 m0, %1, %2\n\ts_add_u32 %0, %5, %6\n\tbuffer_load_dwordx4 %3, %4, %0 offen sc1 lds" \
    : "=&s"(t_) : "s"(m0b), "i"(ldsoff), "v"(voff), "s"(rs), "s"(sbase), "i"(soffimm) : "scc", "memory"); } while (0)

__device__ __forceinline__ rsrc_t mk_rsrc(const void* p, unsigned bytes) { return __builtin_amdgcn_make_buffer_rsrc((void*)p, 0, bytes, 0x00020000); }
__device__ __forceinline__ float u2f(unsigned u) { return __builtin_bit_cast(float, u); }
__device__ __forceinline__ unsigned f2u(float f) { return __builtin_bit_cast(unsigned, f); }
__device__ __forceinline__ unsigned pk_bf16(float a, float b) { bf16x2 v; v[0] = (__bf16)a; v[1] = (__bf16)b; return __builtin_bit_cast(unsigned, v); }
__device__ __forceinline__ float bf_lo(unsigned u) { return u2f(u << 16); }
__device__ __forceinline__ float bf_hi(unsigned u) { return u2f(u & 0xffff0000u); }
__device__ __forceinline__ float rbf(float f) { return (float)(__bf16)f; }
__device__ __forceinline__ f32x4 mfma32(u32x4 a, u32x4 b, f32x4 c) {
    return __builtin_amdgcn_mfma_f32_16x16x32_bf16(__builtin_bit_cast(bf16x8, a), __builtin_bit_cast(bf16x8, b), c, 0, 0, 0);
}
__device__ __forceinline__ f32x4 mfma16(u32x2 a, u32x2 b, f32x4 c) {
    return __builtin_amdgcn_mfma_f32_16x16x16bf16_1k(__builtin_bit_cast(s16x4, a), __builtin_bit_cast(s16x4, b), c, 0, 0, 0);
}
__device__ __forceinline__ u32x4 lds128(unsigned a) { return *(LAS u32x4*)a; }
__device__ __forceinline__ f32x4 lds128f(unsigned a) { return *(LAS f32x4*)a; }
__device__ __forceinline__ f32x2 lds64f(unsigned a) { return *(LAS f32x2*)a; }
__device__ __forceinline__ u32x2 tr_read(unsigned a) { return __builtin_bit_cast(u32x2, __builtin_amdgcn_ds_read_tr16_b64_v4i16((LAS s16x4*)a)); }
__device__ __forceinline__ u32x4 tr_frag(unsigned a, int step) { const u32x2 x = tr_read(a), y = tr_read(a + step); return (u32x4){x.x, x.y, y.x, y.y}; }
__device__ __forceinline__ float swap16_lo(float v) {
    const unsigned a = f2u(v); auto r = __builtin_amdgcn_permlane16_swap(a, a, false, false); const unsigned x = r[0]; return u2f(x);
}
__device__ __forceinline__ float xmax16_32(float v) {
    unsigned a = f2u(v);
    auto r = __builtin_amdgcn_permlane16_swap(a, a, false, false);
    const unsigned r0 = r[0], r1 = r[1];
    v = __builtin_fmaxf(u2f(r0), u2f(r1));
    a = f2u(v);
    auto s = __builtin_amdgcn_permlane32_swap(a, a, false, false);
    const unsigned s0 = s[0], s1 = s[1];
    return __builtin_fmaxf(u2f(s0), u2f(s1));
}
__device__ __forceinline__ float bcast_row0(float v) { return __builtin_amdgcn_update_dpp(0.f, v, 0x150, 0xf, 0xf, false); }
__device__ __forceinline__ float diag4(f32x4 d, unsigned lane) { const float a = (lane & 1) ? d[1] : d[0], b = (lane & 1) ? d[3] : d[2]; return (lane & 2) ? b : a; }
__device__ __forceinline__ f32x2 ln_stats(float s, float q) {
    const float mean = s * (1.0f / DM);
    const float var = __builtin_fmaxf(q * (1.0f / DM) - mean * mean, 0.0f);
    const float sd = rbf(__builtin_amdgcn_sqrtf(rbf(rbf(var) + 1e-5f)));
    return (f32x2){rbf(mean), __builtin_amdgcn_rcpf(sd)};
}
__device__ __forceinline__ void wait_flags(gu64* base, unsigned n, u64 epoch, unsigned lane) {
    for (unsigned spins = 0; spins < kSpinCap; ++spins) {
        u64 v = epoch;
        if (lane < n) v = __hip_atomic_load(base + lane * 32, __ATOMIC_RELAXED, __HIP_MEMORY_SCOPE_AGENT);
        if (__all(v == epoch)) break;
    }
    __builtin_amdgcn_fence(__ATOMIC_ACQUIRE, "wavefront");
}

struct Params { const u16* x; const u16* wqkv; const u16* wo; const u16* wg; const u16* wu; const u16* wd; u16* out; unsigned char* ws; };

#define STAMP(i) do {} while (0)

__global__ void __launch_bounds__(512, 2) __attribute__((amdgpu_waves_per_eu(2, 2))) fused_block(Params P
) {
    const unsigned tid = threadIdx.x, lane = tid & 63;
    const unsigned w = __builtin_amdgcn_readfirstlane(tid >> 6);
    const unsigned b = blockIdx.x, xcd = b & 7, jj = b >> 3;
    const unsigned p = 4 * (xcd >> 1) + (jj >> 3);
    const unsigned hf = (jj >> 2) & 1, hg = 4 * (xcd & 1) + (jj & 3);
    const unsigned c = 8 * hf + hg;
    const unsigned r16 = lane & 15, g = lane >> 4, l8 = lane >> 3, l7 = lane & 7, q4 = r16 >> 2, p4 = lane & 3;
    STAMP(0);
    const unsigned wk = w * 1024;
    const rsrc_t rs_x = mk_rsrc(P.x, SEQ * DM * 2);
    const rsrc_t rs_w = mk_rsrc(P.wqkv, DM * 3 * DM * 2);
    gu64* const flags = (gu64*)(P.ws + WS_FLAG) + (size_t)p * 64 * 32;
    const unsigned T0 = 128 * p + 64 * hf;
    STAMP(18);
    u32x4 ONES = {0x3F803F80u, 0x3F803F80u, 0x3F803F80u, 0x3F803F80u};
    PIN(ONES);
    const f32x4 Z4 = {0.f, 0.f, 0.f, 0.f};
    const unsigned fsw = ((q4 >> 1) & 1) | ((g & 1) << 1);
    const unsigned wsw = (l7 ^ (((g & 1) | ((w & 1) << 1)) << 1)) * 16;
    u64 gen = 0;
    const unsigned rt4 = w & 3, kh = w >> 2;
    const unsigned tok_off = (128 * p + 64 * hf + 32 * (rt4 & 1) + (rt4 >> 1) + 2 * r16) * 1024 + (64 * hg + 32 * kh + 16 * (g & 1) + 8 * (g >> 1)) * 2;
    u32x4 xr;

    {
        const unsigned rot = (2 * (jj & 3) + (jj >> 2) + (xcd >> 1)) & 7;
        const unsigned hh = w & 3, par = w >> 2;
        const unsigned vh = l8 * 2048 + ((l7 ^ g) * 16);
        const unsigned vxr = vh ^ ((w & 1) * 64);
        const unsigned sxr = (T0 + 32 * ((w >> 1) & 1) + (w >> 2) + 16 * (w & 1)) * 1024;
        const unsigned vw = (8 * w + l8) * 3072 + wsw + hg * 128;
        {
            const unsigned kap = (w + rot) & 7;
            const unsigned sh = (T0 == 0 ? 0 : T0 - 16) * 1024 + kap * 128;
            const unsigned hb = L1_H + w * 2048;
            if (lane >= 8) { DMA(hb, 0, vh, rs_x, sh, 0); const unsigned vh1 = vh ^ 64; DMA(hb, 1024, vh1, rs_x, sh, 1024); }
            else { *(LAS u32x4*)(hb + lane * 16) = ONES; *(LAS u32x4*)(hb + 1024 + lane * 16) = ONES; }
        }
        unsigned kx[8];
#pragma unroll
        for (int q = 0; q < 8; ++q) kx[q] = (q + rot) & 7;
#define ISSUE_X(q) DMA(wk, L1_X + (q) * 8192, vxr, rs_x, sxr + kx[q] * 128, 0)
#define ISSUE_W1(q, mat) DMA(wk, L1_W + ((q) % 3) * L1_WSLOT + (mat) * 8192, vw, rs_w, kx[q] * 196608, (mat) * 1024)
#define ISSUE_W(q) do { ISSUE_W1(q, 0); ISSUE_W1(q, 1); ISSUE_W1(q, 2); } while (0)
        ISSUE_X(0); ISSUE_W(0);
        __builtin_amdgcn_s_barrier();
        ISSUE_X(1); ISSUE_W(1);
        u64 genv;
        STAMP(17);
        const unsigned xa = (r16 >> 3) * 1024 + (r16 & 7) * 128 + ((g ^ ((r16 >> 1) & 7)) * 16);
        const unsigned xc0 = xa + par * 4096, xc1 = xc0 ^ 64;
        unsigned xh0 = xa + L1_H, xh1 = xh0 ^ 64;
        PIN(xh0); PIN(xh1);
        unsigned wa = L1_W + (8 * g + q4) * 128 + ((hh ^ fsw) * 32) + 8 * p4, wa2 = wa + 2 * L1_WSLOT;
        PIN(wa); PIN(wa2);
        const unsigned sxa = (xa ^ ((hh & 1) * 64));
        unsigned stc = sxa + par * 4096 + (hh >> 1) * 8192, sth = sxa + L1_H + (hh >> 1) * 2048;
        PIN(stc); PIN(sth);
        f32x4 mh, mp, mc;
#pragma unroll
        for (int r = 0; r < 4; ++r) {
            const unsigned kap = 4 * g + r;
            mh[r] = (T0 > 0 && (kap >> 3) == par && (kap & 7) >= r16 + 1) ? 0.f : -1e30f;
            mp[r] = (kap >= r16 + 9) ? 0.f : -1e30f;
            mc[r] = (kap <= r16 && r16 - kap <= 7) ? 0.f : -1e30f;
        }
        PIN(mh); PIN(mp); PIN(mc);

        f32x4 aq[3] = {Z4, Z4, Z4}, ak[3] = {Z4, Z4, Z4}, av[3] = {Z4, Z4, Z4};
        f32x4 gr[3] = {Z4, Z4, Z4}, rs[3] = {Z4, Z4, Z4};
        f32x2 sL[3]; f32x4 sR[3][2];
        const unsigned part = L1_SCR, fin = L1_SCR + 2560 + w * 384;
        u32x4 xf[2][3], wf[2][3], sf[2][3];
        const rsrc_t rs_wo = mk_rsrc(P.wo, DM * DM * 2);
        const unsigned vwo = (8 * w + l8) * 1024 + wsw + hg * 128;
#define LOAD1(s) do { const int kq_ = (s) >> 1, kk_ = (s) & 1; \
            xf[(s) & 1][0] = lds128((kk_ ? xh1 : xh0) + kq_ * 2048); xf[(s) & 1][1] = lds128((kk_ ? xc1 : xc0) + kq_ * 8192); xf[(s) & 1][2] = lds128((kk_ ? xc1 : xc0) + kq_ * 8192 + 2048); \
            _Pragma("unroll") for (int mat = 0; mat < 3; ++mat) wf[(s) & 1][mat] = tr_frag((kq_ % 3 == 2 ? wa2 : wa + (kq_ % 3) * L1_WSLOT) + mat * 8192 + kk_ * 4096, 512); } while (0)
#define JOBS1(it) do { \
            if ((it) == 0) { ISSUE_X(2); ISSUE_W(2); } \
            if ((it) >= 1 && (it) <= 5) ISSUE_W((it) + 2 > 7 ? 7 : (it) + 2); \
            if ((it) == 1) { ISSUE_X(3); ISSUE_X(4); ISSUE_X(5); } \
            if ((it) == 2) { ISSUE_X(6); ISSUE_X(7); } \
            if ((it) == 6) { DMA(wk, L2_WO + 6 * 8192, vwo, rs_wo, 0u, 6 * 65536); DMA(wk, L2_WO + 7 * 8192, vwo, rs_wo, 0u, 7 * 65536); \
                             DMA(wk, 0, vwo, rs_wo, 0u, 3 * 65536); DMA(wk, 8192, vwo, rs_wo, 0u, 4 * 65536); DMA(wk, 16384, vwo, rs_wo, 0u, 5 * 65536); } \
            if ((it) == 7) { DMA(wk, L2_WO, vwo, rs_wo, 0u, 0); DMA(wk, L2_WO + 8192, vwo, rs_wo, 0u, 65536); DMA(wk, L2_WO + 2 * 8192, vwo, rs_wo, 0u, 2 * 65536); } \
            __builtin_amdgcn_sched_barrier(0); } while (0)
        WAIT_VM(4);
        BAR();
        STAMP(1);
        LOAD1(0);
#pragma unroll
        for (int s = 0; s < 16; ++s) {
            if (s & 1) {
                if (s == 1) WAIT_VM(4); else if (s == 3) WAIT_VM(6); else if (s == 5) WAIT_VM(8); else if (s == 7) WAIT_VM(6); else if (s < 13) WAIT_VM(3); else if (s == 13) WAIT_VM(6);
                if (s < 15) BAR();
                if (s == 9) { asm volatile("" : "+v"(genv)); gen = ((u64)(unsigned)__builtin_amdgcn_readfirstlane((unsigned)(genv >> 32)) << 32) | (unsigned)__builtin_amdgcn_readfirstlane((unsigned)genv); }
            }
            if ((s & 1) || s == 0) { if (w < 4) JOBS1((s + 1) / 2); }
            if (s < 15) LOAD1(s + 1);
            if (s >= 6 && s <= 9) { const int j = s - 6; sf[j & 1][0] = lds128(sth + j * 4096); sf[j & 1][1] = lds128(stc + j * 16384); sf[j & 1][2] = lds128(stc + j * 16384 + 2048); }
            f32x2 pp[4];
            if (s == 11) {
                const unsigned pa = part + ((g == 0 ? 0 : (g == 3 ? 0 : 2 * par + g)) * 64 + r16) * 8;
                pp[0] = lds64f(pa); pp[1] = lds64f(pa + 128); pp[2] = lds64f(pa + 256); pp[3] = lds64f(pa + 384);
            }
            if (s == 13) {
#pragma unroll
                for (int t = 0; t < 3; ++t) { sL[t] = lds64f(fin + (t * 16 + r16) * 8); sR[t][0] = lds128f(fin + (t * 16 + 4 * g) * 8); sR[t][1] = lds128f(fin + (t * 16 + 4 * g) * 8 + 16); }
            }
#pragma unroll
            for (int t = 0; t < 3; ++t) {
                aq[t] = mfma32(wf[s & 1][0], xf[s & 1][t], aq[t]); ak[t] = mfma32(wf[s & 1][1], xf[s & 1][t], ak[t]); av[t] = mfma32(xf[s & 1][t], wf[s & 1][2], av[t]);
            }
            if (s >= 7 && s <= 10) {
#pragma unroll
                for (int t = 0; t < 3; ++t) { gr[t] = mfma32(sf[(s - 7) & 1][t], sf[(s - 7) & 1][t], gr[t]); rs[t] = mfma32(ONES, sf[(s - 7) & 1][t], rs[t]); }
            }
#pragma unroll
            for (int i = 0; i < 9; ++i) { __builtin_amdgcn_sched_group_barrier(0x8, 1, 0); __builtin_amdgcn_sched_group_barrier(0x100, 1, 0); }
#pragma unroll
            for (int i = 0; i < 6; ++i) { __builtin_amdgcn_sched_group_barrier(0x8, 1, 0); __builtin_amdgcn_sched_group_barrier(0x100, 2, 0); }
            __builtin_amdgcn_sched_barrier(0);
            if (s == 10 && g == q4) {
                *(LAS f32x2*)(part + (hh * 16 + r16) * 8) = (f32x2){rs[0][0], diag4(gr[0], lane)};
                *(LAS f32x2*)(part + (((1 + 2 * par) * 4 + hh) * 16 + r16) * 8) = (f32x2){rs[1][0], diag4(gr[1], lane)};
                *(LAS f32x2*)(part + (((2 + 2 * par) * 4 + hh) * 16 + r16) * 8) = (f32x2){rs[2][0], diag4(gr[2], lane)};
            }
            if (s == 11) {
                const f32x2 sm = (pp[0] + pp[1]) + (pp[2] + pp[3]);
                const f32x2 res = ln_stats(sm.x, sm.y);
                if (g < 3) *(LAS f32x2*)(fin + (g * 16 + r16) * 8) = res;
            }
            __builtin_amdgcn_sched_barrier(0);
            if (!(s & 1)) { if (w >= 4) JOBS1(s / 2); }
            if (s == 12) asm volatile("buffer_load_dwordx4 %0, %1, %2, 0 offen" : "=v"(xr) : "v"(tok_off), "s"(rs_x) : "memory");
            if (s == 4) { const gu64* gp = flags + FL_GEN * 32; asm volatile("global_load_dwordx2 %0, %1, off sc1" : "=v"(genv) : "v"(gp) : "memory"); }
        }
        STAMP(2);
        f32x4 csq, csk;
#pragma unroll
        for (int r = 0; r < 4; ++r) { csq[r] = bcast_row0(aq[0][r]); csk[r] = bcast_row0(ak[0][r]); }
        const float csv = swap16_lo(av[0][0]);
        u32x2 qb[2], kb[3], vb[3];
#pragma unroll
        for (int t = 0; t < 3; ++t) {
            const f32x4 kv = (ak[t] - sL[t].x * csk) * sL[t].y;
            kb[t] = (u32x2){pk_bf16(kv[0], kv[1]), pk_bf16(kv[2], kv[3])};
            if (t > 0) { const f32x4 qv = (aq[t] - sL[t].x * csq) * sL[t].y; qb[t - 1] = (u32x2){pk_bf16(qv[0], qv[1]), pk_bf16(qv[2], qv[3])}; }
            const f32x4 mn = {sR[t][0][0], sR[t][0][2], sR[t][1][0], sR[t][1][2]}, rd = {sR[t][0][1], sR[t][0][3], sR[t][1][1], sR[t][1][3]};
            const f32x4 vv = (av[t] - mn * csv) * rd;
            vb[t] = (u32x2){pk_bf16(vv[0], vv[1]), pk_bf16(vv[2], vv[3])};
        }
        const float SC = 0.25f * 1.44269504088896f;
        u32x2 ob[2];
#pragma unroll
        for (int t = 0; t < 2; ++t) {
            f32x4 sp = mfma16(kb[t], qb[t], t == 0 ? mh : mp), sc = mfma16(kb[t + 1], qb[t], mc);
            float mx = __builtin_fmaxf(__builtin_fmaxf(__builtin_fmaxf(sp[0], sp[1]), __builtin_fmaxf(sp[2], sp[3])), __builtin_fmaxf(__builtin_fmaxf(sc[0], sc[1]), __builtin_fmaxf(sc[2], sc[3])));
            mx = xmax16_32(mx);
            const float mb = -mx * SC;
#pragma unroll
            for (int r = 0; r < 4; ++r) { sp[r] = __builtin_amdgcn_exp2f(sp[r] * SC + mb); sc[r] = __builtin_amdgcn_exp2f(sc[r] * SC + mb); }
            const u32x4 pf = {pk_bf16(sp[0], sp[1]), pk_bf16(sp[2], sp[3]), pk_bf16(sc[0], sc[1]), pk_bf16(sc[2], sc[3])};
            const u32x4 vf = {vb[t].x, vb[t].y, vb[t + 1].x, vb[t + 1].y};
            const f32x4 sm = mfma32(ONES, pf, Z4);
            const f32x4 o = mfma32(vf, pf, Z4) * __builtin_amdgcn_rcpf(sm[0]);
            ob[t] = (u32x2){pk_bf16(o[0], o[1]), pk_bf16(o[2], o[3])};
        }
        auto s0 = __builtin_amdgcn_permlane16_swap(ob[0].x, ob[1].x, false, false);
        auto s1 = __builtin_amdgcn_permlane16_swap(ob[0].y, ob[1].y, false, false);
        const u32x4 o_frag = {s0[0], s1[0], s0[1], s1[1]};
        const rsrc_t rs_ws1 = mk_rsrc(P.ws, WS_BYTES);
        const unsigned off = ((g & 1) * 16384) + ((2 * (hh & 1) + (g >> 1)) * 16 + r16) * 16;
        const unsigned soff = WS_O + ((p * 8 + 4 * hf + 2 * par) * 16 + 2 * hg + (hh >> 1)) * 1024;
        __builtin_amdgcn_raw_buffer_store_b128(o_frag, rs_ws1, off + soff, 0, AUX_SC1);
        asm volatile("" ::: "memory");
    }
    STAMP(3);
    const rsrc_t rs_ws = mk_rsrc(P.ws, WS_BYTES);
    const u64 epoch = gen + 1;
    const unsigned n3 = w & 3, m3 = w >> 2;
    const rsrc_t rs_gu = mk_rsrc(m3 ? P.wu : P.wg, DM * FF * 2);
    unsigned vgu[3];
#pragma unroll
    for (int i = 0; i < 3; ++i) {
        const unsigned s = (n3 * 3 + i) * 64 + lane;
        const unsigned ks2 = s >= 384 ? 1 : 0, s2 = s - ks2 * 384, row = (s2 * 43691u) >> 19, pos = s2 - row * 12;
        int u = (int)pos - (int)(((row >> 3) & 1) + 2 * m3); if (u < 0) u += 12;
        u = min(u, (int)(c < 15 ? 171 - 12 * c : 0));
        vgu[i] = (ks2 * 32 + row) * (FF * 2) + c * 192 + u * 16;
    }
    unsigned mgu[3];
#pragma unroll
    for (int i = 0; i < 3; ++i) { const unsigned pi = n3 * 3 + i; mgu[i] = L3_B + (pi >= 6 ? L3_BSLOT + (pi - 6) * 1024 : pi * 1024) + m3 * 6144; }
#define ISSUE_B1(pr, i_) DMA(mgu[i_], ((pr) & 3) * 2 * L3_BSLOT, vgu[i_], rs_gu, 0u, (pr) * 64 * FF * 2)
#define ISSUE_B(pr) do { _Pragma("unroll") for (int i_ = 0; i_ < 3; ++i_) DMA(mgu[i_], ((pr) & 3) * 2 * L3_BSLOT, vgu[i_], rs_gu, 0u, (pr) * 64 * FF * 2); } while (0)

    u32x4 h_keep;
    {
        WAIT_VM(0);
        BAR();
        if (tid == 0) __hip_atomic_store(flags + (FL_1 + c) * 32, epoch, __ATOMIC_RELAXED, __HIP_MEMORY_SCOPE_AGENT);
        STAMP(4);
        asm volatile("" : "+v"(xr));
        const unsigned wl = (8 * g + q4) * 128 + (fsw * 32) + 8 * p4;
        const unsigned wb0 = wl + (kh ? 8192 : L2_WO), wb1 = wl + (kh ? L2_WO + 49152 : L2_WO + 16384), wb2 = wl + (kh ? L2_WO + 57344 : 0);
        unsigned abase = lane * 16;
        const unsigned asoff = WS_O + ((p * 8 + 4 * hf + rt4) * 16 + 8 * kh) * 1024;
        PIN(abase);
        u32x4 bf[8][4];
#pragma unroll
        for (int k = 0; k < 8; ++k)
#pragma unroll
            for (int ct = 0; ct < 4; ++ct) bf[k][ct] = tr_frag(((k < 4 ? wb0 : (k < 6 ? wb1 : wb2)) ^ (ct * 32)) + (k < 4 ? k : (k < 6 ? k - 4 : k - 6)) * 4096, 512);
        __builtin_amdgcn_sched_barrier(0);
        STAMP(19);
        if (w == 0) wait_flags(flags + (FL_1 + 8 * hf) * 32, 8, epoch, lane);
        BAR();
        STAMP(5);
        u32x4 af[8];
#pragma unroll
        for (int k = 0; k < 4; ++k) af[k] = __builtin_amdgcn_raw_buffer_load_b128(rs_ws, abase + (k & 3) * 1024, asoff + (k >> 2) * 4096, AUX_SC1);
        __builtin_amdgcn_sched_barrier(0);
        f32x4 acc[4] = {Z4, Z4, Z4, Z4};
#pragma unroll
        for (int k = 0; k < 8; ++k) {
            if (k + 4 < 8) af[k + 4] = __builtin_amdgcn_raw_buffer_load_b128(rs_ws, abase + (k & 3) * 1024, asoff + 4096, AUX_SC1);
            __builtin_amdgcn_sched_barrier(0);
#pragma unroll
            for (int ct = 0; ct < 4; ++ct) acc[ct] = mfma32(bf[k][ct], af[k], acc[ct]);
            __builtin_amdgcn_sched_barrier(0);
        }
        STAMP(6);
        const unsigned red = L2_RED + w * 2048 + lane * 16;
        *(LAS f32x4*)red = kh ? acc[0] : acc[2];
        *(LAS f32x4*)(red + 1024) = kh ? acc[1] : acc[3];
        BAR();
        const f32x4 a0 = (kh ? acc[2] : acc[0]) + lds128f(red ^ 8192);
        const f32x4 a1 = (kh ? acc[3] : acc[1]) + lds128f((red ^ 8192) + 1024);
        auto s0 = __builtin_amdgcn_permlane16_swap(pk_bf16(a0[0], a0[1]), pk_bf16(a1[0], a1[1]), false, false);
        auto s1 = __builtin_amdgcn_permlane16_swap(pk_bf16(a0[2], a0[3]), pk_bf16(a1[2], a1[3]), false, false);
        const u32x4 at = {s0[0], s1[0], s0[1], s1[1]};
#pragma unroll
        for (int i = 0; i < 4; ++i) h_keep[i] = pk_bf16(bf_lo(at[i]) + bf_lo(xr[i]), bf_hi(at[i]) + bf_hi(xr[i]));
        const unsigned hoff = ((2 * (g & 1) + (g >> 1)) * 16 + r16) * 16;
        const unsigned hsoff = WS_H + ((p * 8 + 4 * hf + rt4) * 16 + 2 * hg + kh) * 1024;
        __builtin_amdgcn_raw_buffer_store_b128(h_keep, rs_ws, hoff + hsoff, 0, AUX_SC1);
        const f32x4 gs = mfma32(h_keep, h_keep, Z4), sm = mfma32(ONES, h_keep, Z4);
        const u32x2 stv = {f2u(sm[0]), f2u(diag4(gs, lane))};
        const unsigned stoff = (g == q4) ? WS_ST + p * 16384 + c * 1024 + w * 128 + r16 * 8 : 0xFFFFFFF0u;
        __builtin_amdgcn_raw_buffer_store_b64(stv, rs_ws, stoff, 0, AUX_SC1);
        asm volatile("" ::: "memory");
    }
    STAMP(7);
    const rsrc_t rs_wd = mk_rsrc(P.wd, FF * DM * 2);
    const unsigned vwd = l8 * 1024 + wsw + hg * 128;
    const unsigned swd = ((w >> 2) * 32 + (w & 3) * 8) * 1024;
#define ISSUE_D2(s0) DMA(wk, (s0) * 4096, vwd, rs_wd, swd, ((s0) < 20 ? (s0) + 2 : (s0) + 3) * 32768)
    {
        const bool active = c < 15;
        WAIT_VM(0);
        BAR();
        if (tid == 0) __hip_atomic_store(flags + (FL_2 + c) * 32, epoch, __ATOMIC_RELAXED, __HIP_MEMORY_SCOPE_AGENT);
        if (active && w != 0) { ISSUE_B(0); ISSUE_B(1); ISSUE_B(2); ISSUE_B(3); }
        STAMP(8);
        unsigned aa0 = L3_A + (4 * m3) * 1024 + lane * 16 + m3 * 8192, aa1 = aa0 ^ 8192;
        unsigned ba0[3], ba1[3];
#pragma unroll
        for (int i = 0; i < 3; ++i) {
            const unsigned mat = p4 >> 1;
            unsigned pos = 3 * n3 + i + (g & 1) + 2 * mat; if (pos >= 12) pos -= 12;
            const unsigned a = L3_B + mat * 6144 + ((8 * g + q4) * 12 + pos) * 16 + 8 * (p4 & 1);
            ba0[i] = a + m3 * L3_BSLOT; ba1[i] = a + (1 - m3) * L3_BSLOT;
            PIN(ba0[i]); PIN(ba1[i]);
        }
        unsigned vl16 = lane * 16;
        const unsigned sa3 = WS_H + (p * 8 + w) * 16384, sst = WS_ST + p * 16384 + w * 2048;
        PIN(aa0); PIN(aa1); PIN(vl16);
        if (w == 0) wait_flags(flags + FL_2 * 32, 16, epoch, lane);
        BAR();
        STAMP(9);
        if (active && w == 0) { ISSUE_B(0); ISSUE_B(1); ISSUE_B(2); ISSUE_B(3); }
        if (tid == 0 && c == 0) __hip_atomic_store(flags + FL_GEN * 32, epoch, __ATOMIC_RELAXED, __HIP_MEMORY_SCOPE_AGENT);
        if (active) {
#define ISSUE_A1(pr, k2) DMA_SC1(wk, L3_A + ((pr) % 3) * 16384 + (k2) * 8192, vl16, rs_ws, sa3, (pr) * 2048 + (k2) * 1024)
#define ISSUE_A(pr) do { ISSUE_A1(pr, 0); ISSUE_A1(pr, 1); } while (0)
            ISSUE_A(0); ISSUE_A(1); ISSUE_A(2);
            DMA_SC1(wk + wk, L3_ST, vl16, rs_ws, sst, 0); DMA_SC1(wk + wk, L3_ST + 1024, vl16, rs_ws, sst, 1024);
            f32x4 acc[4][3], cs[3] = {Z4, Z4, Z4};
#pragma unroll
            for (int t = 0; t < 4; ++t)
#pragma unroll
                for (int i = 0; i < 3; ++i) acc[t][i] = Z4;
            f32x2 st[4];
            u32x4 af[2][4], bf[2][3];
            unsigned sa = L3_ST + (tid >> 8) * 8192 + (tid & 3) * 2048 + ((tid >> 2) & 63) * 8, sf = L3_ST + 512 + m3 * 8192 + r16 * 8;
            PIN(sa); PIN(sf);
#define LOAD3(s) do { const int pr_ = (s) >> 1, k2_ = (s) & 1; \
                bf[k2_][0] = tr_frag((k2_ ? ba1[0] : ba0[0]) + (pr_ & 3) * 2 * L3_BSLOT, 4 * 12 * 16); \
                _Pragma("unroll") for (int t = 0; t < 4; ++t) af[k2_][t] = lds128((k2_ ? aa1 : aa0) + (pr_ % 3) * 16384 + t * 1024); \
                _Pragma("unroll") for (int i = 1; i < 3; ++i) bf[k2_][i] = tr_frag((k2_ ? ba1[i] : ba0[i]) + (pr_ & 3) * 2 * L3_BSLOT, 4 * 12 * 16); } while (0)
#define JOBS3(po) do { \
                if ((po) + 3 < 8) ISSUE_A((po) + 3 < 8 ? (po) + 3 : 7); \
                if ((po) + 4 < 8) ISSUE_B((po) + 4 < 8 ? (po) + 4 : 7); \
                if ((po) >= 4) { ISSUE_D2(12 + 6 * ((po) >= 4 ? (po) - 4 : 0)); ISSUE_D2(14 + 6 * ((po) >= 4 ? (po) - 4 : 0)); ISSUE_D2(16 + 6 * ((po) >= 4 ? (po) - 4 : 0)); } \
                __builtin_amdgcn_sched_barrier(0); } while (0)
            WAIT_VM(6);
            BAR();
            STAMP(10);
            LOAD3(0);
#pragma unroll
            for (int s = 0; s < 16; ++s) {
                const int pr = s >> 1;
                if ((s & 1) && s < 15) {
                    if (s == 1) WAIT_VM(4); else if (s == 3) WAIT_VM(7); else if (s < 13) WAIT_VM(8); else WAIT_VM(6);
                    BAR();
                }
                if ((s & 1) && s < 15) { if (w < 4) JOBS3(s >> 1); }
                if (s < 15) LOAD3(s + 1);
                f32x2 pp[4];
                if (s == 6) { pp[0] = lds64f(sa); pp[1] = lds64f(sa + 512); pp[2] = lds64f(sa + 1024); pp[3] = lds64f(sa + 1536); }
                if (s == 12) {
#pragma unroll
                    for (int t = 0; t < 4; ++t) st[t] = lds64f(sf + t * 128);
                }
#pragma unroll
                for (int i = 0; i < 3; ++i) {
#pragma unroll
                    for (int t = 0; t < 4; ++t) acc[t][i] = mfma32(bf[s & 1][i], af[s & 1][t], acc[t][i]);
                    if ((s & 1) == 0) cs[i] = mfma32(bf[s & 1][i], ONES, cs[i]);
                }
#pragma unroll
                for (int i = 0; i < 10; ++i) { __builtin_amdgcn_sched_group_barrier(0x8, 1, 0); __builtin_amdgcn_sched_group_barrier(0x100, 1, 0); }
#pragma unroll
                for (int i = 0; i < 5; ++i) { __builtin_amdgcn_sched_group_barrier(0x8, 1, 0); __builtin_amdgcn_sched_group_barrier(0x100, 1, 0); }
                __builtin_amdgcn_sched_barrier(0);
                if (s == 6) {
                    const f32x2 sm = (pp[0] + pp[1]) + (pp[2] + pp[3]);
                    float sx = sm.x, sq = sm.y;
                    sx += __builtin_amdgcn_update_dpp(0.f, sx, 0xB1, 0xf, 0xf, false); sq += __builtin_amdgcn_update_dpp(0.f, sq, 0xB1, 0xf, 0xf, false);
                    sx += __builtin_amdgcn_update_dpp(0.f, sx, 0x4E, 0xf, 0xf, false); sq += __builtin_amdgcn_update_dpp(0.f, sq, 0x4E, 0xf, 0xf, false);
                    if ((lane & 3) == 0) *(LAS f32x2*)(sa + 512) = ln_stats(sx, sq);
                }
                __builtin_amdgcn_sched_barrier(0);
                if (!(s & 1) && s >= 2) { if (w >= 4) JOBS3((s >> 1) - 1); }
            }
            STAMP(11);
            if (r16 == 0) {
#pragma unroll
                for (int i = 0; i < 3; ++i) *(LAS f32x4*)(L3_ST + 1024 + (3 * w + i) * 64 + g * 16) = cs[i];
            }
            BAR();
            DMA(wk, 30 * 4096, vwd, rs_wd, swd, 0);
            DMA(wk, 32 * 4096, vwd, rs_wd, swd + (kh ? 35 : 22) * 32768, 0);
            ISSUE_D2(0); ISSUE_D2(2); ISSUE_D2(4);
            const unsigned hi = lane >> 5;
            const float r01 = hi ? st[1].y : st[0].y, r23 = hi ? st[3].y : st[2].y;
            const float rr01 = r01 * r01, rr23 = r23 * r23;
            const unsigned tau = 2 * (g & 1) + (g >> 1);
#pragma unroll
            for (int i = 0; i < 3; ++i) {
                const f32x4 cv = cs[i] + lds128f(L3_ST + 1024 + (3 * (w ^ 4) + i) * 64 + g * 16);
                u32x2 hp[2];
#pragma unroll
                for (int pp = 0; pp < 2; ++pp) {
                    const f32x4 e0 = acc[2 * pp][i] - st[2 * pp].x * cv, e1 = acc[2 * pp + 1][i] - st[2 * pp + 1].x * cv;
                    float hv[4];
#pragma unroll
                    for (int r = 0; r < 4; ++r) {
                        auto sw = __builtin_amdgcn_permlane32_swap(f2u(e0[r]), f2u(e1[r]), false, false);
                        const unsigned x0 = sw[0], x1 = sw[1];
                        hv[r] = __builtin_fmaxf(u2f(x0), 0.0f) * (u2f(x1) * (pp ? rr23 : rr01));
                    }
                    hp[pp] = (u32x2){pk_bf16(hv[0], hv[1]), pk_bf16(hv[2], hv[3])};
                }
                auto s0 = __builtin_amdgcn_permlane16_swap(hp[0].x, hp[1].x, false, false);
                auto s1 = __builtin_amdgcn_permlane16_swap(hp[0].y, hp[1].y, false, false);
                const u32x4 hf4 = {s0[0], s1[0], s0[1], s1[1]};
                const unsigned U = 12 * c + 3 * n3 + i;
                const unsigned soff = WS_HID + ((p * 8 + 4 * m3) * NKS4 + (U >> 2)) * 1024 + (U & 3) * 256;
                const unsigned off = U < 172 ? tau * (NKS4 * 1024) + r16 * 16 + soff : 0xFFFFFFF0u;
                __builtin_amdgcn_raw_buffer_store_b128(hf4, rs_ws, off, 0, AUX_SC1);
            }
            asm volatile("" ::: "memory");
        }
    }
    STAMP(12);
    {
        const bool active = c < 15;
        if (!active) {
            ISSUE_D2(12); ISSUE_D2(14); ISSUE_D2(16); ISSUE_D2(18); ISSUE_D2(20); ISSUE_D2(22); ISSUE_D2(24); ISSUE_D2(26); ISSUE_D2(28);
            DMA(wk, 30 * 4096, vwd, rs_wd, swd, 0);
            DMA(wk, 32 * 4096, vwd, rs_wd, swd + (kh ? 35 : 22) * 32768, 0);
            ISSUE_D2(0); ISSUE_D2(2); ISSUE_D2(4);
        }
        WAIT_VM(0);
        BAR();
        if (tid == 0 && active) __hip_atomic_store(flags + (FL_3 + c) * 32, epoch, __ATOMIC_RELAXED, __HIP_MEMORY_SCOPE_AGENT);
        STAMP(13);
#define REST_D() do { ISSUE_D2(6); ISSUE_D2(8); ISSUE_D2(10); ISSUE_D2(34); ISSUE_D2(36); ISSUE_D2(38); } while (0)
        if (w != 0) REST_D();
        const unsigned wb0 = (8 * g + q4) * 128 + (fsw * 32) + 8 * p4;
        u32x4 bq[22][4];
#pragma unroll
        for (int ct = 0; ct < 4; ++ct) {
            const u32x4 t0 = tr_frag((wb0 ^ (ct * 32)) + 30 * 4096, 512);
            bq[1][ct] = tr_frag((wb0 ^ (ct * 32)) + (kh ? 32 : 31) * 4096, 512);
            bq[0][ct] = kh ? (u32x4){0u, 0u, 0u, 0u} : t0;
        }
        BAR();
#define LAST_D() do { ISSUE_D2(30); DMA(wk, 32 * 4096, vwd, rs_wd, swd, 35 * 32768); } while (0)
        if (w != 0) LAST_D();
        unsigned wbk = wb0 + kh * (20 * 4096);
        unsigned abase = lane * 16;
        const unsigned asoff = WS_HID + ((p * 8 + 4 * hf + rt4) * NKS4 + 21 * kh) * 1024;
        PIN(wbk); PIN(abase);
        if (w == 0) wait_flags(flags + FL_3 * 32, 15, epoch, lane);
        BAR();
        STAMP(14);
        u32x4 af[22];
#define LOADA4(k) af[k] = __builtin_amdgcn_raw_buffer_load_b128(rs_ws, abase + ((k) & 3) * 1024, asoff + ((k) >> 2) * 4096, AUX_SC1)
        if (w == 0) { REST_D(); LAST_D(); }
#pragma unroll
        for (int k = 0; k < 6; ++k) LOADA4(k);
        WAIT_VM(6);
        BAR();
#define LOAD4(j) do { _Pragma("unroll") for (int ct = 0; ct < 4; ++ct) bq[j][ct] = tr_frag((wbk ^ (ct * 32)) + ((j) - 2) * 4096, 512); } while (0)
        LOAD4(2); LOAD4(3); LOAD4(4); LOAD4(5);
        __builtin_amdgcn_sched_barrier(0);
        f32x4 acc[4] = {Z4, Z4, Z4, Z4};
#pragma unroll
        for (int j = 0; j < 22; ++j) {
            if (j + 6 < 22) { LOADA4(j + 6); LOAD4(j + 6); }
#pragma unroll
            for (int ct = 0; ct < 4; ++ct) acc[ct] = mfma32(bq[j][ct], af[j], acc[ct]);
#pragma unroll
            for (int i = 0; i < 4; ++i) { __builtin_amdgcn_sched_group_barrier(0x8, 1, 0); __builtin_amdgcn_sched_group_barrier(0x100, 2, 0); }
            __builtin_amdgcn_sched_barrier(0);
        }
        STAMP(15);
        BAR();
        const unsigned red = w * 2048 + lane * 16;
        *(LAS f32x4*)red = kh ? acc[0] : acc[2];
        *(LAS f32x4*)(red + 1024) = kh ? acc[1] : acc[3];
        BAR();
        const f32x4 a0 = (kh ? acc[2] : acc[0]) + lds128f(red ^ 8192);
        const f32x4 a1 = (kh ? acc[3] : acc[1]) + lds128f((red ^ 8192) + 1024);
        auto s0 = __builtin_amdgcn_permlane16_swap(pk_bf16(a0[0], a0[1]), pk_bf16(a1[0], a1[1]), false, false);
        auto s1 = __builtin_amdgcn_permlane16_swap(pk_bf16(a0[2], a0[3]), pk_bf16(a1[2], a1[3]), false, false);
        const u32x4 at = {s0[0], s1[0], s0[1], s1[1]};
        u32x4 o;
#pragma unroll
        for (int i = 0; i < 4; ++i) o[i] = pk_bf16(bf_lo(at[i]) + bf_lo(h_keep[i]), bf_hi(at[i]) + bf_hi(h_keep[i]));
        *(u32x4*)((char*)P.out + tok_off) = o;
    }
    STAMP(16);
}

}

extern "C" void kernel_launch(void* const* d_in, const int* in_sizes, int n_in, void* d_out, int out_size, void* d_ws, size_t ws_size, hipStream_t stream) {
    Params P;
    P.x = (const u16*)d_in[0]; P.wqkv = (const u16*)d_in[1]; P.wo = (const u16*)d_in[2];
    P.wg = (const u16*)d_in[4]; P.wu = (const u16*)d_in[5]; P.wd = (const u16*)d_in[6];
    P.out = (u16*)d_out; P.ws = (unsigned char*)d_ws;
    (void)hipFuncSetAttribute((const void*)fused_block, hipFuncAttributeMaxDynamicSharedMemorySize, LDS_BYTES);
    hipLaunchKernelGGL(fused_block, dim3(256), dim3(512), LDS_BYTES, stream, P);
}
```

```cpp
#include <hip/hip_runtime.h>
#include <hip/hip_bf16.h>
#include <stdint.h>
#pragma clang diagnostic ignored "-Wint-to-pointer-cast"

#define LAS __attribute__((address_space(3)))
#define GAS __attribute__((address_space(1)))
typedef unsigned short u16;
typedef unsigned long long u64;
typedef short s16x4 __attribute__((ext_vector_type(4)));
typedef __bf16 bf16x8 __attribute__((ext_vector_type(8)));
typedef __bf16 bf16x2 __attribute__((ext_vector_type(2)));
typedef float f32x4 __attribute__((ext_vector_type(4)));
typedef float f32x2 __attribute__((ext_vector_type(2)));
typedef unsigned u32x4 __attribute__((ext_vector_type(4)));
typedef unsigned u32x2 __attribute__((ext_vector_type(2)));
typedef GAS u64 gu64;
typedef __amdgpu_buffer_rsrc_t rsrc_t;

namespace {

constexpr int SEQ = 2048, DM = 512, FF = 1376;
constexpr int NKS4 = FF / 32;
constexpr unsigned WS_O = 0;
constexpr unsigned WS_H = 2u << 20;
constexpr unsigned WS_HID = 4u << 20;
constexpr unsigned WS_ST = 10u << 20;
constexpr unsigned WS_FLAG = 12u << 20;
constexpr unsigned WS_BYTES = WS_FLAG + 16 * 64 * 256;
constexpr int FL_GEN = 0, FL_1 = 8, FL_2 = 24, FL_3 = 40;
constexpr int AUX_SC1 = 16;
constexpr unsigned kSpinCap = 1u << 16;

constexpr int LDS_BYTES = 163840;
constexpr int L1_X = 0, L1_H = 65536, L1_W = 81920, L1_WSLOT = 24576, L1_SCR = 155648;
constexpr int L2_WO = 81920, L2_RED = 114688;
constexpr int L3_A = 0, L3_B = 49152, L3_BSLOT = 12288, L3_ST = 147456;

__device__ __forceinline__ constexpr int vm_imm(int n) { return (n & 0xF) | ((n >> 4) << 14) | (0x7 << 4) | (0xF << 8); }
#define WAIT_VM(n) asm volatile("s_waitcnt vmcnt(%0)" :: "i"(n) : "memory")
#define BAR() do { asm volatile("s_waitcnt lgkmcnt(0)" ::: "memory"); __builtin_amdgcn_s_barrier(); asm volatile("" ::: "memory"); } while (0)
#define PIN(x) asm volatile("" : "+v"(x))
#define DMA(m0b, ldsoff, voff, rs, sbase, soffimm) do { unsigned t_; asm volatile("s_add_u32 m0, %1, %2\n\ts_add_u32 %0, %5, %6\n\tbuffer_load_dwordx4 %3, %4, %0 offen lds" \
    : "=&s"(t_) : "s"(m0b), "i"(ldsoff), "v"(voff), "s"(rs), "s"(sbase), "i"(soffimm) : "scc", "memory"); } while (0)
#define DMA_SC1(m0b, ldsoff, voff, rs, sbase, soffimm) do { unsigned t_; asm volatile("s_add_u32 m0, %1, %2\n\ts_add_u32 %0, %5, %6\n\tbuffer_load_dwordx4 %3, %4, %0 offen sc1 lds" \
    : "=&s"(t_) : "s"(m0b), "i"(ldsoff), "v"(voff), "s"(rs), "s"(sbase), "i"(soffimm) : "scc", "memory"); } while (0)

__device__ __forceinline__ rsrc_t mk_rsrc(const void* p, unsigned bytes) { return __builtin_amdgcn_make_buffer_rsrc((void*)p, 0, bytes, 0x00020000); }
__device__ __forceinline__ float u2f(unsigned u) { return __builtin_bit_cast(float, u); }
__device__ __forceinline__ unsigned f2u(float f) { return __builtin_bit_cast(unsigned, f); }
__device__ __forceinline__ unsigned pk_bf16(float a, float b) { bf16x2 v; v[0] = (__bf16)a; v[1] = (__bf16)b; return __builtin_bit_cast(unsigned, v); }
__device__ __forceinline__ float bf_lo(unsigned u) { return u2f(u << 16); }
__device__ __forceinline__ float bf_hi(unsigned u) { return u2f(u & 0xffff0000u); }
__device__ __forceinline__ float rbf(float f) { return (float)(__bf16)f; }
__device__ __forceinline__ f32x4 mfma32(u32x4 a, u32x4 b, f32x4 c) {
    return __builtin_amdgcn_mfma_f32_16x16x32_bf16(__builtin_bit_cast(bf16x8, a), __builtin_bit_cast(bf16x8, b), c, 0, 0, 0);
}
__device__ __forceinline__ f32x4 mfma16(u32x2 a, u32x2 b, f32x4 c) {
    return __builtin_amdgcn_mfma_f32_16x16x16bf16_1k(__builtin_bit_cast(s16x4, a), __builtin_bit_cast(s16x4, b), c, 0, 0, 0);
}
__device__ __forceinline__ u32x4 lds128(unsigned a) { return *(LAS u32x4*)a; }
__device__ __forceinline__ f32x4 lds128f(unsigned a) { return *(LAS f32x4*)a; }
__device__ __forceinline__ f32x2 lds64f(unsigned a) { return *(LAS f32x2*)a; }
__device__ __forceinline__ u32x2 tr_read(unsigned a) { return __builtin_bit_cast(u32x2, __builtin_amdgcn_ds_read_tr16_b64_v4i16((LAS s16x4*)a)); }
__device__ __forceinline__ u32x4 tr_frag(unsigned a, int step) { const u32x2 x = tr_read(a), y = tr_read(a + step); return (u32x4){x.x, x.y, y.x, y.y}; }
__device__ __forceinline__ float swap16_lo(float v) {
    const unsigned a = f2u(v); auto r = __builtin_amdgcn_permlane16_swap(a, a, false, false); const unsigned x = r[0]; return u2f(x);
}
__device__ __forceinline__ float xmax16_32(float v) {
    unsigned a = f2u(v);
    auto r = __builtin_amdgcn_permlane16_swap(a, a, false, false);
    const unsigned r0 = r[0], r1 = r[1];
    v = __builtin_fmaxf(u2f(r0), u2f(r1));
    a = f2u(v);
    auto s = __builtin_amdgcn_permlane32_swap(a, a, false, false);
    const unsigned s0 = s[0], s1 = s[1];
    return __builtin_fmaxf(u2f(s0), u2f(s1));
}
__device__ __forceinline__ float bcast_row0(float v) { return __builtin_amdgcn_update_dpp(0.f, v, 0x150, 0xf, 0xf, false); }
__device__ __forceinline__ float diag4(f32x4 d, unsigned lane) { const float a = (lane & 1) ? d[1] : d[0], b = (lane & 1) ? d[3] : d[2]; return (lane & 2) ? b : a; }
__device__ __forceinline__ f32x2 ln_stats(float s, float q) {
    const float mean = s * (1.0f / DM);
    const float var = __builtin_fmaxf(q * (1.0f / DM) - mean * mean, 0.0f);
    const float sd = rbf(__builtin_amdgcn_sqrtf(rbf(rbf(var) + 1e-5f)));
    return (f32x2){rbf(mean), __builtin_amdgcn_rcpf(sd)};
}
__device__ __forceinline__ void wait_flags(gu64* base, unsigned n, u64 epoch, unsigned lane) {
    for (unsigned spins = 0; spins < kSpinCap; ++spins) {
        u64 v = epoch;
        if (lane < n) v = __hip_atomic_load(base + lane * 32, __ATOMIC_RELAXED, __HIP_MEMORY_SCOPE_AGENT);
        if (__all(v == epoch)) break;
    }
    __builtin_amdgcn_fence(__ATOMIC_ACQUIRE, "wavefront");
}

struct Params { const u16* x; const u16* wqkv; const u16* wo; const u16* wg; const u16* wu; const u16* wd; u16* out; unsigned char* ws; };

#define STAMP(i) do {} while (0)

__global__ void __launch_bounds__(512, 2) __attribute__((amdgpu_waves_per_eu(2, 2))) fused_block(Params P
) {
    const unsigned tid = threadIdx.x, lane = tid & 63;
    const unsigned w = __builtin_amdgcn_readfirstlane(tid >> 6);
    const unsigned b = blockIdx.x, xcd = b & 7, jj = b >> 3;
    const unsigned p = 4 * (xcd >> 1) + (jj >> 3);
    const unsigned hf = (jj >> 2) & 1, hg = 4 * (xcd & 1) + (jj & 3);
    const unsigned c = 8 * hf + hg;
    const unsigned r16 = lane & 15, g = lane >> 4, l8 = lane >> 3, l7 = lane & 7, q4 = r16 >> 2, p4 = lane & 3;
    STAMP(0);
    const unsigned wk = w * 1024;
    const rsrc_t rs_x = mk_rsrc(P.x, SEQ * DM * 2);
    const rsrc_t rs_w = mk_rsrc(P.wqkv, DM * 3 * DM * 2);
    gu64* const flags = (gu64*)(P.ws + WS_FLAG) + (size_t)p * 64 * 32;
    const unsigned T0 = 128 * p + 64 * hf;
    STAMP(18);
    u32x4 ONES = {0x3F803F80u, 0x3F803F80u, 0x3F803F80u, 0x3F803F80u};
    PIN(ONES);
    const f32x4 Z4 = {0.f, 0.f, 0.f, 0.f};
    const unsigned fsw = ((q4 >> 1) & 1) | ((g & 1) << 1);
    const unsigned wsw = (l7 ^ (((g & 1) | ((w & 1) << 1)) << 1)) * 16;
    u64 gen = 0;
    const unsigned rt4 = w & 3, kh = w >> 2;
    const unsigned tok_off = (128 * p + 64 * hf + 32 * (rt4 & 1) + (rt4 >> 1) + 2 * r16) * 1024 + (64 * hg + 32 * kh + 16 * (g & 1) + 8 * (g >> 1)) * 2;
    u32x4 xr;

    {
        const unsigned rot = (2 * (jj & 3) + (jj >> 2) + (xcd >> 1)) & 7;
        const unsigned hh = w & 3, par = w >> 2;
        const unsigned vh = l8 * 2048 + ((l7 ^ g) * 16);
        const unsigned vxr = vh ^ ((w & 1) * 64);
        const unsigned sxr = (T0 + 32 * ((w >> 1) & 1) + (w >> 2) + 16 * (w & 1)) * 1024;
        const unsigned vw = (8 * w + l8) * 3072 + wsw + hg * 128;
        {
            const unsigned kap = (w + rot) & 7;
            const unsigned sh = (T0 == 0 ? 0 : T0 - 16) * 1024 + kap * 128;
            const unsigned hb = L1_H + w * 2048;
            if (lane >= 8) { DMA(hb, 0, vh, rs_x, sh, 0); const unsigned vh1 = vh ^ 64; DMA(hb, 1024, vh1, rs_x, sh, 1024); }
            else { *(LAS u32x4*)(hb + lane * 16) = ONES; *(LAS u32x4*)(hb + 1024 + lane * 16) = ONES; }
        }
        unsigned kx[8];
#pragma unroll
        for (int q = 0; q < 8; ++q) kx[q] = (q + rot) & 7;
#define ISSUE_X(q) DMA(wk, L1_X + (q) * 8192, vxr, rs_x, sxr + kx[q] * 128, 0)
#define ISSUE_W1(q, mat) DMA(wk, L1_W + ((q) % 3) * L1_WSLOT + (mat) * 8192, vw, rs_w, kx[q] * 196608, (mat) * 1024)
#define ISSUE_W(q) do { ISSUE_W1(q, 0); ISSUE_W1(q, 1); ISSUE_W1(q, 2); } while (0)
        ISSUE_X(0); ISSUE_W(0);
        __builtin_amdgcn_s_barrier();
        ISSUE_X(1); ISSUE_W(1);
        u64 genv;
        STAMP(17);
        const unsigned xa = (r16 >> 3) * 1024 + (r16 & 7) * 128 + ((g ^ ((r16 >> 1) & 7)) * 16);
        const unsigned xc0 = xa + par * 4096, xc1 = xc0 ^ 64;
        unsigned xh0 = xa + L1_H, xh1 = xh0 ^ 64;
        PIN(xh0); PIN(xh1);
        unsigned wa = L1_W + (8 * g + q4) * 128 + ((hh ^ fsw) * 32) + 8 * p4, wa2 = wa + 2 * L1_WSLOT;
        PIN(wa); PIN(wa2);
        const unsigned sxa = (xa ^ ((hh & 1) * 64));
        unsigned stc = sxa + par * 4096 + (hh >> 1) * 8192, sth = sxa + L1_H + (hh >> 1) * 2048;
        PIN(stc); PIN(sth);
        f32x4 mh, mp, mc;
#pragma unroll
        for (int r = 0; r < 4; ++r) {
            const unsigned kap = 4 * g + r;
            mh[r] = (T0 > 0 && (kap >> 3) == par && (kap & 7) >= r16 + 1) ? 0.f : -1e30f;
            mp[r] = (kap >= r16 + 9) ? 0.f : -1e30f;
            mc[r] = (kap <= r16 && r16 - kap <= 7) ? 0.f : -1e30f;
        }
        PIN(mh); PIN(mp); PIN(mc);

        f32x4 aq[3] = {Z4, Z4, Z4}, ak[3] = {Z4, Z4, Z4}, av[3] = {Z4, Z4, Z4};
        f32x4 gr[3] = {Z4, Z4, Z4}, rs[3] = {Z4, Z4, Z4};
        f32x2 sL[3]; f32x4 sR[3][2];
        const unsigned part = L1_SCR, fin = L1_SCR + 2560 + w * 384;
        u32x4 xf[2][3], wf[2][3], sf[2][3];
        const rsrc_t rs_wo = mk_rsrc(P.wo, DM * DM * 2);
        const unsigned vwo = (8 * w + l8) * 1024 + wsw + hg * 128;
#define LOAD1(s) do { const int kq_ = (s) >> 1, kk_ = (s) & 1; \
            xf[(s) & 1][0] = lds128((kk_ ? xh1 : xh0) + kq_ * 2048); xf[(s) & 1][1] = lds128((kk_ ? xc1 : xc0) + kq_ * 8192); xf[(s) & 1][2] = lds128((kk_ ? xc1 : xc0) + kq_ * 8192 + 2048); \
            _Pragma("unroll") for (int mat = 0; mat < 3; ++mat) wf[(s) & 1][mat] = tr_frag((kq_ % 3 == 2 ? wa2 : wa + (kq_ % 3) * L1_WSLOT) + mat * 8192 + kk_ * 4096, 512); } while (0)
#define JOBS1(it) do { \
            if ((it) == 0) { ISSUE_X(2); ISSUE_W(2); } \
            if ((it) >= 1 && (it) <= 5) ISSUE_W((it) + 2 > 7 ? 7 : (it) + 2); \
            if ((it) == 1) { ISSUE_X(3); ISSUE_X(4); ISSUE_X(5); } \
            if ((it) == 2) { ISSUE_X(6); ISSUE_X(7); } \
            if ((it) == 6) { DMA(wk, L2_WO + 6 * 8192, vwo, rs_wo, 0u, 6 * 65536); DMA(wk, L2_WO + 7 * 8192, vwo, rs_wo, 0u, 7 * 65536); \
                             DMA(wk, 0, vwo, rs_wo, 0u, 3 * 65536); DMA(wk, 8192, vwo, rs_wo, 0u, 4 * 65536); DMA(wk, 16384, vwo, rs_wo, 0u, 5 * 65536); } \
            if ((it) == 7) { DMA(wk, L2_WO, vwo, rs_wo, 0u, 0); DMA(wk, L2_WO + 8192, vwo, rs_wo, 0u, 65536); DMA(wk, L2_WO + 2 * 8192, vwo, rs_wo, 0u, 2 * 65536); } \
            __builtin_amdgcn_sched_barrier(0); } while (0)
        WAIT_VM(4);
        BAR();
        STAMP(1);
        LOAD1(0);
#pragma unroll
        for (int s = 0; s < 16; ++s) {
            if (s & 1) {
                if (s == 1) WAIT_VM(4); else if (s == 3) WAIT_VM(6); else if (s == 5) WAIT_VM(8); else if (s == 7) WAIT_VM(6); else if (s < 13) WAIT_VM(3); else if (s == 13) WAIT_VM(6);
                if (s < 15) BAR();
                if (s == 9) { asm volatile("" : "+v"(genv)); gen = ((u64)(unsigned)__builtin_amdgcn_readfirstlane((unsigned)(genv >> 32)) << 32) | (unsigned)__builtin_amdgcn_readfirstlane((unsigned)genv); }
            }
            if ((s & 1) || s == 0) { if (w < 4) JOBS1((s + 1) / 2); }
            if (s < 15) LOAD1(s + 1);
            if (s >= 6 && s <= 9) { const int j = s - 6; sf[j & 1][0] = lds128(sth + j * 4096); sf[j & 1][1] = lds128(stc + j * 16384); sf[j & 1][2] = lds128(stc + j * 16384 + 2048); }
            f32x2 pp[4];
            if (s == 11) {
                const unsigned pa = part + ((g == 0 ? 0 : (g == 3 ? 0 : 2 * par + g)) * 64 + r16) * 8;
                pp[0] = lds64f(pa); pp[1] = lds64f(pa + 128); pp[2] = lds64f(pa + 256); pp[3] = lds64f(pa + 384);
            }
            if (s == 13) {
#pragma unroll
                for (int t = 0; t < 3; ++t) { sL[t] = lds64f(fin + (t * 16 + r16) * 8); sR[t][0] = lds128f(fin + (t * 16 + 4 * g) * 8); sR[t][1] = lds128f(fin + (t * 16 + 4 * g) * 8 + 16); }
            }
#pragma unroll
            for (int t = 0; t < 3; ++t) {
                aq[t] = mfma32(wf[s & 1][0], xf[s & 1][t], aq[t]); ak[t] = mfma32(wf[s & 1][1], xf[s & 1][t], ak[t]); av[t] = mfma32(xf[s & 1][t], wf[s & 1][2], av[t]);
            }
            if (s >= 7 && s <= 10) {
#pragma unroll
                for (int t = 0; t < 3; ++t) { gr[t] = mfma32(sf[(s - 7) & 1][t], sf[(s - 7) & 1][t], gr[t]); rs[t] = mfma32(ONES, sf[(s - 7) & 1][t], rs[t]); }
            }
#pragma unroll
            for (int i = 0; i < 9; ++i) { __builtin_amdgcn_sched_group_barrier(0x8, 1, 0); __builtin_amdgcn_sched_group_barrier(0x100, 1, 0); }
#pragma unroll
            for (int i = 0; i < 6; ++i) { __builtin_amdgcn_sched_group_barrier(0x8, 1, 0); __builtin_amdgcn_sched_group_barrier(0x100, 2, 0); }
            __builtin_amdgcn_sched_barrier(0);
            if (s == 10 && g == q4) {
                *(LAS f32x2*)(part + (hh * 16 + r16) * 8) = (f32x2){rs[0][0], diag4(gr[0], lane)};
                *(LAS f32x2*)(part + (((1 + 2 * par) * 4 + hh) * 16 + r16) * 8) = (f32x2){rs[1][0], diag4(gr[1], lane)};
                *(LAS f32x2*)(part + (((2 + 2 * par) * 4 + hh) * 16 + r16) * 8) = (f32x2){rs[2][0], diag4(gr[2], lane)};
            }
            if (s == 11) {
                const f32x2 sm = (pp[0] + pp[1]) + (pp[2] + pp[3]);
                const f32x2 res = ln_stats(sm.x, sm.y);
                if (g < 3) *(LAS f32x2*)(fin + (g * 16 + r16) * 8) = res;
            }
            __builtin_amdgcn_sched_barrier(0);
            if (!(s & 1)) { if (w >= 4) JOBS1(s / 2); }
            if (s == 12) asm volatile("buffer_load_dwordx4 %0, %1, %2, 0 offen" : "=v"(xr) : "v"(tok_off), "s"(rs_x) : "memory");
            if (s == 4) { const gu64* gp = flags + FL_GEN * 32; asm volatile("global_load_dwordx2 %0, %1, off sc1" : "=v"(genv) : "v"(gp) : "memory"); }
        }
        STAMP(2);
        f32x4 csq, csk;
#pragma unroll
        for (int r = 0; r < 4; ++r) { csq[r] = bcast_row0(aq[0][r]); csk[r] = bcast_row0(ak[0][r]); }
        const float csv = swap16_lo(av[0][0]);
        u32x2 qb[2], kb[3], vb[3];
#pragma unroll
        for (int t = 0; t < 3; ++t) {
            const f32x4 kv = (ak[t] - sL[t].x * csk) * sL[t].y;
            kb[t] = (u32x2){pk_bf16(kv[0], kv[1]), pk_bf16(kv[2], kv[3])};
            if (t > 0) { const f32x4 qv = (aq[t] - sL[t].x * csq) * sL[t].y; qb[t - 1] = (u32x2){pk_bf16(qv[0], qv[1]), pk_bf16(qv[2], qv[3])}; }
            const f32x4 mn = {sR[t][0][0], sR[t][0][2], sR[t][1][0], sR[t][1][2]}, rd = {sR[t][0][1], sR[t][0][3], sR[t][1][1], sR[t][1][3]};
            const f32x4 vv = (av[t] - mn * csv) * rd;
            vb[t] = (u32x2){pk_bf16(vv[0], vv[1]), pk_bf16(vv[2], vv[3])};
        }
        const float SC = 0.25f * 1.44269504088896f;
        u32x2 ob[2];
#pragma unroll
        for (int t = 0; t < 2; ++t) {
            f32x4 sp = mfma16(kb[t], qb[t], t == 0 ? mh : mp), sc = mfma16(kb[t + 1], qb[t], mc);
            float mx = __builtin_fmaxf(__builtin_fmaxf(__builtin_fmaxf(sp[0], sp[1]), __builtin_fmaxf(sp[2], sp[3])), __builtin_fmaxf(__builtin_fmaxf(sc[0], sc[1]), __builtin_fmaxf(sc[2], sc[3])));
            mx = xmax16_32(mx);
            const float mb = -mx * SC;
#pragma unroll
            for (int r = 0; r < 4; ++r) { sp[r] = __builtin_amdgcn_exp2f(sp[r] * SC + mb); sc[r] = __builtin_amdgcn_exp2f(sc[r] * SC + mb); }
            const u32x4 pf = {pk_bf16(sp[0], sp[1]), pk_bf16(sp[2], sp[3]), pk_bf16(sc[0], sc[1]), pk_bf16(sc[2], sc[3])};
            const u32x4 vf = {vb[t].x, vb[t].y, vb[t + 1].x, vb[t + 1].y};
            const f32x4 sm = mfma32(ONES, pf, Z4);
            const f32x4 o = mfma32(vf, pf, Z4) * __builtin_amdgcn_rcpf(sm[0]);
            ob[t] = (u32x2){pk_bf16(o[0], o[1]), pk_bf16(o[2], o[3])};
        }
        auto s0 = __builtin_amdgcn_permlane16_swap(ob[0].x, ob[1].x, false, false);
        auto s1 = __builtin_amdgcn_permlane16_swap(ob[0].y, ob[1].y, false, false);
        const u32x4 o_frag = {s0[0], s1[0], s0[1], s1[1]};
        const rsrc_t rs_ws1 = mk_rsrc(P.ws, WS_BYTES);
        const unsigned off = ((g & 1) * 16384) + ((2 * (hh & 1) + (g >> 1)) * 16 + r16) * 16;
        const unsigned soff = WS_O + ((p * 8 + 4 * hf + 2 * par) * 16 + 2 * hg + (hh >> 1)) * 1024;
        __builtin_amdgcn_raw_buffer_store_b128(o_frag, rs_ws1, off + soff, 0, AUX_SC1);
        asm volatile("" ::: "memory");
    }
    STAMP(3);
    const rsrc_t rs_ws = mk_rsrc(P.ws, WS_BYTES);
    const u64 epoch = gen + 1;
    const unsigned n3 = w & 3, m3 = w >> 2;
    const rsrc_t rs_gu = mk_rsrc(m3 ? P.wu : P.wg, DM * FF * 2);
    unsigned vgu[3];
#pragma unroll
    for (int i = 0; i < 3; ++i) {
        const unsigned s = (n3 * 3 + i) * 64 + lane;
        const unsigned ks2 = s >= 384 ? 1 : 0, s2 = s - ks2 * 384, row = (s2 * 43691u) >> 19, pos = s2 - row * 12;
        int u = (int)pos - (int)(((row >> 3) & 1) + 2 * m3); if (u < 0) u += 12;
        u = min(u, (int)(c < 15 ? 171 - 12 * c : 0));
        vgu[i] = (ks2 * 32 + row) * (FF * 2) + c * 192 + u * 16;
    }
    unsigned mgu[3];
#pragma unroll
    for (int i = 0; i < 3; ++i) { const unsigned pi = n3 * 3 + i; mgu[i] = L3_B + (pi >= 6 ? L3_BSLOT + (pi - 6) * 1024 : pi * 1024) + m3 * 6144; }
    const unsigned rot3 = (2 * (jj >> 3) + (jj & 7)) & 7;
#define ISSUE_B(pr) do { const unsigned sb_ = (((pr) + rot3) & 7) * (64 * FF * 2); _Pragma("unroll") for (int i_ = 0; i_ < 3; ++i_) DMA(mgu[i_], ((pr) & 3) * 2 * L3_BSLOT, vgu[i_], rs_gu, sb_, 0); } while (0)

    u32x4 h_keep;
    {
        WAIT_VM(0);
        BAR();
        if (tid == 0) __hip_atomic_store(flags + (FL_1 + c) * 32, epoch, __ATOMIC_RELAXED, __HIP_MEMORY_SCOPE_AGENT);
        STAMP(4);
        asm volatile("" : "+v"(xr));
        const unsigned wl = (8 * g + q4) * 128 + (fsw * 32) + 8 * p4;
        const unsigned wb0 = wl + (kh ? 8192 : L2_WO), wb1 = wl + (kh ? L2_WO + 49152 : L2_WO + 16384), wb2 = wl + (kh ? L2_WO + 57344 : 0);
        unsigned abase = lane * 16;
        const unsigned asoff = WS_O + ((p * 8 + 4 * hf + rt4) * 16 + 8 * kh) * 1024;
        PIN(abase);
        u32x4 bf[8][4];
#pragma unroll
        for (int k = 0; k < 8; ++k)
#pragma unroll
            for (int ct = 0; ct < 4; ++ct) bf[k][ct] = tr_frag(((k < 4 ? wb0 : (k < 6 ? wb1 : wb2)) ^ (ct * 32)) + (k < 4 ? k : (k < 6 ? k - 4 : k - 6)) * 4096, 512);
        __builtin_amdgcn_sched_barrier(0);
        STAMP(19);
        if (w == 0) wait_flags(flags + (FL_1 + 8 * hf) * 32, 8, epoch, lane);
        BAR();
        STAMP(5);
        u32x4 af[8];
#pragma unroll
        for (int k = 0; k < 4; ++k) af[k] = __builtin_amdgcn_raw_buffer_load_b128(rs_ws, abase + (k & 3) * 1024, asoff + (k >> 2) * 4096, AUX_SC1);
        __builtin_amdgcn_sched_barrier(0);
        f32x4 acc[4] = {Z4, Z4, Z4, Z4};
#pragma unroll
        for (int k = 0; k < 8; ++k) {
            if (k + 4 < 8) af[k + 4] = __builtin_amdgcn_raw_buffer_load_b128(rs_ws, abase + (k & 3) * 1024, asoff + 4096, AUX_SC1);
            __builtin_amdgcn_sched_barrier(0);
#pragma unroll
            for (int ct = 0; ct < 4; ++ct) acc[ct] = mfma32(bf[k][ct], af[k], acc[ct]);
            __builtin_amdgcn_sched_barrier(0);
        }
        STAMP(6);
        const unsigned red = L2_RED + w * 2048 + lane * 16;
        *(LAS f32x4*)red = kh ? acc[0] : acc[2];
        *(LAS f32x4*)(red + 1024) = kh ? acc[1] : acc[3];
        BAR();
        const f32x4 a0 = (kh ? acc[2] : acc[0]) + lds128f(red ^ 8192);
        const f32x4 a1 = (kh ? acc[3] : acc[1]) + lds128f((red ^ 8192) + 1024);
        auto s0 = __builtin_amdgcn_permlane16_swap(pk_bf16(a0[0], a0[1]), pk_bf16(a1[0], a1[1]), false, false);
        auto s1 = __builtin_amdgcn_permlane16_swap(pk_bf16(a0[2], a0[3]), pk_bf16(a1[2], a1[3]), false, false);
        const u32x4 at = {s0[0], s1[0], s0[1], s1[1]};
#pragma unroll
        for (int i = 0; i < 4; ++i) h_keep[i] = pk_bf16(bf_lo(at[i]) + bf_lo(xr[i]), bf_hi(at[i]) + bf_hi(xr[i]));
        const unsigned hoff = ((2 * (g & 1) + (g >> 1)) * 16 + r16) * 16;
        const unsigned hsoff = WS_H + ((p * 8 + 4 * hf + rt4) * 16 + 2 * hg + kh) * 1024;
        __builtin_amdgcn_raw_buffer_store_b128(h_keep, rs_ws, hoff + hsoff, 0, AUX_SC1);
        const f32x4 gs = mfma32(h_keep, h_keep, Z4), sm = mfma32(ONES, h_keep, Z4);
        const u32x2 stv = {f2u(sm[0]), f2u(diag4(gs, lane))};
        const unsigned stoff = (g == q4) ? WS_ST + p * 16384 + c * 1024 + w * 128 + r16 * 8 : 0xFFFFFFF0u;
        __builtin_amdgcn_raw_buffer_store_b64(stv, rs_ws, stoff, 0, AUX_SC1);
        asm volatile("" ::: "memory");
    }
    STAMP(7);
    const rsrc_t rs_wd = mk_rsrc(P.wd, FF * DM * 2);
    const unsigned vwd = l8 * 1024 + wsw + hg * 128;
    const unsigned swd = ((w >> 2) * 32 + (w & 3) * 8) * 1024;
#define ISSUE_D2(s0) DMA(wk, (s0) * 4096, vwd, rs_wd, swd, ((s0) < 20 ? (s0) + 2 : (s0) + 3) * 32768)
    {
        const bool active = c < 15;
        WAIT_VM(0);
        BAR();
        if (tid == 0) __hip_atomic_store(flags + (FL_2 + c) * 32, epoch, __ATOMIC_RELAXED, __HIP_MEMORY_SCOPE_AGENT);
        if (active && w != 0) { ISSUE_B(0); ISSUE_B(1); ISSUE_B(2); ISSUE_B(3); }
        STAMP(8);
        unsigned aa0 = L3_A + (4 * m3) * 1024 + lane * 16 + m3 * 8192, aa1 = aa0 ^ 8192;
        unsigned ba0[3], ba1[3];
#pragma unroll
        for (int i = 0; i < 3; ++i) {
            const unsigned mat = p4 >> 1;
            unsigned pos = 3 * n3 + i + (g & 1) + 2 * mat; if (pos >= 12) pos -= 12;
            const unsigned a = L3_B + mat * 6144 + ((8 * g + q4) * 12 + pos) * 16 + 8 * (p4 & 1);
            ba0[i] = a + m3 * L3_BSLOT; ba1[i] = a + (1 - m3) * L3_BSLOT;
            PIN(ba0[i]); PIN(ba1[i]);
        }
        unsigned vl16 = lane * 16;
        const unsigned sa3 = WS_H + (p * 8 + w) * 16384, sst = WS_ST + p * 16384 + w * 2048;
        PIN(aa0); PIN(aa1); PIN(vl16);
        if (w == 0) wait_flags(flags + FL_2 * 32, 16, epoch, lane);
        BAR();
        STAMP(9);
        if (active && w == 0) { ISSUE_B(0); ISSUE_B(1); ISSUE_B(2); ISSUE_B(3); }
        if (tid == 0 && c == 0) __hip_atomic_store(flags + FL_GEN * 32, epoch, __ATOMIC_RELAXED, __HIP_MEMORY_SCOPE_AGENT);
        if (active) {
#define ISSUE_A(pr) do { const unsigned sa_ = sa3 + (((pr) + rot3) & 7) * 2048; DMA_SC1(wk, L3_A + ((pr) % 3) * 16384, vl16, rs_ws, sa_, 0); DMA_SC1(wk, L3_A + ((pr) % 3) * 16384 + 8192, vl16, rs_ws, sa_, 1024); } while (0)
            ISSUE_A(0); ISSUE_A(1); ISSUE_A(2);
            DMA_SC1(wk + wk, L3_ST, vl16, rs_ws, sst, 0); DMA_SC1(wk + wk, L3_ST + 1024, vl16, rs_ws, sst, 1024);
            f32x4 acc[4][3], cs[3] = {Z4, Z4, Z4};
#pragma unroll
            for (int t = 0; t < 4; ++t)
#pragma unroll
                for (int i = 0; i < 3; ++i) acc[t][i] = Z4;
            f32x2 st[4];
            u32x4 af[2][4], bf[2][3];
            unsigned sa = L3_ST + (tid >> 8) * 8192 + (tid & 3) * 2048 + ((tid >> 2) & 63) * 8, sf = L3_ST + 512 + m3 * 8192 + r16 * 8;
            PIN(sa); PIN(sf);
#define LOAD3(s) do { const int pr_ = (s) >> 1, k2_ = (s) & 1; \
                bf[k2_][0] = tr_frag((k2_ ? ba1[0] : ba0[0]) + (pr_ & 3) * 2 * L3_BSLOT, 4 * 12 * 16); \
                _Pragma("unroll") for (int t = 0; t < 4; ++t) af[k2_][t] = lds128((k2_ ? aa1 : aa0) + (pr_ % 3) * 16384 + t * 1024); \
                _Pragma("unroll") for (int i = 1; i < 3; ++i) bf[k2_][i] = tr_frag((k2_ ? ba1[i] : ba0[i]) + (pr_ & 3) * 2 * L3_BSLOT, 4 * 12 * 16); } while (0)
#define JOBS3(po) do { \
                if ((po) + 3 < 8) ISSUE_A((po) + 3 < 8 ? (po) + 3 : 7); \
                if ((po) + 4 < 8) ISSUE_B((po) + 4 < 8 ? (po) + 4 : 7); \
                if ((po) >= 4) { ISSUE_D2(12 + 6 * ((po) >= 4 ? (po) - 4 : 0)); ISSUE_D2(14 + 6 * ((po) >= 4 ? (po) - 4 : 0)); ISSUE_D2(16 + 6 * ((po) >= 4 ? (po) - 4 : 0)); } \
                __builtin_amdgcn_sched_barrier(0); } while (0)
            WAIT_VM(6);
            BAR();
            STAMP(10);
            LOAD3(0);
#pragma unroll
            for (int s = 0; s < 16; ++s) {
                const int pr = s >> 1;
                if ((s & 1) && s < 15) {
                    if (s == 1) WAIT_VM(4); else if (s == 3) WAIT_VM(7); else if (s < 13) WAIT_VM(8); else WAIT_VM(6);
                    BAR();
                }
                if ((s & 1) && s < 15) { if (w < 4) JOBS3(s >> 1); }
                if (s < 15) LOAD3(s + 1);
                f32x2 pp[4];
                if (s == 6) { pp[0] = lds64f(sa); pp[1] = lds64f(sa + 512); pp[2] = lds64f(sa + 1024); pp[3] = lds64f(sa + 1536); }
                if (s == 12) {
#pragma unroll
                    for (int t = 0; t < 4; ++t) st[t] = lds64f(sf + t * 128);
                }
#pragma unroll
                for (int i = 0; i < 3; ++i) {
#pragma unroll
                    for (int t = 0; t < 4; ++t) acc[t][i] = mfma32(bf[s & 1][i], af[s & 1][t], acc[t][i]);
                    if ((s & 1) == 0) cs[i] = mfma32(bf[s & 1][i], ONES, cs[i]);
                }
#pragma unroll
                for (int i = 0; i < 10; ++i) { __builtin_amdgcn_sched_group_barrier(0x8, 1, 0); __builtin_amdgcn_sched_group_barrier(0x100, 1, 0); }
#pragma unroll
                for (int i = 0; i < 5; ++i) { __builtin_amdgcn_sched_group_barrier(0x8, 1, 0); __builtin_amdgcn_sched_group_barrier(0x100, 1, 0); }
                __builtin_amdgcn_sched_barrier(0);
                if (s == 6) {
                    const f32x2 sm = (pp[0] + pp[1]) + (pp[2] + pp[3]);
                    float sx = sm.x, sq = sm.y;
                    sx += __builtin_amdgcn_update_dpp(0.f, sx, 0xB1, 0xf, 0xf, false); sq += __builtin_amdgcn_update_dpp(0.f, sq, 0xB1, 0xf, 0xf, false);
                    sx += __builtin_amdgcn_update_dpp(0.f, sx, 0x4E, 0xf, 0xf, false); sq += __builtin_amdgcn_update_dpp(0.f, sq, 0x4E, 0xf, 0xf, false);
                    if ((lane & 3) == 0) *(LAS f32x2*)(sa + 512) = ln_stats(sx, sq);
                }
                __builtin_amdgcn_sched_barrier(0);
                if (!(s & 1) && s >= 2) { if (w >= 4) JOBS3((s >> 1) - 1); }
            }
            STAMP(11);
            if (r16 == 0) {
#pragma unroll
                for (int i = 0; i < 3; ++i) *(LAS f32x4*)(L3_ST + 1024 + (3 * w + i) * 64 + g * 16) = cs[i];
            }
            BAR();
            DMA(wk, 30 * 4096, vwd, rs_wd, swd, 0);
            DMA(wk, 32 * 4096, vwd, rs_wd, swd + (kh ? 35 : 22) * 32768, 0);
            ISSUE_D2(0); ISSUE_D2(2); ISSUE_D2(4);
            const unsigned hi = lane >> 5;
            const float r01 = hi ? st[1].y : st[0].y, r23 = hi ? st[3].y : st[2].y;
            const float rr01 = r01 * r01, rr23 = r23 * r23;
            const unsigned tau = 2 * (g & 1) + (g >> 1);
#pragma unroll
            for (int i = 0; i < 3; ++i) {
                const f32x4 cv = cs[i] + lds128f(L3_ST + 1024 + (3 * (w ^ 4) + i) * 64 + g * 16);
                u32x2 hp[2];
#pragma unroll
                for (int pp = 0; pp < 2; ++pp) {
                    const f32x4 e0 = acc[2 * pp][i] - st[2 * pp].x * cv, e1 = acc[2 * pp + 1][i] - st[2 * pp + 1].x * cv;
                    float hv[4];
#pragma unroll
                    for (int r = 0; r < 4; ++r) {
                        auto sw = __builtin_amdgcn_permlane32_swap(f2u(e0[r]), f2u(e1[r]), false, false);
                        const unsigned x0 = sw[0], x1 = sw[1];
                        hv[r] = __builtin_fmaxf(u2f(x0), 0.0f) * (u2f(x1) * (pp ? rr23 : rr01));
                    }
                    hp[pp] = (u32x2){pk_bf16(hv[0], hv[1]), pk_bf16(hv[2], hv[3])};
                }
                auto s0 = __builtin_amdgcn_permlane16_swap(hp[0].x, hp[1].x, false, false);
                auto s1 = __builtin_amdgcn_permlane16_swap(hp[0].y, hp[1].y, false, false);
                const u32x4 hf4 = {s0[0], s1[0], s0[1], s1[1]};
                const unsigned U = 12 * c + 3 * n3 + i;
                const unsigned soff = WS_HID + ((p * 8 + 4 * m3) * NKS4 + (U >> 2)) * 1024 + (U & 3) * 256;
                const unsigned off = U < 172 ? tau * (NKS4 * 1024) + r16 * 16 + soff : 0xFFFFFFF0u;
                __builtin_amdgcn_raw_buffer_store_b128(hf4, rs_ws, off, 0, AUX_SC1);
            }
            asm volatile("" ::: "memory");
        }
    }
    STAMP(12);
    {
        const bool active = c < 15;
        if (!active) {
            ISSUE_D2(12); ISSUE_D2(14); ISSUE_D2(16); ISSUE_D2(18); ISSUE_D2(20); ISSUE_D2(22); ISSUE_D2(24); ISSUE_D2(26); ISSUE_D2(28);
            DMA(wk, 30 * 4096, vwd, rs_wd, swd, 0);
            DMA(wk, 32 * 4096, vwd, rs_wd, swd + (kh ? 35 : 22) * 32768, 0);
            ISSUE_D2(0); ISSUE_D2(2); ISSUE_D2(4);
        }
        WAIT_VM(0);
        BAR();
        if (tid == 0 && active) __hip_atomic_store(flags + (FL_3 + c) * 32, epoch, __ATOMIC_RELAXED, __HIP_MEMORY_SCOPE_AGENT);
        STAMP(13);
#define REST_D() do { ISSUE_D2(6); ISSUE_D2(8); ISSUE_D2(10); ISSUE_D2(34); ISSUE_D2(36); ISSUE_D2(38); } while (0)
        if (w != 0) REST_D();
        const unsigned wb0 = (8 * g + q4) * 128 + (fsw * 32) + 8 * p4;
        u32x4 bq[22][4];
#pragma unroll
        for (int ct = 0; ct < 4; ++ct) {
            const u32x4 t0 = tr_frag((wb0 ^ (ct * 32)) + 30 * 4096, 512);
            bq[1][ct] = tr_frag((wb0 ^ (ct * 32)) + (kh ? 32 : 31) * 4096, 512);
            bq[0][ct] = kh ? (u32x4){0u, 0u, 0u, 0u} : t0;
        }
        BAR();
#define LAST_D() do { ISSUE_D2(30); DMA(wk, 32 * 4096, vwd, rs_wd, swd, 35 * 32768); } while (0)
        if (w != 0) LAST_D();
        unsigned wbk = wb0 + kh * (20 * 4096);
        unsigned abase = lane * 16;
        const unsigned asoff = WS_HID + ((p * 8 + 4 * hf + rt4) * NKS4 + 21 * kh) * 1024;
        PIN(wbk); PIN(abase);
        if (w == 0) wait_flags(flags + FL_3 * 32, 15, epoch, lane);
        BAR();
        STAMP(14);
        u32x4 af[22];
#define LOADA4(k) af[k] = __builtin_amdgcn_raw_buffer_load_b128(rs_ws, abase + ((k) & 3) * 1024, asoff + ((k) >> 2) * 4096, AUX_SC1)
        if (w == 0) { REST_D(); LAST_D(); }
#pragma unroll
        for (int k = 0; k < 6; ++k) LOADA4(k);
        WAIT_VM(6);
        BAR();
#define LOAD4(j) do { _Pragma("unroll") for (int ct = 0; ct < 4; ++ct) bq[j][ct] = tr_frag((wbk ^ (ct * 32)) + ((j) - 2) * 4096, 512); } while (0)
        LOAD4(2); LOAD4(3); LOAD4(4); LOAD4(5);
        __builtin_amdgcn_sched_barrier(0);
        f32x4 acc[4] = {Z4, Z4, Z4, Z4};
#pragma unroll
        for (int j = 0; j < 22; ++j) {
            if (j + 6 < 22) { LOADA4(j + 6); LOAD4(j + 6); }
#pragma unroll
            for (int ct = 0; ct < 4; ++ct) acc[ct] = mfma32(bq[j][ct], af[j], acc[ct]);
#pragma unroll
            for (int i = 0; i < 4; ++i) { __builtin_amdgcn_sched_group_barrier(0x8, 1, 0); __builtin_amdgcn_sched_group_barrier(0x100, 2, 0); }
            __builtin_amdgcn_sched_barrier(0);
        }
        STAMP(15);
        BAR();
        const unsigned red = w * 2048 + lane * 16;
        *(LAS f32x4*)red = kh ? acc[0] : acc[2];
        *(LAS f32x4*)(red + 1024) = kh ? acc[1] : acc[3];
        BAR();
        const f32x4 a0 = (kh ? acc[2] : acc[0]) + lds128f(red ^ 8192);
        const f32x4 a1 = (kh ? acc[3] : acc[1]) + lds128f((red ^ 8192) + 1024);
        auto s0 = __builtin_amdgcn_permlane16_swap(pk_bf16(a0[0], a0[1]), pk_bf16(a1[0], a1[1]), false, false);
        auto s1 = __builtin_amdgcn_permlane16_swap(pk_bf16(a0[2], a0[3]), pk_bf16(a1[2], a1[3]), false, false);
        const u32x4 at = {s0[0], s1[0], s0[1], s1[1]};
        u32x4 o;
#pragma unroll
        for (int i = 0; i < 4; ++i) o[i] = pk_bf16(bf_lo(at[i]) + bf_lo(h_keep[i]), bf_hi(at[i]) + bf_hi(h_keep[i]));
        *(u32x4*)((char*)P.out + tok_off) = o;
    }
    STAMP(16);
}

}

extern "C" void kernel_launch(void* const* d_in, const int* in_sizes, int n_in, void* d_out, int out_size, void* d_ws, size_t ws_size, hipStream_t stream) {
    Params P;
    P.x = (const u16*)d_in[0]; P.wqkv = (const u16*)d_in[1]; P.wo = (const u16*)d_in[2];
    P.wg = (const u16*)d_in[4]; P.wu = (const u16*)d_in[5]; P.wd = (const u16*)d_in[6];
    P.out = (u16*)d_out; P.ws = (unsigned char*)d_ws;
    (void)hipFuncSetAttribute((const void*)fused_block, hipFuncAttributeMaxDynamicSharedMemorySize, LDS_BYTES);
    hipLaunchKernelGGL(fused_block, dim3(256), dim3(512), LDS_BYTES, stream, P);
}
```

```cpp
#include <hip/hip_runtime.h>
#include <hip/hip_bf16.h>
#include <stdint.h>
#pragma clang diagnostic ignored "-Wint-to-pointer-cast"

#define LAS __attribute__((address_space(3)))
#define GAS __attribute__((address_space(1)))
typedef unsigned short u16;
typedef unsigned long long u64;
typedef short s16x4 __attribute__((ext_vector_type(4)));
typedef __bf16 bf16x8 __attribute__((ext_vector_type(8)));
typedef __bf16 bf16x2 __attribute__((ext_vector_type(2)));
typedef float f32x4 __attribute__((ext_vector_type(4)));
typedef float f32x2 __attribute__((ext_vector_type(2)));
typedef unsigned u32x4 __attribute__((ext_vector_type(4)));
typedef unsigned u32x2 __attribute__((ext_vector_type(2)));
typedef GAS u64 gu64;
typedef __amdgpu_buffer_rsrc_t rsrc_t;

namespace {

constexpr int SEQ = 2048, DM = 512, FF = 1376;
constexpr int NKS4 = FF / 32;
constexpr unsigned WS_FLAG = 0;
constexpr unsigned WS_ST = 16384;
constexpr unsigned WS_O = 40960;
constexpr unsigned WS_H = WS_O + 131072;
constexpr unsigned WS_HID = WS_H + 131072;
constexpr unsigned WS_SPARE = WS_HID + 8 * 43 * 1024;
constexpr unsigned WS_BYTES = WS_SPARE + 8192;
constexpr int FL_GEN = 0, FL_1 = 8, FL_2 = 24, FL_3 = 40;
constexpr int AUX_SC1 = 16;
constexpr unsigned kSpinCap = 1u << 16;

constexpr int LDS_BYTES = 163840;
constexpr int L1_X = 0, L1_H = 65536, L1_W = 81920, L1_WSLOT = 24576, L1_SCR = 155648;
constexpr int L2_WO = 81920, L2_RED = 114688;
constexpr int L3_A = 0, L3_B = 49152, L3_BSLOT = 12288, L3_ST = 147456;

__device__ __forceinline__ constexpr int vm_imm(int n) { return (n & 0xF) | ((n >> 4) << 14) | (0x7 << 4) | (0xF << 8); }
#define WAIT_VM(n) asm volatile("s_waitcnt vmcnt(%0)" :: "i"(n) : "memory")
#define BAR() do { asm volatile("s_waitcnt lgkmcnt(0)" ::: "memory"); __builtin_amdgcn_s_barrier(); asm volatile("" ::: "memory"); } while (0)
#define PIN(x) asm volatile("" : "+v"(x))
#define DMA(m0b, ldsoff, voff, rs, sbase, soffimm) do { unsigned t_; asm volatile("s_add_u32 m0, %1, %2\n\ts_add_u32 %0, %5, %6\n\tbuffer_load_dwordx4 %3, %4, %0 offen lds" \
    : "=&s"(t_) : "s"(m0b), "i"(ldsoff), "v"(voff), "s"(rs), "s"(sbase), "i"(soffimm) : "scc", "memory"); } while (0)
#define DMA_SC1(m0b, ldsoff, voff, rs, sbase, soffimm) do { unsigned t_; asm volatile("s_add_u32 m0, %1, %2\n\ts_add_u32 %0, %5, %6\n\tbuffer_load_dwordx4 %3, %4, %0 offen sc1 lds" \
    : "=&s"(t_) : "s"(m0b), "i"(ldsoff), "v"(voff), "s"(rs), "s"(sbase), "i"(soffimm) : "scc", "memory"); } while (0)

__device__ __forceinline__ rsrc_t mk_rsrc(const void* p, unsigned bytes) { return __builtin_amdgcn_make_buffer_rsrc((void*)p, 0, bytes, 0x00020000); }
__device__ __forceinline__ float u2f(unsigned u) { return __builtin_bit_cast(float, u); }
__device__ __forceinline__ unsigned f2u(float f) { return __builtin_bit_cast(unsigned, f); }
__device__ __forceinline__ unsigned pk_bf16(float a, float b) { bf16x2 v; v[0] = (__bf16)a; v[1] = (__bf16)b; return __builtin_bit_cast(unsigned, v); }
__device__ __forceinline__ float bf_lo(unsigned u) { return u2f(u << 16); }
__device__ __forceinline__ float bf_hi(unsigned u) { return u2f(u & 0xffff0000u); }
__device__ __forceinline__ float rbf(float f) { return (float)(__bf16)f; }
__device__ __forceinline__ f32x4 mfma32(u32x4 a, u32x4 b, f32x4 c) {
    return __builtin_amdgcn_mfma_f32_16x16x32_bf16(__builtin_bit_cast(bf16x8, a), __builtin_bit_cast(bf16x8, b), c, 0, 0, 0);
}
__device__ __forceinline__ f32x4 mfma16(u32x2 a, u32x2 b, f32x4 c) {
    return __builtin_amdgcn_mfma_f32_16x16x16bf16_1k(__builtin_bit_cast(s16x4, a), __builtin_bit_cast(s16x4, b), c, 0, 0, 0);
}
__device__ __forceinline__ u32x4 lds128(unsigned a) { return *(LAS u32x4*)a; }
__device__ __forceinline__ f32x4 lds128f(unsigned a) { return *(LAS f32x4*)a; }
__device__ __forceinline__ f32x2 lds64f(unsigned a) { return *(LAS f32x2*)a; }
__device__ __forceinline__ u32x2 tr_read(unsigned a) { return __builtin_bit_cast(u32x2, __builtin_amdgcn_ds_read_tr16_b64_v4i16((LAS s16x4*)a)); }
__device__ __forceinline__ u32x4 tr_frag(unsigned a, int step) { const u32x2 x = tr_read(a), y = tr_read(a + step); return (u32x4){x.x, x.y, y.x, y.y}; }
__device__ __forceinline__ float swap16_lo(float v) {
    const unsigned a = f2u(v); auto r = __builtin_amdgcn_permlane16_swap(a, a, false, false); const unsigned x = r[0]; return u2f(x);
}
__device__ __forceinline__ float xmax16_32(float v) {
    unsigned a = f2u(v);
    auto r = __builtin_amdgcn_permlane16_swap(a, a, false, false);
    const unsigned r0 = r[0], r1 = r[1];
    v = __builtin_fmaxf(u2f(r0), u2f(r1));
    a = f2u(v);
    auto s = __builtin_amdgcn_permlane32_swap(a, a, false, false);
    const unsigned s0 = s[0], s1 = s[1];
    return __builtin_fmaxf(u2f(s0), u2f(s1));
}
__device__ __forceinline__ float bcast_row0(float v) { return __builtin_amdgcn_update_dpp(0.f, v, 0x150, 0xf, 0xf, false); }
__device__ __forceinline__ float diag4(f32x4 d, unsigned lane) { const float a = (lane & 1) ? d[1] : d[0], b = (lane & 1) ? d[3] : d[2]; return (lane & 2) ? b : a; }
__device__ __forceinline__ f32x2 ln_stats(float s, float q) {
    const float mean = s * (1.0f / DM);
    const float var = __builtin_fmaxf(q * (1.0f / DM) - mean * mean, 0.0f);
    const float sd = rbf(__builtin_amdgcn_sqrtf(rbf(rbf(var) + 1e-5f)));
    return (f32x2){rbf(mean), __builtin_amdgcn_rcpf(sd)};
}
__device__ __forceinline__ void wait_flags(gu64* base, unsigned n, u64 epoch, unsigned lane) {
    for (unsigned spins = 0; spins < kSpinCap; ++spins) {
        u64 v = epoch;
        if (lane < n) v = __hip_atomic_load(base + lane * 32, __ATOMIC_RELAXED, __HIP_MEMORY_SCOPE_AGENT);
        if (__all(v == epoch)) break;
    }
    __builtin_amdgcn_fence(__ATOMIC_ACQUIRE, "wavefront");
}

struct Params { const u16* x; const u16* wqkv; const u16* wo; const u16* wg; const u16* wu; const u16* wd; u16* out; unsigned char* ws; };

#define STAMP(i) do {} while (0)

__global__ void __launch_bounds__(512, 2) __attribute__((amdgpu_waves_per_eu(2, 2))) fused_block(Params P
) {
    const unsigned tid = threadIdx.x, lane = tid & 63;
    const unsigned w = __builtin_amdgcn_readfirstlane(tid >> 6);
    const unsigned b = blockIdx.x, xcd = b & 7, jj = b >> 3;
    const unsigned p = 4 * (xcd >> 1) + (jj >> 3);
    const unsigned hf = (jj >> 2) & 1, hg = 4 * (xcd & 1) + (jj & 3);
    const unsigned c = 8 * hf + hg;
    const unsigned r16 = lane & 15, g = lane >> 4, l8 = lane >> 3, l7 = lane & 7, q4 = r16 >> 2, p4 = lane & 3;
    STAMP(0);
    const unsigned wk = w * 1024;
    const rsrc_t rs_x = mk_rsrc(P.x, SEQ * DM * 2);
    const rsrc_t rs_w = mk_rsrc(P.wqkv, DM * 3 * DM * 2);
    unsigned char* const wsp = P.ws + (size_t)p * WS_BYTES;
    gu64* const flags = (gu64*)(wsp + WS_FLAG);
    const unsigned T0 = 128 * p + 64 * hf;
    STAMP(18);
    u32x4 ONES = {0x3F803F80u, 0x3F803F80u, 0x3F803F80u, 0x3F803F80u};
    PIN(ONES);
    const f32x4 Z4 = {0.f, 0.f, 0.f, 0.f};
    const unsigned fsw = ((q4 >> 1) & 1) | ((g & 1) << 1);
    const unsigned wsw = (l7 ^ (((g & 1) | ((w & 1) << 1)) << 1)) * 16;
    u64 gen = 0;
    const unsigned rt4 = w & 3, kh = w >> 2;
    const unsigned tok_off = (128 * p + 64 * hf + 32 * (rt4 & 1) + (rt4 >> 1) + 2 * r16) * 1024 + (64 * hg + 32 * kh + 16 * (g & 1) + 8 * (g >> 1)) * 2;
    u32x4 xr;

    {
        const unsigned rot = (2 * (jj & 3) + (jj >> 2) + (xcd >> 1)) & 7;
        const unsigned hh = w & 3, par = w >> 2;
        const unsigned vh = l8 * 2048 + ((l7 ^ g) * 16);
        const unsigned vxr = vh ^ ((w & 1) * 64);
        const unsigned sxr = (T0 + 32 * ((w >> 1) & 1) + (w >> 2) + 16 * (w & 1)) * 1024;
        const unsigned vw = (8 * w + l8) * 3072 + wsw + hg * 128;
        {
            const unsigned kap = (w + rot) & 7;
            const unsigned sh = (T0 == 0 ? 0 : T0 - 16) * 1024 + kap * 128;
            const unsigned hb = L1_H + w * 2048;
            if (lane >= 8) { DMA(hb, 0, vh, rs_x, sh, 0); const unsigned vh1 = vh ^ 64; DMA(hb, 1024, vh1, rs_x, sh, 1024); }
            else { *(LAS u32x4*)(hb + lane * 16) = ONES; *(LAS u32x4*)(hb + 1024 + lane * 16) = ONES; }
        }
        unsigned kx[8];
#pragma unroll
        for (int q = 0; q < 8; ++q) kx[q] = (q + rot) & 7;
#define ISSUE_X(q) DMA(wk, L1_X + (q) * 8192, vxr, rs_x, sxr + kx[q] * 128, 0)
#define ISSUE_W1(q, mat) DMA(wk, L1_W + ((q) % 3) * L1_WSLOT + (mat) * 8192, vw, rs_w, kx[q] * 196608, (mat) * 1024)
#define ISSUE_W(q) do { ISSUE_W1(q, 0); ISSUE_W1(q, 1); ISSUE_W1(q, 2); } while (0)
        ISSUE_X(0); ISSUE_W(0);
        __builtin_amdgcn_s_barrier();
        ISSUE_X(1); ISSUE_W(1);
        u64 genv;
        { const gu64* gp = flags + FL_GEN * 32; asm volatile("global_load_dwordx2 %0, %1, off sc1" : "=v"(genv) : "v"(gp) : "memory"); }
        {
#define TOUCH(ptr) asm volatile("s_mov_b32 m0, %0\n\ts_nop 0\n\tglobal_load_lds_dwordx4 %1, off" :: "s"(L1_SCR + 6144), "v"(ptr) : "memory")
            const char* t0 = (const char*)wsp + WS_SPARE + lane * 16;
            TOUCH(t0);
            const char* t1 = lane < 22 ? (const char*)P.wg + lane * 65536 : (lane < 44 ? (const char*)P.wu + (lane - 22) * 65536 : (const char*)P.wd + (lane - 44) * 65536);
            TOUCH(t1);
            const char* t2 = lane < 8 ? (const char*)P.wo + lane * 65536 : (lane < 32 ? (const char*)P.out + p * 131072 + (lane - 8) * 4096 : (const char*)P.wd + (20 + (lane & 1)) * 65536);
            TOUCH(t2);
#pragma unroll
            for (int i = 0; i < 3; ++i) {
                unsigned q = b * 24 + w * 3 + i; if (q >= 4640) q -= 4640;
                const char* base = q < 512 ? (const char*)P.wo + q * 1024 : (q < 1888 ? (const char*)P.wg + (q - 512) * 1024 : (q < 3264 ? (const char*)P.wu + (q - 1888) * 1024 : (const char*)P.wd + (q - 3264) * 1024));
                const char* t3 = base + lane * 16;
                TOUCH(t3);
            }
        }
#define NTOUCH 7
        STAMP(17);
        const unsigned xa = (r16 >> 3) * 1024 + (r16 & 7) * 128 + ((g ^ ((r16 >> 1) & 7)) * 16);
        const unsigned xc0 = xa + par * 4096, xc1 = xc0 ^ 64;
        unsigned xh0 = xa + L1_H, xh1 = xh0 ^ 64;
        PIN(xh0); PIN(xh1);
        unsigned wa = L1_W + (8 * g + q4) * 128 + ((hh ^ fsw) * 32) + 8 * p4, wa2 = wa + 2 * L1_WSLOT;
        PIN(wa); PIN(wa2);
        const unsigned sxa = (xa ^ ((hh & 1) * 64));
        unsigned stc = sxa + par * 4096 + (hh >> 1) * 8192, sth = sxa + L1_H + (hh >> 1) * 2048;
        PIN(stc); PIN(sth);
        f32x4 mh, mp, mc;
#pragma unroll
        for (int r = 0; r < 4; ++r) {
            const unsigned kap = 4 * g + r;
            mh[r] = (T0 > 0 && (kap >> 3) == par && (kap & 7) >= r16 + 1) ? 0.f : -1e30f;
            mp[r] = (kap >= r16 + 9) ? 0.f : -1e30f;
            mc[r] = (kap <= r16 && r16 - kap <= 7) ? 0.f : -1e30f;
        }
        PIN(mh); PIN(mp); PIN(mc);

        f32x4 aq[3] = {Z4, Z4, Z4}, ak[3] = {Z4, Z4, Z4}, av[3] = {Z4, Z4, Z4};
        f32x4 gr[3] = {Z4, Z4, Z4}, rs[3] = {Z4, Z4, Z4};
        f32x2 sL[3]; f32x4 sR[3][2];
        const unsigned part = L1_SCR, fin = L1_SCR + 2560 + w * 384;
        u32x4 xf[2][3], wf[2][3], sf[2][3];
        const rsrc_t rs_wo = mk_rsrc(P.wo, DM * DM * 2);
        const unsigned vwo = (8 * w + l8) * 1024 + wsw + hg * 128;
#define LOAD1(s) do { const int kq_ = (s) >> 1, kk_ = (s) & 1; \
            xf[(s) & 1][0] = lds128((kk_ ? xh1 : xh0) + kq_ * 2048); xf[(s) & 1][1] = lds128((kk_ ? xc1 : xc0) + kq_ * 8192); xf[(s) & 1][2] = lds128((kk_ ? xc1 : xc0) + kq_ * 8192 + 2048); \
            _Pragma("unroll") for (int mat = 0; mat < 3; ++mat) wf[(s) & 1][mat] = tr_frag((kq_ % 3 == 2 ? wa2 : wa + (kq_ % 3) * L1_WSLOT) + mat * 8192 + kk_ * 4096, 512); } while (0)
#define JOBS1(it) do { \
            if ((it) == 0) { ISSUE_X(2); ISSUE_W(2); } \
            if ((it) >= 1 && (it) <= 5) ISSUE_W((it) + 2 > 7 ? 7 : (it) + 2); \
            if ((it) == 1) { ISSUE_X(3); ISSUE_X(4); ISSUE_X(5); } \
            if ((it) == 2) { ISSUE_X(6); ISSUE_X(7); } \
            if ((it) == 6) { DMA(wk, L2_WO + 6 * 8192, vwo, rs_wo, 0u, 6 * 65536); DMA(wk, L2_WO + 7 * 8192, vwo, rs_wo, 0u, 7 * 65536); \
                             DMA(wk, 0, vwo, rs_wo, 0u, 3 * 65536); DMA(wk, 8192, vwo, rs_wo, 0u, 4 * 65536); DMA(wk, 16384, vwo, rs_wo, 0u, 5 * 65536); } \
            if ((it) == 7) { DMA(wk, L2_WO, vwo, rs_wo, 0u, 0); DMA(wk, L2_WO + 8192, vwo, rs_wo, 0u, 65536); DMA(wk, L2_WO + 2 * 8192, vwo, rs_wo, 0u, 2 * 65536); } \
            __builtin_amdgcn_sched_barrier(0); } while (0)
        WAIT_VM(4 + NTOUCH);
        BAR();
        STAMP(1);
        LOAD1(0);
#pragma unroll
        for (int s = 0; s < 16; ++s) {
            if (s & 1) {
                if (s == 1) WAIT_VM(4 + NTOUCH); else if (s == 3) WAIT_VM(6); else if (s == 5) WAIT_VM(7); else if (s == 7) WAIT_VM(5); else if (s < 13) WAIT_VM(3); else if (s == 13) WAIT_VM(6);
                if (s < 15) BAR();
                if (s == 9) { asm volatile("" : "+v"(genv)); gen = ((u64)(unsigned)__builtin_amdgcn_readfirstlane((unsigned)(genv >> 32)) << 32) | (unsigned)__builtin_amdgcn_readfirstlane((unsigned)genv); }
            }
            if ((s & 1) || s == 0) { if (w < 4) JOBS1((s + 1) / 2); }
            if (s < 15) LOAD1(s + 1);
            if (s >= 6 && s <= 9) { const int j = s - 6; sf[j & 1][0] = lds128(sth + j * 4096); sf[j & 1][1] = lds128(stc + j * 16384); sf[j & 1][2] = lds128(stc + j * 16384 + 2048); }
            f32x2 pp[4];
            if (s == 11) {
                const unsigned pa = part + ((g == 0 ? 0 : (g == 3 ? 0 : 2 * par + g)) * 64 + r16) * 8;
                pp[0] = lds64f(pa); pp[1] = lds64f(pa + 128); pp[2] = lds64f(pa + 256); pp[3] = lds64f(pa + 384);
            }
            if (s == 13) {
#pragma unroll
                for (int t = 0; t < 3; ++t) { sL[t] = lds64f(fin + (t * 16 + r16) * 8); sR[t][0] = lds128f(fin + (t * 16 + 4 * g) * 8); sR[t][1] = lds128f(fin + (t * 16 + 4 * g) * 8 + 16); }
            }
#pragma unroll
            for (int t = 0; t < 3; ++t) {
                aq[t] = mfma32(wf[s & 1][0], xf[s & 1][t], aq[t]); ak[t] = mfma32(wf[s & 1][1], xf[s & 1][t], ak[t]); av[t] = mfma32(xf[s & 1][t], wf[s & 1][2], av[t]);
            }
            if (s >= 7 && s <= 10) {
#pragma unroll
                for (int t = 0; t < 3; ++t) { gr[t] = mfma32(sf[(s - 7) & 1][t], sf[(s - 7) & 1][t], gr[t]); rs[t] = mfma32(ONES, sf[(s - 7) & 1][t], rs[t]); }
            }
#pragma unroll
            for (int i = 0; i < 9; ++i) { __builtin_amdgcn_sched_group_barrier(0x8, 1, 0); __builtin_amdgcn_sched_group_barrier(0x100, 1, 0); }
#pragma unroll
            for (int i = 0; i < 6; ++i) { __builtin_amdgcn_sched_group_barrier(0x8, 1, 0); __builtin_amdgcn_sched_group_barrier(0x100, 2, 0); }
            __builtin_amdgcn_sched_barrier(0);
            if (s == 10 && g == q4) {
                *(LAS f32x2*)(part + (hh * 16 + r16) * 8) = (f32x2){rs[0][0], diag4(gr[0], lane)};
                *(LAS f32x2*)(part + (((1 + 2 * par) * 4 + hh) * 16 + r16) * 8) = (f32x2){rs[1][0], diag4(gr[1], lane)};
                *(LAS f32x2*)(part + (((2 + 2 * par) * 4 + hh) * 16 + r16) * 8) = (f32x2){rs[2][0], diag4(gr[2], lane)};
            }
            if (s == 11) {
                const f32x2 sm = (pp[0] + pp[1]) + (pp[2] + pp[3]);
                const f32x2 res = ln_stats(sm.x, sm.y);
                if (g < 3) *(LAS f32x2*)(fin + (g * 16 + r16) * 8) = res;
            }
            __builtin_amdgcn_sched_barrier(0);
            if (!(s & 1)) { if (w >= 4) JOBS1(s / 2); }
            if (s == 12) asm volatile("buffer_load_dwordx4 %0, %1, %2, 0 offen" : "=v"(xr) : "v"(tok_off), "s"(rs_x) : "memory");
        }
        STAMP(2);
        f32x4 csq, csk;
#pragma unroll
        for (int r = 0; r < 4; ++r) { csq[r] = bcast_row0(aq[0][r]); csk[r] = bcast_row0(ak[0][r]); }
        const float csv = swap16_lo(av[0][0]);
        u32x2 qb[2], kb[3], vb[3];
#pragma unroll
        for (int t = 0; t < 3; ++t) {
            const f32x4 kv = (ak[t] - sL[t].x * csk) * sL[t].y;
            kb[t] = (u32x2){pk_bf16(kv[0], kv[1]), pk_bf16(kv[2], kv[3])};
            if (t > 0) { const f32x4 qv = (aq[t] - sL[t].x * csq) * sL[t].y; qb[t - 1] = (u32x2){pk_bf16(qv[0], qv[1]), pk_bf16(qv[2], qv[3])}; }
            const f32x4 mn = {sR[t][0][0], sR[t][0][2], sR[t][1][0], sR[t][1][2]}, rd = {sR[t][0][1], sR[t][0][3], sR[t][1][1], sR[t][1][3]};
            const f32x4 vv = (av[t] - mn * csv) * rd;
            vb[t] = (u32x2){pk_bf16(vv[0], vv[1]), pk_bf16(vv[2], vv[3])};
        }
        const float SC = 0.25f * 1.44269504088896f;
        u32x2 ob[2];
#pragma unroll
        for (int t = 0; t < 2; ++t) {
            f32x4 sp = mfma16(kb[t], qb[t], t == 0 ? mh : mp), sc = mfma16(kb[t + 1], qb[t], mc);
            float mx = __builtin_fmaxf(__builtin_fmaxf(__builtin_fmaxf(sp[0], sp[1]), __builtin_fmaxf(sp[2], sp[3])), __builtin_fmaxf(__builtin_fmaxf(sc[0], sc[1]), __builtin_fmaxf(sc[2], sc[3])));
            mx = xmax16_32(mx);
            const float mb = -mx * SC;
#pragma unroll
            for (int r = 0; r < 4; ++r) { sp[r] = __builtin_amdgcn_exp2f(sp[r] * SC + mb); sc[r] = __builtin_amdgcn_exp2f(sc[r] * SC + mb); }
            const u32x4 pf = {pk_bf16(sp[0], sp[1]), pk_bf16(sp[2], sp[3]), pk_bf16(sc[0], sc[1]), pk_bf16(sc[2], sc[3])};
            const u32x4 vf = {vb[t].x, vb[t].y, vb[t + 1].x, vb[t + 1].y};
            const f32x4 sm = mfma32(ONES, pf, Z4);
            const f32x4 o = mfma32(vf, pf, Z4) * __builtin_amdgcn_rcpf(sm[0]);
            ob[t] = (u32x2){pk_bf16(o[0], o[1]), pk_bf16(o[2], o[3])};
        }
        auto s0 = __builtin_amdgcn_permlane16_swap(ob[0].x, ob[1].x, false, false);
        auto s1 = __builtin_amdgcn_permlane16_swap(ob[0].y, ob[1].y, false, false);
        const u32x4 o_frag = {s0[0], s1[0], s0[1], s1[1]};
        const rsrc_t rs_ws1 = mk_rsrc(wsp, WS_BYTES);
        const unsigned off = ((g & 1) * 16384) + ((2 * (hh & 1) + (g >> 1)) * 16 + r16) * 16;
        const unsigned soff = WS_O + ((4 * hf + 2 * par) * 16 + 2 * hg + (hh >> 1)) * 1024;
        __builtin_amdgcn_raw_buffer_store_b128(o_frag, rs_ws1, off + soff, 0, AUX_SC1);
        asm volatile("" ::: "memory");
    }
    STAMP(3);
    const rsrc_t rs_ws = mk_rsrc(wsp, WS_BYTES);
    const u64 epoch = gen + 1;
    const unsigned n3 = w & 3, m3 = w >> 2;
    const rsrc_t rs_gu = mk_rsrc(m3 ? P.wu : P.wg, DM * FF * 2);
    unsigned vgu[3];
#pragma unroll
    for (int i = 0; i < 3; ++i) {
        const unsigned s = (n3 * 3 + i) * 64 + lane;
        const unsigned ks2 = s >= 384 ? 1 : 0, s2 = s - ks2 * 384, row = (s2 * 43691u) >> 19, pos = s2 - row * 12;
        int u = (int)pos - (int)(((row >> 3) & 1) + 2 * m3); if (u < 0) u += 12;
        u = min(u, (int)(c < 15 ? 171 - 12 * c : 0));
        vgu[i] = (ks2 * 32 + row) * (FF * 2) + c * 192 + u * 16;
    }
    unsigned mgu[3];
#pragma unroll
    for (int i = 0; i < 3; ++i) { const unsigned pi = n3 * 3 + i; mgu[i] = L3_B + (pi >= 6 ? L3_BSLOT + (pi - 6) * 1024 : pi * 1024) + m3 * 6144; }
    const unsigned rot3 = (2 * (jj >> 3) + (jj & 7)) & 7;
#define ISSUE_B(pr) do { const unsigned sb_ = (((pr) + rot3) & 7) * (64 * FF * 2); _Pragma("unroll") for (int i_ = 0; i_ < 3; ++i_) DMA(mgu[i_], ((pr) & 3) * 2 * L3_BSLOT, vgu[i_], rs_gu, sb_, 0); } while (0)

    u32x4 h_keep;
    {
        WAIT_VM(0);
        BAR();
        if (tid == 0) __hip_atomic_store(flags + (FL_1 + c) * 32, epoch, __ATOMIC_RELAXED, __HIP_MEMORY_SCOPE_AGENT);
        STAMP(4);
        asm volatile("" : "+v"(xr));
        const unsigned wl = (8 * g + q4) * 128 + (fsw * 32) + 8 * p4;
        const unsigned wb0 = wl + (kh ? 8192 : L2_WO), wb1 = wl + (kh ? L2_WO + 49152 : L2_WO + 16384), wb2 = wl + (kh ? L2_WO + 57344 : 0);
        unsigned abase = lane * 16;
        const unsigned asoff = WS_O + ((4 * hf + rt4) * 16 + 8 * kh) * 1024;
        PIN(abase);
        u32x4 bf[8][4];
#pragma unroll
        for (int k = 0; k < 8; ++k)
#pragma unroll
            for (int ct = 0; ct < 4; ++ct) bf[k][ct] = tr_frag(((k < 4 ? wb0 : (k < 6 ? wb1 : wb2)) ^ (ct * 32)) + (k < 4 ? k : (k < 6 ? k - 4 : k - 6)) * 4096, 512);
        __builtin_amdgcn_sched_barrier(0);
        STAMP(19);
        if (w == 0) wait_flags(flags + (FL_1 + 8 * hf) * 32, 8, epoch, lane);
        BAR();
        STAMP(5);
        u32x4 af[8];
#pragma unroll
        for (int k = 0; k < 4; ++k) af[k] = __builtin_amdgcn_raw_buffer_load_b128(rs_ws, abase + (k & 3) * 1024, asoff + (k >> 2) * 4096, AUX_SC1);
        __builtin_amdgcn_sched_barrier(0);
        f32x4 acc[4] = {Z4, Z4, Z4, Z4};
#pragma unroll
        for (int k = 0; k < 8; ++k) {
            if (k + 4 < 8) af[k + 4] = __builtin_amdgcn_raw_buffer_load_b128(rs_ws, abase + (k & 3) * 1024, asoff + 4096, AUX_SC1);
            __builtin_amdgcn_sched_barrier(0);
#pragma unroll
            for (int ct = 0; ct < 4; ++ct) acc[ct] = mfma32(bf[k][ct], af[k], acc[ct]);
            __builtin_amdgcn_sched_barrier(0);
        }
        STAMP(6);
        const unsigned red = L2_RED + w * 2048 + lane * 16;
        *(LAS f32x4*)red = kh ? acc[0] : acc[2];
        *(LAS f32x4*)(red + 1024) = kh ? acc[1] : acc[3];
        BAR();
        const f32x4 a0 = (kh ? acc[2] : acc[0]) + lds128f(red ^ 8192);
        const f32x4 a1 = (kh ? acc[3] : acc[1]) + lds128f((red ^ 8192) + 1024);
        auto s0 = __builtin_amdgcn_permlane16_swap(pk_bf16(a0[0], a0[1]), pk_bf16(a1[0], a1[1]), false, false);
        auto s1 = __builtin_amdgcn_permlane16_swap(pk_bf16(a0[2], a0[3]), pk_bf16(a1[2], a1[3]), false, false);
        const u32x4 at = {s0[0], s1[0], s0[1], s1[1]};
#pragma unroll
        for (int i = 0; i < 4; ++i) h_keep[i] = pk_bf16(bf_lo(at[i]) + bf_lo(xr[i]), bf_hi(at[i]) + bf_hi(xr[i]));
        const unsigned hoff = ((2 * (g & 1) + (g >> 1)) * 16 + r16) * 16;
        const unsigned hsoff = WS_H + ((4 * hf + rt4) * 16 + 2 * hg + kh) * 1024;
        __builtin_amdgcn_raw_buffer_store_b128(h_keep, rs_ws, hoff + hsoff, 0, AUX_SC1);
        const f32x4 gs = mfma32(h_keep, h_keep, Z4), sm = mfma32(ONES, h_keep, Z4);
        const u32x2 stv = {f2u(sm[0]), f2u(diag4(gs, lane))};
        const unsigned stoff = (g == q4) ? WS_ST + c * 1024 + w * 128 + r16 * 8 : 0xFFFFFFF0u;
        __builtin_amdgcn_raw_buffer_store_b64(stv, rs_ws, stoff, 0, AUX_SC1);
        asm volatile("" ::: "memory");
    }
    STAMP(7);
    const rsrc_t rs_wd = mk_rsrc(P.wd, FF * DM * 2);
    const unsigned vwd = l8 * 1024 + wsw + hg * 128;
    const unsigned swd = ((w >> 2) * 32 + (w & 3) * 8) * 1024;
#define ISSUE_D2(s0) DMA(wk, (s0) * 4096, vwd, rs_wd, swd, ((s0) < 20 ? (s0) + 2 : (s0) + 3) * 32768)
    {
        const bool active = c < 15;
        WAIT_VM(0);
        BAR();
        if (tid == 0) __hip_atomic_store(flags + (FL_2 + c) * 32, epoch, __ATOMIC_RELAXED, __HIP_MEMORY_SCOPE_AGENT);
        if (active && w != 0) { ISSUE_B(0); ISSUE_B(1); ISSUE_B(2); ISSUE_B(3); }
        STAMP(8);
        unsigned aa0 = L3_A + (4 * m3) * 1024 + lane * 16 + m3 * 8192, aa1 = aa0 ^ 8192;
        unsigned ba0[3], ba1[3];
#pragma unroll
        for (int i = 0; i < 3; ++i) {
            const unsigned mat = p4 >> 1;
            unsigned pos = 3 * n3 + i + (g & 1) + 2 * mat; if (pos >= 12) pos -= 12;
            const unsigned a = L3_B + mat * 6144 + ((8 * g + q4) * 12 + pos) * 16 + 8 * (p4 & 1);
            ba0[i] = a + m3 * L3_BSLOT; ba1[i] = a + (1 - m3) * L3_BSLOT;
            PIN(ba0[i]); PIN(ba1[i]);
        }
        unsigned vl16 = lane * 16;
        const unsigned sa3 = WS_H + w * 16384, sst = WS_ST + w * 2048;
        PIN(aa0); PIN(aa1); PIN(vl16);
        if (w == 0) wait_flags(flags + FL_2 * 32, 16, epoch, lane);
        BAR();
        STAMP(9);
        if (active && w == 0) { ISSUE_B(0); ISSUE_B(1); ISSUE_B(2); ISSUE_B(3); }
        if (tid == 0 && c == 0) __hip_atomic_store(flags + FL_GEN * 32, epoch, __ATOMIC_RELAXED, __HIP_MEMORY_SCOPE_AGENT);
        if (active) {
#define ISSUE_A(pr) do { const unsigned sa_ = sa3 + (((pr) + rot3) & 7) * 2048; DMA_SC1(wk, L3_A + ((pr) % 3) * 16384, vl16, rs_ws, sa_, 0); DMA_SC1(wk, L3_A + ((pr) % 3) * 16384 + 8192, vl16, rs_ws, sa_, 1024); } while (0)
            ISSUE_A(0); ISSUE_A(1); ISSUE_A(2);
            DMA_SC1(wk + wk, L3_ST, vl16, rs_ws, sst, 0); DMA_SC1(wk + wk, L3_ST + 1024, vl16, rs_ws, sst, 1024);
            f32x4 acc[4][3], cs[3] = {Z4, Z4, Z4};
#pragma unroll
            for (int t = 0; t < 4; ++t)
#pragma unroll
                for (int i = 0; i < 3; ++i) acc[t][i] = Z4;
            f32x2 st[4];
            u32x4 af[2][4], bf[2][3];
            unsigned sa = L3_ST + (tid >> 8) * 8192 + (tid & 3) * 2048 + ((tid >> 2) & 63) * 8, sf = L3_ST + 512 + m3 * 8192 + r16 * 8;
            PIN(sa); PIN(sf);
#define LOAD3(s) do { const int pr_ = (s) >> 1, k2_ = (s) & 1; \
                bf[k2_][0] = tr_frag((k2_ ? ba1[0] : ba0[0]) + (pr_ & 3) * 2 * L3_BSLOT, 4 * 12 * 16); \
                _Pragma("unroll") for (int t = 0; t < 4; ++t) af[k2_][t] = lds128((k2_ ? aa1 : aa0) + (pr_ % 3) * 16384 + t * 1024); \
                _Pragma("unroll") for (int i = 1; i < 3; ++i) bf[k2_][i] = tr_frag((k2_ ? ba1[i] : ba0[i]) + (pr_ & 3) * 2 * L3_BSLOT, 4 * 12 * 16); } while (0)
#define JOBS3(po) do { \
                if ((po) + 3 < 8) ISSUE_A((po) + 3 < 8 ? (po) + 3 : 7); \
                if ((po) + 4 < 8) ISSUE_B((po) + 4 < 8 ? (po) + 4 : 7); \
                if ((po) >= 4) { ISSUE_D2(12 + 6 * ((po) >= 4 ? (po) - 4 : 0)); ISSUE_D2(14 + 6 * ((po) >= 4 ? (po) - 4 : 0)); ISSUE_D2(16 + 6 * ((po) >= 4 ? (po) - 4 : 0)); } \
                __builtin_amdgcn_sched_barrier(0); } while (0)
            WAIT_VM(6);
            BAR();
            STAMP(10);
            LOAD3(0);
#pragma unroll
            for (int s = 0; s < 16; ++s) {
                const int pr = s >> 1;
                if ((s & 1) && s < 15) {
                    if (s == 1) WAIT_VM(4); else if (s == 3) WAIT_VM(7); else if (s < 13) WAIT_VM(8); else WAIT_VM(6);
                    BAR();
                }
                if ((s & 1) && s < 15) { if (w < 4) JOBS3(s >> 1); }
                if (s < 15) LOAD3(s + 1);
                f32x2 pp[4];
                if (s == 6) { pp[0] = lds64f(sa); pp[1] = lds64f(sa + 512); pp[2] = lds64f(sa + 1024); pp[3] = lds64f(sa + 1536); }
                if (s == 12) {
#pragma unroll
                    for (int t = 0; t < 4; ++t) st[t] = lds64f(sf + t * 128);
                }
#pragma unroll
                for (int i = 0; i < 3; ++i) {
#pragma unroll
                    for (int t = 0; t < 4; ++t) acc[t][i] = mfma32(bf[s & 1][i], af[s & 1][t], acc[t][i]);
                    if ((s & 1) == 0) cs[i] = mfma32(bf[s & 1][i], ONES, cs[i]);
                }
#pragma unroll
                for (int i = 0; i < 10; ++i) { __builtin_amdgcn_sched_group_barrier(0x8, 1, 0); __builtin_amdgcn_sched_group_barrier(0x100, 1, 0); }
#pragma unroll
                for (int i = 0; i < 5; ++i) { __builtin_amdgcn_sched_group_barrier(0x8, 1, 0); __builtin_amdgcn_sched_group_barrier(0x100, 1, 0); }
                __builtin_amdgcn_sched_barrier(0);
                if (s == 6) {
                    const f32x2 sm = (pp[0] + pp[1]) + (pp[2] + pp[3]);
                    float sx = sm.x, sq = sm.y;
                    sx += __builtin_amdgcn_update_dpp(0.f, sx, 0xB1, 0xf, 0xf, false); sq += __builtin_amdgcn_update_dpp(0.f, sq, 0xB1, 0xf, 0xf, false);
                    sx += __builtin_amdgcn_update_dpp(0.f, sx, 0x4E, 0xf, 0xf, false); sq += __builtin_amdgcn_update_dpp(0.f, sq, 0x4E, 0xf, 0xf, false);
                    if ((lane & 3) == 0) *(LAS f32x2*)(sa + 512) = ln_stats(sx, sq);
                }
                __builtin_amdgcn_sched_barrier(0);
                if (!(s & 1) && s >= 2) { if (w >= 4) JOBS3((s >> 1) - 1); }
            }
            STAMP(11);
            if (r16 == 0) {
#pragma unroll
                for (int i = 0; i < 3; ++i) *(LAS f32x4*)(L3_ST + 1024 + (3 * w + i) * 64 + g * 16) = cs[i];
            }
            BAR();
            DMA(wk, 30 * 4096, vwd, rs_wd, swd, 0);
            DMA(wk, 32 * 4096, vwd, rs_wd, swd + (kh ? 35 : 22) * 32768, 0);
            ISSUE_D2(0); ISSUE_D2(2); ISSUE_D2(4);
            const unsigned hi = lane >> 5;
            const float r01 = hi ? st[1].y : st[0].y, r23 = hi ? st[3].y : st[2].y;
            const float rr01 = r01 * r01, rr23 = r23 * r23;
            const unsigned tau = 2 * (g & 1) + (g >> 1);
#pragma unroll
            for (int i = 0; i < 3; ++i) {
                const f32x4 cv = cs[i] + lds128f(L3_ST + 1024 + (3 * (w ^ 4) + i) * 64 + g * 16);
                u32x2 hp[2];
#pragma unroll
                for (int pp = 0; pp < 2; ++pp) {
                    const f32x4 e0 = acc[2 * pp][i] - st[2 * pp].x * cv, e1 = acc[2 * pp + 1][i] - st[2 * pp + 1].x * cv;
                    float hv[4];
#pragma unroll
                    for (int r = 0; r < 4; ++r) {
                        auto sw = __builtin_amdgcn_permlane32_swap(f2u(e0[r]), f2u(e1[r]), false, false);
                        const unsigned x0 = sw[0], x1 = sw[1];
                        hv[r] = __builtin_fmaxf(u2f(x0), 0.0f) * (u2f(x1) * (pp ? rr23 : rr01));
                    }
                    hp[pp] = (u32x2){pk_bf16(hv[0], hv[1]), pk_bf16(hv[2], hv[3])};
                }
                auto s0 = __builtin_amdgcn_permlane16_swap(hp[0].x, hp[1].x, false, false);
                auto s1 = __builtin_amdgcn_permlane16_swap(hp[0].y, hp[1].y, false, false);
                const u32x4 hf4 = {s0[0], s1[0], s0[1], s1[1]};
                const unsigned U = 12 * c + 3 * n3 + i;
                const unsigned soff = WS_HID + ((4 * m3) * NKS4 + (U >> 2)) * 1024 + (U & 3) * 256;
                const unsigned off = U < 172 ? tau * (NKS4 * 1024) + r16 * 16 + soff : 0xFFFFFFF0u;
                __builtin_amdgcn_raw_buffer_store_b128(hf4, rs_ws, off, 0, AUX_SC1);
            }
            asm volatile("" ::: "memory");
        }
    }
    STAMP(12);
    {
        const bool active = c < 15;
        if (!active) {
            ISSUE_D2(12); ISSUE_D2(14); ISSUE_D2(16); ISSUE_D2(18); ISSUE_D2(20); ISSUE_D2(22); ISSUE_D2(24); ISSUE_D2(26); ISSUE_D2(28);
            DMA(wk, 30 * 4096, vwd, rs_wd, swd, 0);
            DMA(wk, 32 * 4096, vwd, rs_wd, swd + (kh ? 35 : 22) * 32768, 0);
            ISSUE_D2(0); ISSUE_D2(2); ISSUE_D2(4);
        }
        WAIT_VM(0);
        BAR();
        if (tid == 0 && active) __hip_atomic_store(flags + (FL_3 + c) * 32, epoch, __ATOMIC_RELAXED, __HIP_MEMORY_SCOPE_AGENT);
        STAMP(13);
#define REST_D() do { ISSUE_D2(6); ISSUE_D2(8); ISSUE_D2(10); ISSUE_D2(34); ISSUE_D2(36); ISSUE_D2(38); } while (0)
        if (w != 0) REST_D();
        const unsigned wb0 = (8 * g + q4) * 128 + (fsw * 32) + 8 * p4;
        u32x4 bq[22][4];
#pragma unroll
        for (int ct = 0; ct < 4; ++ct) {
            const u32x4 t0 = tr_frag((wb0 ^ (ct * 32)) + 30 * 4096, 512);
            bq[1][ct] = tr_frag((wb0 ^ (ct * 32)) + (kh ? 32 : 31) * 4096, 512);
            bq[0][ct] = kh ? (u32x4){0u, 0u, 0u, 0u} : t0;
        }
        BAR();
#define LAST_D() do { ISSUE_D2(30); DMA(wk, 32 * 4096, vwd, rs_wd, swd, 35 * 32768); } while (0)
        if (w != 0) LAST_D();
        unsigned wbk = wb0 + kh * (20 * 4096);
        unsigned abase = lane * 16;
        const unsigned asoff = WS_HID + ((4 * hf + rt4) * NKS4 + 21 * kh) * 1024;
        PIN(wbk); PIN(abase);
        if (w == 0) wait_flags(flags + FL_3 * 32, 15, epoch, lane);
        BAR();
        STAMP(14);
        u32x4 af[22];
#define LOADA4(k) af[k] = __builtin_amdgcn_raw_buffer_load_b128(rs_ws, abase + ((k) & 3) * 1024, asoff + ((k) >> 2) * 4096, AUX_SC1)
        if (w == 0) { REST_D(); LAST_D(); }
#pragma unroll
        for (int k = 0; k < 6; ++k) LOADA4(k);
        WAIT_VM(6);
        BAR();
#define LOAD4(j) do { _Pragma("unroll") for (int ct = 0; ct < 4; ++ct) bq[j][ct] = tr_frag((wbk ^ (ct * 32)) + ((j) - 2) * 4096, 512); } while (0)
        LOAD4(2); LOAD4(3); LOAD4(4); LOAD4(5);
        __builtin_amdgcn_sched_barrier(0);
        f32x4 acc[4] = {Z4, Z4, Z4, Z4};
#pragma unroll
        for (int j = 0; j < 22; ++j) {
            if (j + 6 < 22) { LOADA4(j + 6); LOAD4(j + 6); }
#pragma unroll
            for (int ct = 0; ct < 4; ++ct) acc[ct] = mfma32(bq[j][ct], af[j], acc[ct]);
#pragma unroll
            for (int i = 0; i < 4; ++i) { __builtin_amdgcn_sched_group_barrier(0x8, 1, 0); __builtin_amdgcn_sched_group_barrier(0x100, 2, 0); }
            __builtin_amdgcn_sched_barrier(0);
        }
        STAMP(15);
        BAR();
        const unsigned red = w * 2048 + lane * 16;
        *(LAS f32x4*)red = kh ? acc[0] : acc[2];
        *(LAS f32x4*)(red + 1024) = kh ? acc[1] : acc[3];
        BAR();
        const f32x4 a0 = (kh ? acc[2] : acc[0]) + lds128f(red ^ 8192);
        const f32x4 a1 = (kh ? acc[3] : acc[1]) + lds128f((red ^ 8192) + 1024);
        auto s0 = __builtin_amdgcn_permlane16_swap(pk_bf16(a0[0], a0[1]), pk_bf16(a1[0], a1[1]), false, false);
        auto s1 = __builtin_amdgcn_permlane16_swap(pk_bf16(a0[2], a0[3]), pk_bf16(a1[2], a1[3]), false, false);
        const u32x4 at = {s0[0], s1[0], s0[1], s1[1]};
        u32x4 o;
#pragma unroll
        for (int i = 0; i < 4; ++i) o[i] = pk_bf16(bf_lo(at[i]) + bf_lo(h_keep[i]), bf_hi(at[i]) + bf_hi(h_keep[i]));
        *(u32x4*)((char*)P.out + tok_off) = o;
    }
    STAMP(16);
}

}

extern "C" void kernel_launch(void* const* d_in, const int* in_sizes, int n_in, void* d_out, int out_size, void* d_ws, size_t ws_size, hipStream_t stream) {
    Params P;
    P.x = (const u16*)d_in[0]; P.wqkv = (const u16*)d_in[1]; P.wo = (const u16*)d_in[2];
    P.wg = (const u16*)d_in[4]; P.wu = (const u16*)d_in[5]; P.wd = (const u16*)d_in[6];
    P.out = (u16*)d_out; P.ws = (unsigned char*)d_ws;
    (void)hipFuncSetAttribute((const void*)fused_block, hipFuncAttributeMaxDynamicSharedMemorySize, LDS_BYTES);
    hipLaunchKernelGGL(fused_block, dim3(256), dim3(512), LDS_BYTES, stream, P);
}
```

```cpp
#include <hip/hip_runtime.h>
#include <hip/hip_bf16.h>
#include <stdint.h>
#pragma clang diagnostic ignored "-Wint-to-pointer-cast"

#define LAS __attribute__((address_space(3)))
#define GAS __attribute__((address_space(1)))
typedef unsigned short u16;
typedef unsigned long long u64;
typedef short s16x4 __attribute__((ext_vector_type(4)));
typedef __bf16 bf16x8 __attribute__((ext_vector_type(8)));
typedef __bf16 bf16x2 __attribute__((ext_vector_type(2)));
typedef float f32x4 __attribute__((ext_vector_type(4)));
typedef float f32x2 __attribute__((ext_vector_type(2)));
typedef unsigned u32x4 __attribute__((ext_vector_type(4)));
typedef unsigned u32x2 __attribute__((ext_vector_type(2)));
typedef GAS u64 gu64;
typedef __amdgpu_buffer_rsrc_t rsrc_t;

namespace {

constexpr int SEQ = 2048, DM = 512, FF = 1376;
constexpr int NKS4 = FF / 32;
constexpr unsigned WS_FLAG = 0;
constexpr unsigned WS_ST = 16384;
constexpr unsigned WS_O = 40960;
constexpr unsigned WS_H = WS_O + 131072;
constexpr unsigned WS_HID = WS_H + 131072;
constexpr unsigned WS_SPARE = WS_HID + 8 * 43 * 1024;
constexpr unsigned WS_BYTES = WS_SPARE + 8192;
constexpr int FL_GEN = 0, FL_1 = 8, FL_2 = 24, FL_3 = 40;
constexpr int AUX_SC1 = 16;
constexpr unsigned kSpinCap = 1u << 16;

constexpr int LDS_BYTES = 163840;
constexpr int L1_X = 0, L1_H = 65536, L1_W = 81920, L1_WSLOT = 24576, L1_SCR = 155648;
constexpr int L2_WO = 81920, L2_RED = 114688;
constexpr int L3_A = 0, L3_B = 49152, L3_BSLOT = 12288, L3_ST = 147456;

__device__ __forceinline__ constexpr int vm_imm(int n) { return (n & 0xF) | ((n >> 4) << 14) | (0x7 << 4) | (0xF << 8); }
#define WAIT_VM(n) asm volatile("s_waitcnt vmcnt(%0)" :: "i"(n) : "memory")
#define BAR() do { asm volatile("s_waitcnt lgkmcnt(0)" ::: "memory"); __builtin_amdgcn_s_barrier(); asm volatile("" ::: "memory"); } while (0)
#define PIN(x) asm volatile("" : "+v"(x))
#define DMA(m0b, ldsoff, voff, rs, sbase, soffimm) do { unsigned t_; asm volatile("s_add_u32 m0, %1, %2\n\ts_add_u32 %0, %5, %6\n\tbuffer_load_dwordx4 %3, %4, %0 offen lds" \
    : "=&s"(t_) : "s"(m0b), "i"(ldsoff), "v"(voff), "s"(rs), "s"(sbase), "i"(soffimm) : "scc", "memory"); } while (0)
#define DMA_SC1(m0b, ldsoff, voff, rs, sbase, soffimm) do { unsigned t_; asm volatile("s_add_u32 m0, %1, %2\n\ts_add_u32 %0, %5, %6\n\tbuffer_load_dwordx4 %3, %4, %0 offen sc1 lds" \
    : "=&s"(t_) : "s"(m0b), "i"(ldsoff), "v"(voff), "s"(rs), "s"(sbase), "i"(soffimm) : "scc", "memory"); } while (0)

__device__ __forceinline__ rsrc_t mk_rsrc(const void* p, unsigned bytes) { return __builtin_amdgcn_make_buffer_rsrc((void*)p, 0, bytes, 0x00020000); }
__device__ __forceinline__ float u2f(unsigned u) { return __builtin_bit_cast(float, u); }
__device__ __forceinline__ unsigned f2u(float f) { return __builtin_bit_cast(unsigned, f); }
__device__ __forceinline__ unsigned pk_bf16(float a, float b) { bf16x2 v; v[0] = (__bf16)a; v[1] = (__bf16)b; return __builtin_bit_cast(unsigned, v); }
__device__ __forceinline__ float bf_lo(unsigned u) { return u2f(u << 16); }
__device__ __forceinline__ float bf_hi(unsigned u) { return u2f(u & 0xffff0000u); }
__device__ __forceinline__ float rbf(float f) { return (float)(__bf16)f; }
__device__ __forceinline__ f32x4 mfma32(u32x4 a, u32x4 b, f32x4 c) {
    return __builtin_amdgcn_mfma_f32_16x16x32_bf16(__builtin_bit_cast(bf16x8, a), __builtin_bit_cast(bf16x8, b), c, 0, 0, 0);
}
__device__ __forceinline__ f32x4 mfma16(u32x2 a, u32x2 b, f32x4 c) {
    return __builtin_amdgcn_mfma_f32_16x16x16bf16_1k(__builtin_bit_cast(s16x4, a), __builtin_bit_cast(s16x4, b), c, 0, 0, 0);
}
__device__ __forceinline__ u32x4 lds128(unsigned a) { return *(LAS u32x4*)a; }
__device__ __forceinline__ f32x4 lds128f(unsigned a) { return *(LAS f32x4*)a; }
__device__ __forceinline__ f32x2 lds64f(unsigned a) { return *(LAS f32x2*)a; }
__device__ __forceinline__ u32x2 tr_read(unsigned a) { return __builtin_bit_cast(u32x2, __builtin_amdgcn_ds_read_tr16_b64_v4i16((LAS s16x4*)a)); }
__device__ __forceinline__ u32x4 tr_frag(unsigned a, int step) { const u32x2 x = tr_read(a), y = tr_read(a + step); return (u32x4){x.x, x.y, y.x, y.y}; }
__device__ __forceinline__ float swap16_lo(float v) {
    const unsigned a = f2u(v); auto r = __builtin_amdgcn_permlane16_swap(a, a, false, false); const unsigned x = r[0]; return u2f(x);
}
__device__ __forceinline__ float xmax16_32(float v) {
    unsigned a = f2u(v);
    auto r = __builtin_amdgcn_permlane16_swap(a, a, false, false);
    const unsigned r0 = r[0], r1 = r[1];
    v = __builtin_fmaxf(u2f(r0), u2f(r1));
    a = f2u(v);
    auto s = __builtin_amdgcn_permlane32_swap(a, a, false, false);
    const unsigned s0 = s[0], s1 = s[1];
    return __builtin_fmaxf(u2f(s0), u2f(s1));
}
__device__ __forceinline__ float bcast_row0(float v) { return __builtin_amdgcn_update_dpp(0.f, v, 0x150, 0xf, 0xf, false); }
__device__ __forceinline__ float diag4(f32x4 d, unsigned lane) { const float a = (lane & 1) ? d[1] : d[0], b = (lane & 1) ? d[3] : d[2]; return (lane & 2) ? b : a; }
__device__ __forceinline__ f32x2 ln_stats(float s, float q) {
    const float mean = s * (1.0f / DM);
    const float var = __builtin_fmaxf(q * (1.0f / DM) - mean * mean, 0.0f);
    const float sd = rbf(__builtin_amdgcn_sqrtf(rbf(rbf(var) + 1e-5f)));
    return (f32x2){rbf(mean), __builtin_amdgcn_rcpf(sd)};
}
__device__ __forceinline__ void wait_flags(gu64* base, unsigned n, u64 epoch, unsigned lane) {
    for (unsigned spins = 0; spins < kSpinCap; ++spins) {
        u64 v = epoch;
        if (lane < n) v = __hip_atomic_load(base + lane * 32, __ATOMIC_RELAXED, __HIP_MEMORY_SCOPE_AGENT);
        if (__all(v == epoch)) break;
    }
    __builtin_amdgcn_fence(__ATOMIC_ACQUIRE, "wavefront");
}

struct Params { const u16* x; const u16* wqkv; const u16* wo; const u16* wg; const u16* wu; const u16* wd; u16* out; unsigned char* ws; };

#define STAMP(i) do {} while (0)

__global__ void __launch_bounds__(512, 2) __attribute__((amdgpu_waves_per_eu(2, 2))) fused_block(Params P
) {
    const unsigned tid = threadIdx.x, lane = tid & 63;
    const unsigned w = __builtin_amdgcn_readfirstlane(tid >> 6);
    const unsigned b = blockIdx.x, xcd = b & 7, jj = b >> 3;
    const unsigned p = 4 * (xcd >> 1) + (jj >> 3);
    const unsigned hf = (jj >> 2) & 1, hg = 4 * (xcd & 1) + (jj & 3);
    const unsigned c = 8 * hf + hg;
    const unsigned r16 = lane & 15, g = lane >> 4, l8 = lane >> 3, l7 = lane & 7, q4 = r16 >> 2, p4 = lane & 3;
    STAMP(0);
    const unsigned wk = w * 1024;
    const rsrc_t rs_x = mk_rsrc(P.x, SEQ * DM * 2);
    const rsrc_t rs_w = mk_rsrc(P.wqkv, DM * 3 * DM * 2);
    unsigned char* const wsp = P.ws + (size_t)p * WS_BYTES;
    gu64* const flags = (gu64*)(wsp + WS_FLAG);
    const unsigned T0 = 128 * p + 64 * hf;
    STAMP(18);
    u32x4 ONES = {0x3F803F80u, 0x3F803F80u, 0x3F803F80u, 0x3F803F80u};
    PIN(ONES);
    const f32x4 Z4 = {0.f, 0.f, 0.f, 0.f};
    const unsigned fsw = ((q4 >> 1) & 1) | ((g & 1) << 1);
    const unsigned wsw = (l7 ^ (((g & 1) | ((w & 1) << 1)) << 1)) * 16;
    u64 gen = 0;
    const unsigned rt4 = w & 3, kh = w >> 2;
    const unsigned tok_off = (128 * p + 64 * hf + 32 * (rt4 & 1) + (rt4 >> 1) + 2 * r16) * 1024 + (64 * hg + 32 * kh + 16 * (g & 1) + 8 * (g >> 1)) * 2;
    u32x4 xr;

    {
        const unsigned rot = (2 * (jj & 3) + (jj >> 2) + (xcd >> 1)) & 7;
        const unsigned hh = w & 3, par = w >> 2;
        const unsigned vh = l8 * 2048 + ((l7 ^ g) * 16);
        const unsigned vxr = vh ^ ((w & 1) * 64);
        const unsigned sxr = (T0 + 32 * ((w >> 1) & 1) + (w >> 2) + 16 * (w & 1)) * 1024;
        const unsigned vw = (8 * w + l8) * 3072 + wsw + hg * 128;
        {
            const unsigned kap = (w + rot) & 7;
            const unsigned sh = (T0 == 0 ? 0 : T0 - 16) * 1024 + kap * 128;
            const unsigned hb = L1_H + w * 2048;
            if (lane >= 8) { DMA(hb, 0, vh, rs_x, sh, 0); const unsigned vh1 = vh ^ 64; DMA(hb, 1024, vh1, rs_x, sh, 1024); }
            else { *(LAS u32x4*)(hb + lane * 16) = ONES; *(LAS u32x4*)(hb + 1024 + lane * 16) = ONES; }
        }
        unsigned kx[8];
#pragma unroll
        for (int q = 0; q < 8; ++q) kx[q] = (q + rot) & 7;
#define TOUCH(ptr) asm volatile("s_mov_b32 m0, %0\n\ts_nop 0\n\tglobal_load_lds_dwordx4 %1, off" :: "s"(L1_SCR + 6144), "v"(ptr) : "memory")
#define ISSUE_X(q) DMA(wk, L1_X + (q) * 8192, vxr, rs_x, sxr + kx[q] * 128, 0)
#define ISSUE_W1(q, mat) DMA(wk, L1_W + ((q) % 3) * L1_WSLOT + (mat) * 8192, vw, rs_w, kx[q] * 196608, (mat) * 1024)
#define ISSUE_W(q) do { ISSUE_W1(q, 0); ISSUE_W1(q, 1); ISSUE_W1(q, 2); } while (0)
        ISSUE_X(0); ISSUE_W(0);
        __builtin_amdgcn_s_barrier();
        ISSUE_X(1); ISSUE_W(1);
        u64 genv;
        { const gu64* gp = flags + FL_GEN * 32; asm volatile("global_load_dwordx2 %0, %1, off sc1" : "=v"(genv) : "v"(gp) : "memory"); }
#define NTOUCH 4
        STAMP(17);
        const unsigned xa = (r16 >> 3) * 1024 + (r16 & 7) * 128 + ((g ^ ((r16 >> 1) & 7)) * 16);
        const unsigned xc0 = xa + par * 4096, xc1 = xc0 ^ 64;
        unsigned xh0 = xa + L1_H, xh1 = xh0 ^ 64;
        PIN(xh0); PIN(xh1);
        unsigned wa = L1_W + (8 * g + q4) * 128 + ((hh ^ fsw) * 32) + 8 * p4, wa2 = wa + 2 * L1_WSLOT;
        PIN(wa); PIN(wa2);
        const unsigned sxa = (xa ^ ((hh & 1) * 64));
        unsigned stc = sxa + par * 4096 + (hh >> 1) * 8192, sth = sxa + L1_H + (hh >> 1) * 2048;
        PIN(stc); PIN(sth);
        f32x4 mh, mp, mc;
#pragma unroll
        for (int r = 0; r < 4; ++r) {
            const unsigned kap = 4 * g + r;
            mh[r] = (T0 > 0 && (kap >> 3) == par && (kap & 7) >= r16 + 1) ? 0.f : -1e30f;
            mp[r] = (kap >= r16 + 9) ? 0.f : -1e30f;
            mc[r] = (kap <= r16 && r16 - kap <= 7) ? 0.f : -1e30f;
        }
        PIN(mh); PIN(mp); PIN(mc);

        f32x4 aq[3] = {Z4, Z4, Z4}, ak[3] = {Z4, Z4, Z4}, av[3] = {Z4, Z4, Z4};
        f32x4 gr[3] = {Z4, Z4, Z4}, rs[3] = {Z4, Z4, Z4};
        f32x2 sL[3]; f32x4 sR[3][2];
        const unsigned part = L1_SCR, fin = L1_SCR + 2560 + w * 384;
        u32x4 xf[2][3], wf[2][3], sf[2][3];
        const rsrc_t rs_wo = mk_rsrc(P.wo, DM * DM * 2);
        const unsigned vwo = (8 * w + l8) * 1024 + wsw + hg * 128;
#define LOAD1(s) do { const int kq_ = (s) >> 1, kk_ = (s) & 1; \
            xf[(s) & 1][0] = lds128((kk_ ? xh1 : xh0) + kq_ * 2048); xf[(s) & 1][1] = lds128((kk_ ? xc1 : xc0) + kq_ * 8192); xf[(s) & 1][2] = lds128((kk_ ? xc1 : xc0) + kq_ * 8192 + 2048); \
            _Pragma("unroll") for (int mat = 0; mat < 3; ++mat) wf[(s) & 1][mat] = tr_frag((kq_ % 3 == 2 ? wa2 : wa + (kq_ % 3) * L1_WSLOT) + mat * 8192 + kk_ * 4096, 512); } while (0)
#define JOBS1(it) do { \
            if ((it) == 0) { ISSUE_X(2); ISSUE_W(2); } \
            if ((it) >= 1 && (it) <= 5) ISSUE_W((it) + 2 > 7 ? 7 : (it) + 2); \
            if ((it) == 1) { ISSUE_X(3); ISSUE_X(4); ISSUE_X(5); } \
            if ((it) == 2) { ISSUE_X(6); ISSUE_X(7); } \
            if ((it) == 6) { DMA(wk, L2_WO + 6 * 8192, vwo, rs_wo, 0u, 6 * 65536); DMA(wk, L2_WO + 7 * 8192, vwo, rs_wo, 0u, 7 * 65536); \
                             DMA(wk, 0, vwo, rs_wo, 0u, 3 * 65536); DMA(wk, 8192, vwo, rs_wo, 0u, 4 * 65536); DMA(wk, 16384, vwo, rs_wo, 0u, 5 * 65536); } \
            if ((it) == 7) { DMA(wk, L2_WO, vwo, rs_wo, 0u, 0); DMA(wk, L2_WO + 8192, vwo, rs_wo, 0u, 65536); DMA(wk, L2_WO + 2 * 8192, vwo, rs_wo, 0u, 2 * 65536); } \
            __builtin_amdgcn_sched_barrier(0); } while (0)
        WAIT_VM(5);
        BAR();
        STAMP(1);
        LOAD1(0);
#pragma unroll
        for (int s = 0; s < 16; ++s) {
            if (s & 1) {
                if (s == 1) WAIT_VM(5 + NTOUCH); else if (s == 3) WAIT_VM(6 + NTOUCH); else if (s == 5) WAIT_VM(7); else if (s == 7) WAIT_VM(5); else if (s < 13) WAIT_VM(3); else if (s == 13) WAIT_VM(6);
                if (s < 15) BAR();
                if (s == 9) { asm volatile("" : "+v"(genv)); gen = ((u64)(unsigned)__builtin_amdgcn_readfirstlane((unsigned)(genv >> 32)) << 32) | (unsigned)__builtin_amdgcn_readfirstlane((unsigned)genv); }
            }
            if ((s & 1) || s == 0) { if (w < 4) JOBS1((s + 1) / 2); }
            if (s < 15) LOAD1(s + 1);
            if (s >= 6 && s <= 9) { const int j = s - 6; sf[j & 1][0] = lds128(sth + j * 4096); sf[j & 1][1] = lds128(stc + j * 16384); sf[j & 1][2] = lds128(stc + j * 16384 + 2048); }
            f32x2 pp[4];
            if (s == 11) {
                const unsigned pa = part + ((g == 0 ? 0 : (g == 3 ? 0 : 2 * par + g)) * 64 + r16) * 8;
                pp[0] = lds64f(pa); pp[1] = lds64f(pa + 128); pp[2] = lds64f(pa + 256); pp[3] = lds64f(pa + 384);
            }
            if (s == 13) {
#pragma unroll
                for (int t = 0; t < 3; ++t) { sL[t] = lds64f(fin + (t * 16 + r16) * 8); sR[t][0] = lds128f(fin + (t * 16 + 4 * g) * 8); sR[t][1] = lds128f(fin + (t * 16 + 4 * g) * 8 + 16); }
            }
#pragma unroll
            for (int t = 0; t < 3; ++t) {
                aq[t] = mfma32(wf[s & 1][0], xf[s & 1][t], aq[t]); ak[t] = mfma32(wf[s & 1][1], xf[s & 1][t], ak[t]); av[t] = mfma32(xf[s & 1][t], wf[s & 1][2], av[t]);
            }
            if (s >= 7 && s <= 10) {
#pragma unroll
                for (int t = 0; t < 3; ++t) { gr[t] = mfma32(sf[(s - 7) & 1][t], sf[(s - 7) & 1][t], gr[t]); rs[t] = mfma32(ONES, sf[(s - 7) & 1][t], rs[t]); }
            }
#pragma unroll
            for (int i = 0; i < 9; ++i) { __builtin_amdgcn_sched_group_barrier(0x8, 1, 0); __builtin_amdgcn_sched_group_barrier(0x100, 1, 0); }
#pragma unroll
            for (int i = 0; i < 6; ++i) { __builtin_amdgcn_sched_group_barrier(0x8, 1, 0); __builtin_amdgcn_sched_group_barrier(0x100, 2, 0); }
            __builtin_amdgcn_sched_barrier(0);
            if (s == 10 && g == q4) {
                *(LAS f32x2*)(part + (hh * 16 + r16) * 8) = (f32x2){rs[0][0], diag4(gr[0], lane)};
                *(LAS f32x2*)(part + (((1 + 2 * par) * 4 + hh) * 16 + r16) * 8) = (f32x2){rs[1][0], diag4(gr[1], lane)};
                *(LAS f32x2*)(part + (((2 + 2 * par) * 4 + hh) * 16 + r16) * 8) = (f32x2){rs[2][0], diag4(gr[2], lane)};
            }
            if (s == 11) {
                const f32x2 sm = (pp[0] + pp[1]) + (pp[2] + pp[3]);
                const f32x2 res = ln_stats(sm.x, sm.y);
                if (g < 3) *(LAS f32x2*)(fin + (g * 16 + r16) * 8) = res;
            }
            __builtin_amdgcn_sched_barrier(0);
            if (!(s & 1)) { if (w >= 4) JOBS1(s / 2); }
            if (s == 0) {
            const char* t1 = lane < 22 ? (const char*)P.wg + lane * 65536 : (lane < 44 ? (const char*)P.wu + (lane - 22) * 65536 : (const char*)P.wd + (lane - 44) * 65536);
            const char* t2 = lane < 8 ? (const char*)P.wo + lane * 65536 : (lane < 32 ? (const char*)P.out + p * 131072 + (lane - 8) * 4096 : (const char*)P.wd + (20 + (lane & 1)) * 65536);
            const char* tw = w == 0 ? (const char*)wsp + WS_FLAG + lane * 256 : (w == 1 ? t1 : (w == 2 ? t2 : (const char*)wsp + ((w & 1) ? WS_SPARE : 32768u) + lane * 16));
            TOUCH(tw);
#pragma unroll
            for (int i = 0; i < 3; ++i) {
                unsigned q = b * 24 + w * 3 + i; if (q >= 4640) q -= 4640;
                const char* base = q < 512 ? (const char*)P.wo + q * 1024 : (q < 1888 ? (const char*)P.wg + (q - 512) * 1024 : (q < 3264 ? (const char*)P.wu + (q - 1888) * 1024 : (const char*)P.wd + (q - 3264) * 1024));
                const char* t3 = base + lane * 16;
                TOUCH(t3);
            }
        }
            if (s == 12) asm volatile("buffer_load_dwordx4 %0, %1, %2, 0 offen" : "=v"(xr) : "v"(tok_off), "s"(rs_x) : "memory");
        }
        STAMP(2);
        f32x4 csq, csk;
#pragma unroll
        for (int r = 0; r < 4; ++r) { csq[r] = bcast_row0(aq[0][r]); csk[r] = bcast_row0(ak[0][r]); }
        const float csv = swap16_lo(av[0][0]);
        u32x2 qb[2], kb[3], vb[3];
#pragma unroll
        for (int t = 0; t < 3; ++t) {
            const f32x4 kv = (ak[t] - sL[t].x * csk) * sL[t].y;
            kb[t] = (u32x2){pk_bf16(kv[0], kv[1]), pk_bf16(kv[2], kv[3])};
            if (t > 0) { const f32x4 qv = (aq[t] - sL[t].x * csq) * sL[t].y; qb[t - 1] = (u32x2){pk_bf16(qv[0], qv[1]), pk_bf16(qv[2], qv[3])}; }
            const f32x4 mn = {sR[t][0][0], sR[t][0][2], sR[t][1][0], sR[t][1][2]}, rd = {sR[t][0][1], sR[t][0][3], sR[t][1][1], sR[t][1][3]};
            const f32x4 vv = (av[t] - mn * csv) * rd;
            vb[t] = (u32x2){pk_bf16(vv[0], vv[1]), pk_bf16(vv[2], vv[3])};
        }
        const float SC = 0.25f * 1.44269504088896f;
        u32x2 ob[2];
#pragma unroll
        for (int t = 0; t < 2; ++t) {
            f32x4 sp = mfma16(kb[t], qb[t], t == 0 ? mh : mp), sc = mfma16(kb[t + 1], qb[t], mc);
            float mx = __builtin_fmaxf(__builtin_fmaxf(__builtin_fmaxf(sp[0], sp[1]), __builtin_fmaxf(sp[2], sp[3])), __builtin_fmaxf(__builtin_fmaxf(sc[0], sc[1]), __builtin_fmaxf(sc[2], sc[3])));
            mx = xmax16_32(mx);
            const float mb = -mx * SC;
#pragma unroll
            for (int r = 0; r < 4; ++r) { sp[r] = __builtin_amdgcn_exp2f(sp[r] * SC + mb); sc[r] = __builtin_amdgcn_exp2f(sc[r] * SC + mb); }
            const u32x4 pf = {pk_bf16(sp[0], sp[1]), pk_bf16(sp[2], sp[3]), pk_bf16(sc[0], sc[1]), pk_bf16(sc[2], sc[3])};
            const u32x4 vf = {vb[t].x, vb[t].y, vb[t + 1].x, vb[t + 1].y};
            const f32x4 sm = mfma32(ONES, pf, Z4);
            const f32x4 o = mfma32(vf, pf, Z4) * __builtin_amdgcn_rcpf(sm[0]);
            ob[t] = (u32x2){pk_bf16(o[0], o[1]), pk_bf16(o[2], o[3])};
        }
        auto s0 = __builtin_amdgcn_permlane16_swap(ob[0].x, ob[1].x, false, false);
        auto s1 = __builtin_amdgcn_permlane16_swap(ob[0].y, ob[1].y, false, false);
        const u32x4 o_frag = {s0[0], s1[0], s0[1], s1[1]};
        const rsrc_t rs_ws1 = mk_rsrc(wsp, WS_BYTES);
        const unsigned off = ((g & 1) * 16384) + ((2 * (hh & 1) + (g >> 1)) * 16 + r16) * 16;
        const unsigned soff = WS_O + ((4 * hf + 2 * par) * 16 + 2 * hg + (hh >> 1)) * 1024;
        __builtin_amdgcn_raw_buffer_store_b128(o_frag, rs_ws1, off + soff, 0, AUX_SC1);
        asm volatile("" ::: "memory");
    }
    STAMP(3);
    const rsrc_t rs_ws = mk_rsrc(wsp, WS_BYTES);
    const u64 epoch = gen + 1;
    const unsigned n3 = w & 3, m3 = w >> 2;
    const rsrc_t rs_gu = mk_rsrc(m3 ? P.wu : P.wg, DM * FF * 2);
    unsigned vgu[3];
#pragma unroll
    for (int i = 0; i < 3; ++i) {
        const unsigned s = (n3 * 3 + i) * 64 + lane;
        const unsigned ks2 = s >= 384 ? 1 : 0, s2 = s - ks2 * 384, row = (s2 * 43691u) >> 19, pos = s2 - row * 12;
        int u = (int)pos - (int)(((row >> 3) & 1) + 2 * m3); if (u < 0) u += 12;
        u = min(u, (int)(c < 15 ? 171 - 12 * c : 0));
        vgu[i] = (ks2 * 32 + row) * (FF * 2) + c * 192 + u * 16;
    }
    unsigned mgu[3];
#pragma unroll
    for (int i = 0; i < 3; ++i) { const unsigned pi = n3 * 3 + i; mgu[i] = L3_B + (pi >= 6 ? L3_BSLOT + (pi - 6) * 1024 : pi * 1024) + m3 * 6144; }
    const unsigned rot3 = (2 * (jj >> 3) + (jj & 7)) & 7;
#define ISSUE_B(pr) do { const unsigned sb_ = (((pr) + rot3) & 7) * (64 * FF * 2); _Pragma("unroll") for (int i_ = 0; i_ < 3; ++i_) DMA(mgu[i_], ((pr) & 3) * 2 * L3_BSLOT, vgu[i_], rs_gu, sb_, 0); } while (0)

    u32x4 h_keep;
    {
        WAIT_VM(0);
        BAR();
        if (tid == 0) __hip_atomic_store(flags + (FL_1 + c) * 32, epoch, __ATOMIC_RELAXED, __HIP_MEMORY_SCOPE_AGENT);
        STAMP(4);
        asm volatile("" : "+v"(xr));
        const unsigned wl = (8 * g + q4) * 128 + (fsw * 32) + 8 * p4;
        const unsigned wb0 = wl + (kh ? 8192 : L2_WO), wb1 = wl + (kh ? L2_WO + 49152 : L2_WO + 16384), wb2 = wl + (kh ? L2_WO + 57344 : 0);
        unsigned abase = lane * 16;
        const unsigned asoff = WS_O + ((4 * hf + rt4) * 16 + 8 * kh) * 1024;
        PIN(abase);
        u32x4 bf[8][4];
#pragma unroll
        for (int k = 0; k < 8; ++k)
#pragma unroll
            for (int ct = 0; ct < 4; ++ct) bf[k][ct] = tr_frag(((k < 4 ? wb0 : (k < 6 ? wb1 : wb2)) ^ (ct * 32)) + (k < 4 ? k : (k < 6 ? k - 4 : k - 6)) * 4096, 512);
        __builtin_amdgcn_sched_barrier(0);
        STAMP(19);
        if (w == 0) wait_flags(flags + (FL_1 + 8 * hf) * 32, 8, epoch, lane);
        BAR();
        STAMP(5);
        u32x4 af[8];
#pragma unroll
        for (int k = 0; k < 4; ++k) af[k] = __builtin_amdgcn_raw_buffer_load_b128(rs_ws, abase + (k & 3) * 1024, asoff + (k >> 2) * 4096, AUX_SC1);
        __builtin_amdgcn_sched_barrier(0);
        f32x4 acc[4] = {Z4, Z4, Z4, Z4};
#pragma unroll
        for (int k = 0; k < 8; ++k) {
            if (k + 4 < 8) af[k + 4] = __builtin_amdgcn_raw_buffer_load_b128(rs_ws, abase + (k & 3) * 1024, asoff + 4096, AUX_SC1);
            __builtin_amdgcn_sched_barrier(0);
#pragma unroll
            for (int ct = 0; ct < 4; ++ct) acc[ct] = mfma32(bf[k][ct], af[k], acc[ct]);
            __builtin_amdgcn_sched_barrier(0);
        }
        STAMP(6);
        const unsigned red = L2_RED + w * 2048 + lane * 16;
        *(LAS f32x4*)red = kh ? acc[0] : acc[2];
        *(LAS f32x4*)(red + 1024) = kh ? acc[1] : acc[3];
        BAR();
        const f32x4 a0 = (kh ? acc[2] : acc[0]) + lds128f(red ^ 8192);
        const f32x4 a1 = (kh ? acc[3] : acc[1]) + lds128f((red ^ 8192) + 1024);
        auto s0 = __builtin_amdgcn_permlane16_swap(pk_bf16(a0[0], a0[1]), pk_bf16(a1[0], a1[1]), false, false);
        auto s1 = __builtin_amdgcn_permlane16_swap(pk_bf16(a0[2], a0[3]), pk_bf16(a1[2], a1[3]), false, false);
        const u32x4 at = {s0[0], s1[0], s0[1], s1[1]};
#pragma unroll
        for (int i = 0; i < 4; ++i) h_keep[i] = pk_bf16(bf_lo(at[i]) + bf_lo(xr[i]), bf_hi(at[i]) + bf_hi(xr[i]));
        const unsigned hoff = ((2 * (g & 1) + (g >> 1)) * 16 + r16) * 16;
        const unsigned hsoff = WS_H + ((4 * hf + rt4) * 16 + 2 * hg + kh) * 1024;
        __builtin_amdgcn_raw_buffer_store_b128(h_keep, rs_ws, hoff + hsoff, 0, AUX_SC1);
        const f32x4 gs = mfma32(h_keep, h_keep, Z4), sm = mfma32(ONES, h_keep, Z4);
        const u32x2 stv = {f2u(sm[0]), f2u(diag4(gs, lane))};
        const unsigned stoff = (g == q4) ? WS_ST + c * 1024 + w * 128 + r16 * 8 : 0xFFFFFFF0u;
        __builtin_amdgcn_raw_buffer_store_b64(stv, rs_ws, stoff, 0, AUX_SC1);
        asm volatile("" ::: "memory");
    }
    STAMP(7);
    const rsrc_t rs_wd = mk_rsrc(P.wd, FF * DM * 2);
    const unsigned vwd = l8 * 1024 + wsw + hg * 128;
    const unsigned swd = ((w >> 2) * 32 + (w & 3) * 8) * 1024;
#define ISSUE_D2(s0) DMA(wk, (s0) * 4096, vwd, rs_wd, swd, ((s0) < 20 ? (s0) + 2 : (s0) + 3) * 32768)
    {
        const bool active = c < 15;
        WAIT_VM(0);
        BAR();
        if (tid == 0) __hip_atomic_store(flags + (FL_2 + c) * 32, epoch, __ATOMIC_RELAXED, __HIP_MEMORY_SCOPE_AGENT);
        if (active && w != 0) { ISSUE_B(0); ISSUE_B(1); ISSUE_B(2); ISSUE_B(3); }
        STAMP(8);
        unsigned aa0 = L3_A + (4 * m3) * 1024 + lane * 16 + m3 * 8192, aa1 = aa0 ^ 8192;
        unsigned ba0[3], ba1[3];
#pragma unroll
        for (int i = 0; i < 3; ++i) {
            const unsigned mat = p4 >> 1;
            unsigned pos = 3 * n3 + i + (g & 1) + 2 * mat; if (pos >= 12) pos -= 12;
            const unsigned a = L3_B + mat * 6144 + ((8 * g + q4) * 12 + pos) * 16 + 8 * (p4 & 1);
            ba0[i] = a + m3 * L3_BSLOT; ba1[i] = a + (1 - m3) * L3_BSLOT;
            PIN(ba0[i]); PIN(ba1[i]);
        }
        unsigned vl16 = lane * 16;
        const unsigned sa3 = WS_H + w * 16384, sst = WS_ST + w * 2048;
        PIN(aa0); PIN(aa1); PIN(vl16);
        if (w == 0) wait_flags(flags + FL_2 * 32, 16, epoch, lane);
        BAR();
        STAMP(9);
        if (active && w == 0) { ISSUE_B(0); ISSUE_B(1); ISSUE_B(2); ISSUE_B(3); }
        if (tid == 0 && c == 0) __hip_atomic_store(flags + FL_GEN * 32, epoch, __ATOMIC_RELAXED, __HIP_MEMORY_SCOPE_AGENT);
        if (active) {
#define ISSUE_A(pr) do { const unsigned sa_ = sa3 + (((pr) + rot3) & 7) * 2048; DMA_SC1(wk, L3_A + ((pr) % 3) * 16384, vl16, rs_ws, sa_, 0); DMA_SC1(wk, L3_A + ((pr) % 3) * 16384 + 8192, vl16, rs_ws, sa_, 1024); } while (0)
            ISSUE_A(0); ISSUE_A(1); ISSUE_A(2);
            DMA_SC1(wk + wk, L3_ST, vl16, rs_ws, sst, 0); DMA_SC1(wk + wk, L3_ST + 1024, vl16, rs_ws, sst, 1024);
            f32x4 acc[4][3], cs[3] = {Z4, Z4, Z4};
#pragma unroll
            for (int t = 0; t < 4; ++t)
#pragma unroll
                for (int i = 0; i < 3; ++i) acc[t][i] = Z4;
            f32x2 st[4];
            u32x4 af[2][4], bf[2][3];
            unsigned sa = L3_ST + (tid >> 8) * 8192 + (tid & 3) * 2048 + ((tid >> 2) & 63) * 8, sf = L3_ST + 512 + m3 * 8192 + r16 * 8;
            PIN(sa); PIN(sf);
#define LOAD3(s) do { const int pr_ = (s) >> 1, k2_ = (s) & 1; \
                bf[k2_][0] = tr_frag((k2_ ? ba1[0] : ba0[0]) + (pr_ & 3) * 2 * L3_BSLOT, 4 * 12 * 16); \
                _Pragma("unroll") for (int t = 0; t < 4; ++t) af[k2_][t] = lds128((k2_ ? aa1 : aa0) + (pr_ % 3) * 16384 + t * 1024); \
                _Pragma("unroll") for (int i = 1; i < 3; ++i) bf[k2_][i] = tr_frag((k2_ ? ba1[i] : ba0[i]) + (pr_ & 3) * 2 * L3_BSLOT, 4 * 12 * 16); } while (0)
#define JOBS3(po) do { \
                if ((po) + 3 < 8) ISSUE_A((po) + 3 < 8 ? (po) + 3 : 7); \
                if ((po) + 4 < 8) ISSUE_B((po) + 4 < 8 ? (po) + 4 : 7); \
                if ((po) >= 4) { ISSUE_D2(12 + 6 * ((po) >= 4 ? (po) - 4 : 0)); ISSUE_D2(14 + 6 * ((po) >= 4 ? (po) - 4 : 0)); ISSUE_D2(16 + 6 * ((po) >= 4 ? (po) - 4 : 0)); } \
                __builtin_amdgcn_sched_barrier(0); } while (0)
            WAIT_VM(6);
            BAR();
            STAMP(10);
            LOAD3(0);
#pragma unroll
            for (int s = 0; s < 16; ++s) {
                const int pr = s >> 1;
                if ((s & 1) && s < 15) {
                    if (s == 1) WAIT_VM(4); else if (s == 3) WAIT_VM(7); else if (s < 13) WAIT_VM(8); else WAIT_VM(6);
                    BAR();
                }
                if ((s & 1) && s < 15) { if (w < 4) JOBS3(s >> 1); }
                if (s < 15) LOAD3(s + 1);
                f32x2 pp[4];
                if (s == 6) { pp[0] = lds64f(sa); pp[1] = lds64f(sa + 512); pp[2] = lds64f(sa + 1024); pp[3] = lds64f(sa + 1536); }
                if (s == 12) {
#pragma unroll
                    for (int t = 0; t < 4; ++t) st[t] = lds64f(sf + t * 128);
                }
#pragma unroll
                for (int i = 0; i < 3; ++i) {
#pragma unroll
                    for (int t = 0; t < 4; ++t) acc[t][i] = mfma32(bf[s & 1][i], af[s & 1][t], acc[t][i]);
                    if ((s & 1) == 0) cs[i] = mfma32(bf[s & 1][i], ONES, cs[i]);
                }
#pragma unroll
                for (int i = 0; i < 10; ++i) { __builtin_amdgcn_sched_group_barrier(0x8, 1, 0); __builtin_amdgcn_sched_group_barrier(0x100, 1, 0); }
#pragma unroll
                for (int i = 0; i < 5; ++i) { __builtin_amdgcn_sched_group_barrier(0x8, 1, 0); __builtin_amdgcn_sched_group_barrier(0x100, 1, 0); }
                __builtin_amdgcn_sched_barrier(0);
                if (s == 6) {
                    const f32x2 sm = (pp[0] + pp[1]) + (pp[2] + pp[3]);
                    float sx = sm.x, sq = sm.y;
                    sx += __builtin_amdgcn_update_dpp(0.f, sx, 0xB1, 0xf, 0xf, false); sq += __builtin_amdgcn_update_dpp(0.f, sq, 0xB1, 0xf, 0xf, false);
                    sx += __builtin_amdgcn_update_dpp(0.f, sx, 0x4E, 0xf, 0xf, false); sq += __builtin_amdgcn_update_dpp(0.f, sq, 0x4E, 0xf, 0xf, false);
                    if ((lane & 3) == 0) *(LAS f32x2*)(sa + 512) = ln_stats(sx, sq);
                }
                __builtin_amdgcn_sched_barrier(0);
                if (!(s & 1) && s >= 2) { if (w >= 4) JOBS3((s >> 1) - 1); }
            }
            STAMP(11);
            if (r16 == 0) {
#pragma unroll
                for (int i = 0; i < 3; ++i) *(LAS f32x4*)(L3_ST + 1024 + (3 * w + i) * 64 + g * 16) = cs[i];
            }
            BAR();
            DMA(wk, 30 * 4096, vwd, rs_wd, swd, 0);
            DMA(wk, 32 * 4096, vwd, rs_wd, swd + (kh ? 35 : 22) * 32768, 0);
            ISSUE_D2(0); ISSUE_D2(2); ISSUE_D2(4);
            const unsigned hi = lane >> 5;
            const float r01 = hi ? st[1].y : st[0].y, r23 = hi ? st[3].y : st[2].y;
            const float rr01 = r01 * r01, rr23 = r23 * r23;
            const unsigned tau = 2 * (g & 1) + (g >> 1);
#pragma unroll
            for (int i = 0; i < 3; ++i) {
                const f32x4 cv = cs[i] + lds128f(L3_ST + 1024 + (3 * (w ^ 4) + i) * 64 + g * 16);
                u32x2 hp[2];
#pragma unroll
                for (int pp = 0; pp < 2; ++pp) {
                    const f32x4 e0 = acc[2 * pp][i] - st[2 * pp].x * cv, e1 = acc[2 * pp + 1][i] - st[2 * pp + 1].x * cv;
                    float hv[4];
#pragma unroll
                    for (int r = 0; r < 4; ++r) {
                        auto sw = __builtin_amdgcn_permlane32_swap(f2u(e0[r]), f2u(e1[r]), false, false);
                        const unsigned x0 = sw[0], x1 = sw[1];
                        hv[r] = __builtin_fmaxf(u2f(x0), 0.0f) * (u2f(x1) * (pp ? rr23 : rr01));
                    }
                    hp[pp] = (u32x2){pk_bf16(hv[0], hv[1]), pk_bf16(hv[2], hv[3])};
                }
                auto s0 = __builtin_amdgcn_permlane16_swap(hp[0].x, hp[1].x, false, false);
                auto s1 = __builtin_amdgcn_permlane16_swap(hp[0].y, hp[1].y, false, false);
                const u32x4 hf4 = {s0[0], s1[0], s0[1], s1[1]};
                const unsigned U = 12 * c + 3 * n3 + i;
                const unsigned soff = WS_HID + ((4 * m3) * NKS4 + (U >> 2)) * 1024 + (U & 3) * 256;
                const unsigned off = U < 172 ? tau * (NKS4 * 1024) + r16 * 16 + soff : 0xFFFFFFF0u;
                __builtin_amdgcn_raw_buffer_store_b128(hf4, rs_ws, off, 0, AUX_SC1);
            }
            asm volatile("" ::: "memory");
        }
    }
    STAMP(12);
    {
        const bool active = c < 15;
        if (!active) {
            ISSUE_D2(12); ISSUE_D2(14); ISSUE_D2(16); ISSUE_D2(18); ISSUE_D2(20); ISSUE_D2(22); ISSUE_D2(24); ISSUE_D2(26); ISSUE_D2(28);
            DMA(wk, 30 * 4096, vwd, rs_wd, swd, 0);
            DMA(wk, 32 * 4096, vwd, rs_wd, swd + (kh ? 35 : 22) * 32768, 0);
            ISSUE_D2(0); ISSUE_D2(2); ISSUE_D2(4);
        }
        WAIT_VM(0);
        BAR();
        if (tid == 0 && active) __hip_atomic_store(flags + (FL_3 + c) * 32, epoch, __ATOMIC_RELAXED, __HIP_MEMORY_SCOPE_AGENT);
        STAMP(13);
#define REST_D() do { ISSUE_D2(6); ISSUE_D2(8); ISSUE_D2(10); ISSUE_D2(34); ISSUE_D2(36); ISSUE_D2(38); } while (0)
        if (w != 0) REST_D();
        const unsigned wb0 = (8 * g + q4) * 128 + (fsw * 32) + 8 * p4;
        u32x4 bq[22][4];
#pragma unroll
        for (int ct = 0; ct < 4; ++ct) {
            const u32x4 t0 = tr_frag((wb0 ^ (ct * 32)) + 30 * 4096, 512);
            bq[1][ct] = tr_frag((wb0 ^ (ct * 32)) + (kh ? 32 : 31) * 4096, 512);
            bq[0][ct] = kh ? (u32x4){0u, 0u, 0u, 0u} : t0;
        }
        BAR();
#define LAST_D() do { ISSUE_D2(30); DMA(wk, 32 * 4096, vwd, rs_wd, swd, 35 * 32768); } while (0)
        if (w != 0) LAST_D();
        unsigned wbk = wb0 + kh * (20 * 4096);
        unsigned abase = lane * 16;
        const unsigned asoff = WS_HID + ((4 * hf + rt4) * NKS4 + 21 * kh) * 1024;
        PIN(wbk); PIN(abase);
        if (w == 0) wait_flags(flags + FL_3 * 32, 15, epoch, lane);
        BAR();
        STAMP(14);
        u32x4 af[22];
#define LOADA4(k) af[k] = __builtin_amdgcn_raw_buffer_load_b128(rs_ws, abase + ((k) & 3) * 1024, asoff + ((k) >> 2) * 4096, AUX_SC1)
        if (w == 0) { REST_D(); LAST_D(); }
#pragma unroll
        for (int k = 0; k < 6; ++k) LOADA4(k);
        WAIT_VM(6);
        BAR();
#define LOAD4(j) do { _Pragma("unroll") for (int ct = 0; ct < 4; ++ct) bq[j][ct] = tr_frag((wbk ^ (ct * 32)) + ((j) - 2) * 4096, 512); } while (0)
        LOAD4(2); LOAD4(3); LOAD4(4); LOAD4(5);
        __builtin_amdgcn_sched_barrier(0);
        f32x4 acc[4] = {Z4, Z4, Z4, Z4};
#pragma unroll
        for (int j = 0; j < 22; ++j) {
            if (j + 6 < 22) { LOADA4(j + 6); LOAD4(j + 6); }
#pragma unroll
            for (int ct = 0; ct < 4; ++ct) acc[ct] = mfma32(bq[j][ct], af[j], acc[ct]);
#pragma unroll
            for (int i = 0; i < 4; ++i) { __builtin_amdgcn_sched_group_barrier(0x8, 1, 0); __builtin_amdgcn_sched_group_barrier(0x100, 2, 0); }
            __builtin_amdgcn_sched_barrier(0);
        }
        STAMP(15);
        BAR();
        const unsigned red = w * 2048 + lane * 16;
        *(LAS f32x4*)red = kh ? acc[0] : acc[2];
        *(LAS f32x4*)(red + 1024) = kh ? acc[1] : acc[3];
        BAR();
        const f32x4 a0 = (kh ? acc[2] : acc[0]) + lds128f(red ^ 8192);
        const f32x4 a1 = (kh ? acc[3] : acc[1]) + lds128f((red ^ 8192) + 1024);
        auto s0 = __builtin_amdgcn_permlane16_swap(pk_bf16(a0[0], a0[1]), pk_bf16(a1[0], a1[1]), false, false);
        auto s1 = __builtin_amdgcn_permlane16_swap(pk_bf16(a0[2], a0[3]), pk_bf16(a1[2], a1[3]), false, false);
        const u32x4 at = {s0[0], s1[0], s0[1], s1[1]};
        u32x4 o;
#pragma unroll
        for (int i = 0; i < 4; ++i) o[i] = pk_bf16(bf_lo(at[i]) + bf_lo(h_keep[i]), bf_hi(at[i]) + bf_hi(h_keep[i]));
        *(u32x4*)((char*)P.out + tok_off) = o;
    }
    STAMP(16);
}

}

extern "C" void kernel_launch(void* const* d_in, const int* in_sizes, int n_in, void* d_out, int out_size, void* d_ws, size_t ws_size, hipStream_t stream) {
    Params P;
    P.x = (const u16*)d_in[0]; P.wqkv = (const u16*)d_in[1]; P.wo = (const u16*)d_in[2];
    P.wg = (const u16*)d_in[4]; P.wu = (const u16*)d_in[5]; P.wd = (const u16*)d_in[6];
    P.out = (u16*)d_out; P.ws = (unsigned char*)d_ws;
    (void)hipFuncSetAttribute((const void*)fused_block, hipFuncAttributeMaxDynamicSharedMemorySize, LDS_BYTES);
    hipLaunchKernelGGL(fused_block, dim3(256), dim3(512), LDS_BYTES, stream, P);
}
```

```cpp
#include <hip/hip_runtime.h>
#include <hip/hip_bf16.h>
#include <stdint.h>
#pragma clang diagnostic ignored "-Wint-to-pointer-cast"

#define LAS __attribute__((address_space(3)))
#define GAS __attribute__((address_space(1)))
typedef unsigned short u16;
typedef unsigned long long u64;
typedef short s16x4 __attribute__((ext_vector_type(4)));
typedef __bf16 bf16x8 __attribute__((ext_vector_type(8)));
typedef __bf16 bf16x2 __attribute__((ext_vector_type(2)));
typedef float f32x4 __attribute__((ext_vector_type(4)));
typedef float f32x2 __attribute__((ext_vector_type(2)));
typedef unsigned u32x4 __attribute__((ext_vector_type(4)));
typedef unsigned u32x2 __attribute__((ext_vector_type(2)));
typedef GAS u64 gu64;
typedef __amdgpu_buffer_rsrc_t rsrc_t;

namespace {

constexpr int SEQ = 2048, DM = 512, FF = 1376;
constexpr int NKS4 = FF / 32;
constexpr unsigned WS_FLAG = 0;
constexpr unsigned WS_ST = 16384;
constexpr unsigned WS_O = 40960;
constexpr unsigned WS_H = WS_O + 131072;
constexpr unsigned WS_HID = WS_H + 131072;
constexpr unsigned WS_SPARE = WS_HID + 8 * 43 * 1024;
constexpr unsigned WS_BYTES = WS_SPARE + 8192;
constexpr int FL_GEN = 0, FL_1 = 8, FL_2 = 24, FL_3 = 40;
constexpr int AUX_SC1 = 16;
constexpr unsigned kSpinCap = 1u << 16;

constexpr int LDS_BYTES = 163840;
constexpr int L1_X = 0, L1_H = 65536, L1_W = 81920, L1_WSLOT = 24576, L1_SCR = 155648;
constexpr int L2_WO = 81920, L2_RED = 114688;
constexpr int L3_A = 0, L3_B = 49152, L3_BSLOT = 12288, L3_ST = 147456;

__device__ __forceinline__ constexpr int vm_imm(int n) { return (n & 0xF) | ((n >> 4) << 14) | (0x7 << 4) | (0xF << 8); }
#define WAIT_VM(n) asm volatile("s_waitcnt vmcnt(%0)" :: "i"(n) : "memory")
#define BAR() do { asm volatile("s_waitcnt lgkmcnt(0)" ::: "memory"); __builtin_amdgcn_s_barrier(); asm volatile("" ::: "memory"); } while (0)
#define PIN(x) asm volatile("" : "+v"(x))
#define DMA(m0b, ldsoff, voff, rs, sbase, soffimm) do { unsigned t_; asm volatile("s_add_u32 m0, %1, %2\n\ts_add_u32 %0, %5, %6\n\tbuffer_load_dwordx4 %3, %4, %0 offen lds" \
    : "=&s"(t_) : "s"(m0b), "i"(ldsoff), "v"(voff), "s"(rs), "s"(sbase), "i"(soffimm) : "scc", "memory"); } while (0)
#define DMA_SC1(m0b, ldsoff, voff, rs, sbase, soffimm) do { unsigned t_; asm volatile("s_add_u32 m0, %1, %2\n\ts_add_u32 %0, %5, %6\n\tbuffer_load_dwordx4 %3, %4, %0 offen sc1 lds" \
    : "=&s"(t_) : "s"(m0b), "i"(ldsoff), "v"(voff), "s"(rs), "s"(sbase), "i"(soffimm) : "scc", "memory"); } while (0)

__device__ __forceinline__ rsrc_t mk_rsrc(const void* p, unsigned bytes) { return __builtin_amdgcn_make_buffer_rsrc((void*)p, 0, bytes, 0x00020000); }
__device__ __forceinline__ float u2f(unsigned u) { return __builtin_bit_cast(float, u); }
__device__ __forceinline__ unsigned f2u(float f) { return __builtin_bit_cast(unsigned, f); }
__device__ __forceinline__ unsigned pk_bf16(float a, float b) { bf16x2 v; v[0] = (__bf16)a; v[1] = (__bf16)b; return __builtin_bit_cast(unsigned, v); }
__device__ __forceinline__ float bf_lo(unsigned u) { return u2f(u << 16); }
__device__ __forceinline__ float bf_hi(unsigned u) { return u2f(u & 0xffff0000u); }
__device__ __forceinline__ float rbf(float f) { return (float)(__bf16)f; }
__device__ __forceinline__ f32x4 mfma32(u32x4 a, u32x4 b, f32x4 c) {
    return __builtin_amdgcn_mfma_f32_16x16x32_bf16(__builtin_bit_cast(bf16x8, a), __builtin_bit_cast(bf16x8, b), c, 0, 0, 0);
}
__device__ __forceinline__ f32x4 mfma16(u32x2 a, u32x2 b, f32x4 c) {
    return __builtin_amdgcn_mfma_f32_16x16x16bf16_1k(__builtin_bit_cast(s16x4, a), __builtin_bit_cast(s16x4, b), c, 0, 0, 0);
}
__device__ __forceinline__ u32x4 lds128(unsigned a) { return *(LAS u32x4*)a; }
__device__ __forceinline__ f32x4 lds128f(unsigned a) { return *(LAS f32x4*)a; }
__device__ __forceinline__ f32x2 lds64f(unsigned a) { return *(LAS f32x2*)a; }
__device__ __forceinline__ u32x2 tr_read(unsigned a) { return __builtin_bit_cast(u32x2, __builtin_amdgcn_ds_read_tr16_b64_v4i16((LAS s16x4*)a)); }
__device__ __forceinline__ u32x4 tr_frag(unsigned a, int step) { const u32x2 x = tr_read(a), y = tr_read(a + step); return (u32x4){x.x, x.y, y.x, y.y}; }
__device__ __forceinline__ float swap16_lo(float v) {
    const unsigned a = f2u(v); auto r = __builtin_amdgcn_permlane16_swap(a, a, false, false); const unsigned x = r[0]; return u2f(x);
}
__device__ __forceinline__ float xmax16_32(float v) {
    unsigned a = f2u(v);
    auto r = __builtin_amdgcn_permlane16_swap(a, a, false, false);
    const unsigned r0 = r[0], r1 = r[1];
    v = __builtin_fmaxf(u2f(r0), u2f(r1));
    a = f2u(v);
    auto s = __builtin_amdgcn_permlane32_swap(a, a, false, false);
    const unsigned s0 = s[0], s1 = s[1];
    return __builtin_fmaxf(u2f(s0), u2f(s1));
}
__device__ __forceinline__ float bcast_row0(float v) { return __builtin_amdgcn_update_dpp(0.f, v, 0x150, 0xf, 0xf, false); }
__device__ __forceinline__ float diag4(f32x4 d, unsigned lane) { const float a = (lane & 1) ? d[1] : d[0], b = (lane & 1) ? d[3] : d[2]; return (lane & 2) ? b : a; }
__device__ __forceinline__ f32x2 ln_stats(float s, float q) {
    const float mean = s * (1.0f / DM);
    const float var = __builtin_fmaxf(q * (1.0f / DM) - mean * mean, 0.0f);
    const float sd = rbf(__builtin_amdgcn_sqrtf(rbf(rbf(var) + 1e-5f)));
    return (f32x2){rbf(mean), __builtin_amdgcn_rcpf(sd)};
}
__device__ __forceinline__ void wait_flags(gu64* base, unsigned n, u64 epoch, unsigned lane) {
    for (unsigned spins = 0; spins < kSpinCap; ++spins) {
        u64 v = epoch;
        if (lane < n) v = __hip_atomic_load(base + lane * 32, __ATOMIC_RELAXED, __HIP_MEMORY_SCOPE_AGENT);
        if (__all(v == epoch)) break;
    }
    __builtin_amdgcn_fence(__ATOMIC_ACQUIRE, "wavefront");
}

struct Params { const u16* x; const u16* wqkv; const u16* wo; const u16* wg; const u16* wu; const u16* wd; u16* out; unsigned char* ws; };

#define STAMP(i) do {} while (0)

__global__ void __launch_bounds__(512, 2) __attribute__((amdgpu_waves_per_eu(2, 2))) fused_block(Params P
) {
    const unsigned tid = threadIdx.x, lane = tid & 63;
    const unsigned w = __builtin_amdgcn_readfirstlane(tid >> 6);
    const unsigned b = blockIdx.x, xcd = b & 7, jj = b >> 3;
    const unsigned p = 4 * (xcd >> 1) + (jj >> 3);
    const unsigned hf = (jj >> 2) & 1, hg = 4 * (xcd & 1) + (jj & 3);
    const unsigned c = 8 * hf + hg;
    const unsigned r16 = lane & 15, g = lane >> 4, l8 = lane >> 3, l7 = lane & 7, q4 = r16 >> 2, p4 = lane & 3;
    STAMP(0);
    const unsigned wk = w * 1024;
    const rsrc_t rs_x = mk_rsrc(P.x, SEQ * DM * 2);
    const rsrc_t rs_w = mk_rsrc(P.wqkv, DM * 3 * DM * 2);
    unsigned char* const wsp = P.ws + (size_t)p * WS_BYTES;
    gu64* const flags = (gu64*)(wsp + WS_FLAG);
    const unsigned T0 = 128 * p + 64 * hf;
    STAMP(18);
    u32x4 ONES = {0x3F803F80u, 0x3F803F80u, 0x3F803F80u, 0x3F803F80u};
    PIN(ONES);
    const f32x4 Z4 = {0.f, 0.f, 0.f, 0.f};
    const unsigned fsw = ((q4 >> 1) & 1) | ((g & 1) << 1);
    const unsigned wsw = (l7 ^ (((g & 1) | ((w & 1) << 1)) << 1)) * 16;
    u64 gen = 0;
    const unsigned rt4 = w & 3, kh = w >> 2, rp4 = rt4 >> 1, kq4 = (rt4 & 1) + 2 * kh;
    const unsigned tok_off = (128 * p + 64 * hf + 32 * (rt4 & 1) + (rt4 >> 1) + 2 * r16) * 1024 + (64 * hg + 32 * kh + 16 * (g & 1) + 8 * (g >> 1)) * 2;
    u32x4 xr;

    {
        const unsigned rot = (2 * (jj & 3) + (jj >> 2) + (xcd >> 1)) & 7;
        const unsigned hh = w & 3, par = w >> 2;
        const unsigned vh = l8 * 2048 + ((l7 ^ g) * 16);
        const unsigned vxr = vh ^ ((w & 1) * 64);
        const unsigned sxr = (T0 + 32 * ((w >> 1) & 1) + (w >> 2) + 16 * (w & 1)) * 1024;
        const unsigned vw = (8 * w + l8) * 3072 + wsw + hg * 128;
        {
            const unsigned kap = (w + rot) & 7;
            const unsigned sh = (T0 == 0 ? 0 : T0 - 16) * 1024 + kap * 128;
            const unsigned hb = L1_H + w * 2048;
            if (lane >= 8) { DMA(hb, 0, vh, rs_x, sh, 0); const unsigned vh1 = vh ^ 64; DMA(hb, 1024, vh1, rs_x, sh, 1024); }
            else { *(LAS u32x4*)(hb + lane * 16) = ONES; *(LAS u32x4*)(hb + 1024 + lane * 16) = ONES; }
        }
        unsigned kx[8];
#pragma unroll
        for (int q = 0; q < 8; ++q) kx[q] = (q + rot) & 7;
#define TOUCH(ptr) asm volatile("s_mov_b32 m0, %0\n\ts_nop 0\n\tglobal_load_lds_dwordx4 %1, off" :: "s"(L1_SCR + 6144), "v"(ptr) : "memory")
#define ISSUE_X(q) DMA(wk, L1_X + (q) * 8192, vxr, rs_x, sxr + kx[q] * 128, 0)
#define ISSUE_W1(q, mat) DMA(wk, L1_W + ((q) % 3) * L1_WSLOT + (mat) * 8192, vw, rs_w, kx[q] * 196608, (mat) * 1024)
#define ISSUE_W(q) do { ISSUE_W1(q, 0); ISSUE_W1(q, 1); ISSUE_W1(q, 2); } while (0)
        ISSUE_X(0); ISSUE_W(0);
        __builtin_amdgcn_s_barrier();
        ISSUE_X(1); ISSUE_W(1);
        u64 genv;
        { const gu64* gp = flags + FL_GEN * 32; asm volatile("global_load_dwordx2 %0, %1, off sc1" : "=v"(genv) : "v"(gp) : "memory"); }
#define NTOUCH 4
        STAMP(17);
        const unsigned xa = (r16 >> 3) * 1024 + (r16 & 7) * 128 + ((g ^ ((r16 >> 1) & 7)) * 16);
        const unsigned xc0 = xa + par * 4096, xc1 = xc0 ^ 64;
        unsigned xh0 = xa + L1_H, xh1 = xh0 ^ 64;
        PIN(xh0); PIN(xh1);
        unsigned wa = L1_W + (8 * g + q4) * 128 + ((hh ^ fsw) * 32) + 8 * p4, wa2 = wa + 2 * L1_WSLOT;
        PIN(wa); PIN(wa2);
        const unsigned sxa = (xa ^ ((hh & 1) * 64));
        unsigned stc = sxa + par * 4096 + (hh >> 1) * 8192, sth = sxa + L1_H + (hh >> 1) * 2048;
        PIN(stc); PIN(sth);
        f32x4 mh, mp, mc;
#pragma unroll
        for (int r = 0; r < 4; ++r) {
            const unsigned kap = 4 * g + r;
            mh[r] = (T0 > 0 && (kap >> 3) == par && (kap & 7) >= r16 + 1) ? 0.f : -1e30f;
            mp[r] = (kap >= r16 + 9) ? 0.f : -1e30f;
            mc[r] = (kap <= r16 && r16 - kap <= 7) ? 0.f : -1e30f;
        }
        PIN(mh); PIN(mp); PIN(mc);

        f32x4 aq[3] = {Z4, Z4, Z4}, ak[3] = {Z4, Z4, Z4}, av[3] = {Z4, Z4, Z4};
        f32x4 gr[3] = {Z4, Z4, Z4}, rs[3] = {Z4, Z4, Z4};
        f32x2 sL[3]; f32x4 sR[3][2];
        const unsigned part = L1_SCR, fin = L1_SCR + 2560 + w * 384;
        u32x4 xf[2][3], wf[2][3], sf[2][3];
        const rsrc_t rs_wo = mk_rsrc(P.wo, DM * DM * 2);
        const unsigned vwo = (8 * w + l8) * 1024 + wsw + hg * 128;
#define LOAD1(s) do { const int kq_ = (s) >> 1, kk_ = (s) & 1; \
            xf[(s) & 1][0] = lds128((kk_ ? xh1 : xh0) + kq_ * 2048); xf[(s) & 1][1] = lds128((kk_ ? xc1 : xc0) + kq_ * 8192); xf[(s) & 1][2] = lds128((kk_ ? xc1 : xc0) + kq_ * 8192 + 2048); \
            _Pragma("unroll") for (int mat = 0; mat < 3; ++mat) wf[(s) & 1][mat] = tr_frag((kq_ % 3 == 2 ? wa2 : wa + (kq_ % 3) * L1_WSLOT) + mat * 8192 + kk_ * 4096, 512); } while (0)
#define X2(q, h) DMA(wk, L1_X + (q) * 8192 + (h) * 4096, vxr, rs_x, sxr + kx[q] * 128, (h) * 1024)
#define W2(q, mat, h) DMA(wk, L1_W + ((q) % 3) * L1_WSLOT + (mat) * 8192 + (h) * 4096, vw, rs_w, kx[q] * 196608, (mat) * 1024 + (h) * 98304)
#define O2(lds, m, h) DMA(wk, (lds) + (h) * 4096, vwo, rs_wo, 0u, (m) * 65536 + (h) * 32768)
#define JOB1(it, j) do { const int pc_ = (j) >> 1, h_ = (j) & 1; \
            if ((it) == 0) { if (pc_ == 0) X2(2, h_); else if (pc_ < 4) W2(2, (pc_ >= 1 && pc_ < 4) ? pc_ - 1 : 0, h_); } \
            if ((it) >= 1 && (it) <= 5 && pc_ < 3) W2(((it) >= 1 && (it) <= 5) ? (it) + 2 : 0, pc_ < 3 ? pc_ : 0, h_); \
            if ((it) == 1 && pc_ >= 3 && pc_ < 6) X2((pc_ >= 3 && pc_ < 6) ? pc_ : 0, h_); \
            if ((it) == 2 && pc_ >= 3 && pc_ < 5) X2((pc_ >= 3 && pc_ < 5) ? pc_ + 3 : 0, h_); \
            if ((it) == 6) { if (pc_ < 2) O2(L2_WO + (6 + (pc_ < 2 ? pc_ : 0)) * 8192, 6 + (pc_ < 2 ? pc_ : 0), h_); else if (pc_ < 5) O2(((pc_ >= 2 && pc_ < 5) ? pc_ - 2 : 0) * 8192, 3 + ((pc_ >= 2 && pc_ < 5) ? pc_ - 2 : 0), h_); } \
            if ((it) == 7 && pc_ < 3) O2(L2_WO + (pc_ < 3 ? pc_ : 0) * 8192, pc_ < 3 ? pc_ : 0, h_); } while (0)
#define SLOT1(it, sl) do { __builtin_amdgcn_sched_barrier(0); if (w < 4) { JOB1(it, 2 * (sl)); JOB1(it, 2 * (sl) + 1); } __builtin_amdgcn_sched_barrier(0); } while (0)
        WAIT_VM(5);
        BAR();
        STAMP(1);
        LOAD1(0);
#pragma unroll
        for (int s = 0; s < 16; ++s) {
            const int it = (s + 1) / 2, sl0 = (s & 1) ? 0 : 3;
            if (s & 1) {
                if (w < 4) {
                    if (s == 1) WAIT_VM(9 + NTOUCH); else if (s == 3) WAIT_VM(12 + NTOUCH); else if (s == 5) WAIT_VM(14); else if (s == 7) WAIT_VM(10); else if (s < 13) WAIT_VM(6); else if (s == 13) WAIT_VM(11);
                } else {
                    if (s == 1) WAIT_VM(1 + NTOUCH); else if (s == 9) WAIT_VM(0);
                }
                if (s < 15) BAR();
                if (s == 9) { asm volatile("" : "+v"(genv)); gen = ((u64)(unsigned)__builtin_amdgcn_readfirstlane((unsigned)(genv >> 32)) << 32) | (unsigned)__builtin_amdgcn_readfirstlane((unsigned)genv); }
            }
            const int n_ = s + 1, kq_ = n_ >> 1, kk_ = n_ & 1, cb = s & 1, nb = n_ & 1;
            const unsigned wsl = (kq_ % 3 == 2 ? wa2 : wa + (kq_ % 3) * L1_WSLOT) + kk_ * 4096;
            if (s < 15) { xf[nb][0] = lds128((kk_ ? xh1 : xh0) + kq_ * 2048); xf[nb][1] = lds128((kk_ ? xc1 : xc0) + kq_ * 8192); xf[nb][2] = lds128((kk_ ? xc1 : xc0) + kq_ * 8192 + 2048); }
#pragma unroll
            for (int t = 0; t < 3; ++t) aq[t] = mfma32(wf[cb][0], xf[cb][t], aq[t]);
#pragma unroll
            for (int i = 0; i < 3; ++i) { __builtin_amdgcn_sched_group_barrier(0x8, 1, 0); __builtin_amdgcn_sched_group_barrier(0x100, 1, 0); }
            if (s == 0) { __builtin_amdgcn_sched_barrier(0); if (w < 4) { JOB1(0, 0); JOB1(0, 1); JOB1(0, 2); } __builtin_amdgcn_sched_barrier(0); } else SLOT1(it, sl0);
            if (s < 15) { wf[nb][0] = tr_frag(wsl, 512); wf[nb][1] = tr_frag(wsl + 8192, 512); }
#pragma unroll
            for (int t = 0; t < 3; ++t) ak[t] = mfma32(wf[cb][1], xf[cb][t], ak[t]);
#pragma unroll
            for (int i = 0; i < 3; ++i) { __builtin_amdgcn_sched_group_barrier(0x8, 1, 0); __builtin_amdgcn_sched_group_barrier(0x100, 2, 0); }
            if (s == 0) { __builtin_amdgcn_sched_barrier(0); if (w < 4) { JOB1(0, 3); JOB1(0, 4); JOB1(0, 5); } __builtin_amdgcn_sched_barrier(0); } else SLOT1(it, sl0 + 1);
            if (s < 15) wf[nb][2] = tr_frag(wsl + 16384, 512);
            if (s >= 6 && s <= 9) { const int j = s - 6; sf[j & 1][0] = lds128(sth + j * 4096); sf[j & 1][1] = lds128(stc + j * 16384); sf[j & 1][2] = lds128(stc + j * 16384 + 2048); }
            f32x2 pp[4];
            if (s == 11) {
                const unsigned pa = part + ((g == 0 ? 0 : (g == 3 ? 0 : 2 * par + g)) * 64 + r16) * 8;
                pp[0] = lds64f(pa); pp[1] = lds64f(pa + 128); pp[2] = lds64f(pa + 256); pp[3] = lds64f(pa + 384);
            }
            if (s == 13) {
#pragma unroll
                for (int t = 0; t < 3; ++t) { sL[t] = lds64f(fin + (t * 16 + r16) * 8); sR[t][0] = lds128f(fin + (t * 16 + 4 * g) * 8); sR[t][1] = lds128f(fin + (t * 16 + 4 * g) * 8 + 16); }
            }
#pragma unroll
            for (int t = 0; t < 3; ++t) av[t] = mfma32(xf[cb][t], wf[cb][2], av[t]);
            if (s >= 7 && s <= 10) {
#pragma unroll
                for (int t = 0; t < 3; ++t) { gr[t] = mfma32(sf[(s - 7) & 1][t], sf[(s - 7) & 1][t], gr[t]); rs[t] = mfma32(ONES, sf[(s - 7) & 1][t], rs[t]); }
            }
#pragma unroll
            for (int i = 0; i < 9; ++i) { __builtin_amdgcn_sched_group_barrier(0x8, 1, 0); __builtin_amdgcn_sched_group_barrier(0x100, 1, 0); }
            __builtin_amdgcn_sched_barrier(0);
            if (s == 10 && g == q4) {
                *(LAS f32x2*)(part + (hh * 16 + r16) * 8) = (f32x2){rs[0][0], diag4(gr[0], lane)};
                *(LAS f32x2*)(part + (((1 + 2 * par) * 4 + hh) * 16 + r16) * 8) = (f32x2){rs[1][0], diag4(gr[1], lane)};
                *(LAS f32x2*)(part + (((2 + 2 * par) * 4 + hh) * 16 + r16) * 8) = (f32x2){rs[2][0], diag4(gr[2], lane)};
            }
            if (s == 11) {
                const f32x2 sm = (pp[0] + pp[1]) + (pp[2] + pp[3]);
                const f32x2 res = ln_stats(sm.x, sm.y);
                if (g < 3) *(LAS f32x2*)(fin + (g * 16 + r16) * 8) = res;
            }
            if (s == 0) { __builtin_amdgcn_sched_barrier(0); if (w < 4) { JOB1(0, 6); JOB1(0, 7); } __builtin_amdgcn_sched_barrier(0); } else SLOT1(it, sl0 + 2);
            if (s == 0) {
            const char* t1 = lane < 22 ? (const char*)P.wg + lane * 65536 : (lane < 44 ? (const char*)P.wu + (lane - 22) * 65536 : (const char*)P.wd + (lane - 44) * 65536);
            const char* t2 = lane < 8 ? (const char*)P.wo + lane * 65536 : (lane < 32 ? (const char*)P.out + p * 131072 + (lane - 8) * 4096 : (const char*)P.wd + (20 + (lane & 1)) * 65536);
            const char* tw = w == 0 ? (const char*)wsp + WS_FLAG + lane * 256 : (w == 1 ? t1 : (w == 2 ? t2 : (const char*)wsp + ((w & 1) ? WS_SPARE : 32768u) + lane * 16));
            TOUCH(tw);
#pragma unroll
            for (int i = 0; i < 3; ++i) {
                unsigned q = b * 24 + w * 3 + i; if (q >= 4640) q -= 4640;
                const char* base = q < 512 ? (const char*)P.wo + q * 1024 : (q < 1888 ? (const char*)P.wg + (q - 512) * 1024 : (q < 3264 ? (const char*)P.wu + (q - 1888) * 1024 : (const char*)P.wd + (q - 3264) * 1024));
                const char* t3 = base + lane * 16;
                TOUCH(t3);
            }

        }
            if (s == 12) asm volatile("buffer_load_dwordx4 %0, %1, %2, 0 offen" : "=v"(xr) : "v"(tok_off), "s"(rs_x) : "memory");
        }
        STAMP(2);
        f32x4 csq, csk;
#pragma unroll
        for (int r = 0; r < 4; ++r) { csq[r] = bcast_row0(aq[0][r]); csk[r] = bcast_row0(ak[0][r]); }
        const float csv = swap16_lo(av[0][0]);
        u32x2 qb[2], kb[3], vb[3];
#pragma unroll
        for (int t = 0; t < 3; ++t) {
            const f32x4 kv = (ak[t] - sL[t].x * csk) * sL[t].y;
            kb[t] = (u32x2){pk_bf16(kv[0], kv[1]), pk_bf16(kv[2], kv[3])};
            if (t > 0) { const f32x4 qv = (aq[t] - sL[t].x * csq) * sL[t].y; qb[t - 1] = (u32x2){pk_bf16(qv[0], qv[1]), pk_bf16(qv[2], qv[3])}; }
            const f32x4 mn = {sR[t][0][0], sR[t][0][2], sR[t][1][0], sR[t][1][2]}, rd = {sR[t][0][1], sR[t][0][3], sR[t][1][1], sR[t][1][3]};
            const f32x4 vv = (av[t] - mn * csv) * rd;
            vb[t] = (u32x2){pk_bf16(vv[0], vv[1]), pk_bf16(vv[2], vv[3])};
        }
        const float SC = 0.25f * 1.44269504088896f;
        u32x2 ob[2];
#pragma unroll
        for (int t = 0; t < 2; ++t) {
            f32x4 sp = mfma16(kb[t], qb[t], t == 0 ? mh : mp), sc = mfma16(kb[t + 1], qb[t], mc);
            float mx = __builtin_fmaxf(__builtin_fmaxf(__builtin_fmaxf(sp[0], sp[1]), __builtin_fmaxf(sp[2], sp[3])), __builtin_fmaxf(__builtin_fmaxf(sc[0], sc[1]), __builtin_fmaxf(sc[2], sc[3])));
            mx = xmax16_32(mx);
            const float mb = -mx * SC;
#pragma unroll
            for (int r = 0; r < 4; ++r) { sp[r] = __builtin_amdgcn_exp2f(sp[r] * SC + mb); sc[r] = __builtin_amdgcn_exp2f(sc[r] * SC + mb); }
            const u32x4 pf = {pk_bf16(sp[0], sp[1]), pk_bf16(sp[2], sp[3]), pk_bf16(sc[0], sc[1]), pk_bf16(sc[2], sc[3])};
            const u32x4 vf = {vb[t].x, vb[t].y, vb[t + 1].x, vb[t + 1].y};
            const f32x4 sm = mfma32(ONES, pf, Z4);
            const f32x4 o = mfma32(vf, pf, Z4) * __builtin_amdgcn_rcpf(sm[0]);
            ob[t] = (u32x2){pk_bf16(o[0], o[1]), pk_bf16(o[2], o[3])};
        }
        auto s0 = __builtin_amdgcn_permlane16_swap(ob[0].x, ob[1].x, false, false);
        auto s1 = __builtin_amdgcn_permlane16_swap(ob[0].y, ob[1].y, false, false);
        const u32x4 o_frag = {s0[0], s1[0], s0[1], s1[1]};
        const rsrc_t rs_ws1 = mk_rsrc(wsp, WS_BYTES);
        const unsigned off = ((g & 1) * 16384) + ((2 * (hh & 1) + (g >> 1)) * 16 + r16) * 16;
        const unsigned soff = WS_O + ((4 * hf + 2 * par) * 16 + 2 * hg + (hh >> 1)) * 1024;
        __builtin_amdgcn_raw_buffer_store_b128(o_frag, rs_ws1, off + soff, 0, AUX_SC1);
        asm volatile("" ::: "memory");
    }
    STAMP(3);
    const rsrc_t rs_ws = mk_rsrc(wsp, WS_BYTES);
    const u64 epoch = gen + 1;
    const unsigned n3 = w & 3, m3 = w >> 2;
    const rsrc_t rs_g = mk_rsrc(P.wg, DM * FF * 2), rs_u = mk_rsrc(P.wu, DM * FF * 2);
    const rsrc_t rs_gu = mk_rsrc(m3 ? P.wu : P.wg, DM * FF * 2);
    unsigned vg0[3], vg1[3];
#pragma unroll
    for (int i = 0; i < 3; ++i) {
        const unsigned s = (n3 * 3 + i) * 64 + lane;
        const unsigned ks2 = s >= 384 ? 1 : 0, s2 = s - ks2 * 384, row = (s2 * 43691u) >> 19, pos = s2 - row * 12;
        const int umax = (int)(c < 15 ? 171 - 12 * c : 0);
        int u0 = (int)pos - (int)((row >> 3) & 1); if (u0 < 0) u0 += 12;
        int u1 = (int)pos - (int)(((row >> 3) & 1) + 2); if (u1 < 0) u1 += 12;
        const unsigned rb = (ks2 * 32 + row) * (FF * 2) + c * 192;
        vg0[i] = rb + min(u0, umax) * 16; vg1[i] = rb + min(u1, umax) * 16;
    }
    unsigned vgu[3];
#pragma unroll
    for (int i = 0; i < 3; ++i) vgu[i] = m3 ? vg1[i] : vg0[i];
    unsigned mg0[3], mgu[3];
#pragma unroll
    for (int i = 0; i < 3; ++i) { const unsigned pi = n3 * 3 + i; mg0[i] = L3_B + (pi >= 6 ? L3_BSLOT + (pi - 6) * 1024 : pi * 1024); mgu[i] = mg0[i] + m3 * 6144; }
    const unsigned rot3 = (2 * (jj >> 3) + (jj & 7)) & 7;
#define ISSUE_B(pr) do { const unsigned sb_ = (((pr) + rot3) & 7) * (64 * FF * 2); _Pragma("unroll") for (int i_ = 0; i_ < 3; ++i_) DMA(mgu[i_], ((pr) & 3) * 2 * L3_BSLOT, vgu[i_], rs_gu, sb_, 0); } while (0)

    u32x4 h_keep;
    {
        WAIT_VM(0);
        BAR();
        if (tid == 0) __hip_atomic_store(flags + (FL_1 + c) * 32, epoch, __ATOMIC_RELAXED, __HIP_MEMORY_SCOPE_AGENT);
        STAMP(4);
        asm volatile("" : "+v"(xr));
        const unsigned wl = (8 * g + q4) * 128 + (fsw * 32) + 8 * p4;
        const unsigned wb0 = wl + (kh ? 8192 : L2_WO), wb1 = wl + (kh ? L2_WO + 49152 : L2_WO + 16384), wb2 = wl + (kh ? L2_WO + 57344 : 0);
        unsigned abase = lane * 16;
        const unsigned asoff = WS_O + ((4 * hf + rt4) * 16 + 8 * kh) * 1024;
        PIN(abase);
        u32x4 bf[8][4];
#pragma unroll
        for (int k = 0; k < 8; ++k)
#pragma unroll
            for (int ct = 0; ct < 4; ++ct) bf[k][ct] = tr_frag(((k < 4 ? wb0 : (k < 6 ? wb1 : wb2)) ^ (ct * 32)) + (k < 4 ? k : (k < 6 ? k - 4 : k - 6)) * 4096, 512);
        __builtin_amdgcn_sched_barrier(0);
        STAMP(19);
        if (w == 0) wait_flags(flags + (FL_1 + 8 * hf) * 32, 8, epoch, lane);
        BAR();
        STAMP(5);
        u32x4 af[8];
#pragma unroll
        for (int k = 0; k < 4; ++k) af[k] = __builtin_amdgcn_raw_buffer_load_b128(rs_ws, abase + (k & 3) * 1024, asoff + (k >> 2) * 4096, AUX_SC1);
        __builtin_amdgcn_sched_barrier(0);
        f32x4 acc[4] = {Z4, Z4, Z4, Z4};
#pragma unroll
        for (int k = 0; k < 8; ++k) {
            if (k + 4 < 8) af[k + 4] = __builtin_amdgcn_raw_buffer_load_b128(rs_ws, abase + (k & 3) * 1024, asoff + 4096, AUX_SC1);
            __builtin_amdgcn_sched_barrier(0);
#pragma unroll
            for (int ct = 0; ct < 4; ++ct) acc[ct] = mfma32(bf[k][ct], af[k], acc[ct]);
            __builtin_amdgcn_sched_barrier(0);
        }
        STAMP(6);
        const unsigned red = L2_RED + w * 2048 + lane * 16;
        *(LAS f32x4*)red = kh ? acc[0] : acc[2];
        *(LAS f32x4*)(red + 1024) = kh ? acc[1] : acc[3];
        BAR();
        const f32x4 a0 = (kh ? acc[2] : acc[0]) + lds128f(red ^ 8192);
        const f32x4 a1 = (kh ? acc[3] : acc[1]) + lds128f((red ^ 8192) + 1024);
        auto s0 = __builtin_amdgcn_permlane16_swap(pk_bf16(a0[0], a0[1]), pk_bf16(a1[0], a1[1]), false, false);
        auto s1 = __builtin_amdgcn_permlane16_swap(pk_bf16(a0[2], a0[3]), pk_bf16(a1[2], a1[3]), false, false);
        const u32x4 at = {s0[0], s1[0], s0[1], s1[1]};
#pragma unroll
        for (int i = 0; i < 4; ++i) h_keep[i] = pk_bf16(bf_lo(at[i]) + bf_lo(xr[i]), bf_hi(at[i]) + bf_hi(xr[i]));
        const unsigned hoff = ((2 * (g & 1) + (g >> 1)) * 16 + r16) * 16;
        const unsigned hsoff = WS_H + ((4 * hf + rt4) * 16 + 2 * hg + kh) * 1024;
        __builtin_amdgcn_raw_buffer_store_b128(h_keep, rs_ws, hoff + hsoff, 0, AUX_SC1);
        const f32x4 gs = mfma32(h_keep, h_keep, Z4), sm = mfma32(ONES, h_keep, Z4);
        const u32x2 stv = {f2u(sm[0]), f2u(diag4(gs, lane))};
        const unsigned stoff = (g == q4) ? WS_ST + c * 1024 + w * 128 + r16 * 8 : 0xFFFFFFF0u;
        __builtin_amdgcn_raw_buffer_store_b64(stv, rs_ws, stoff, 0, AUX_SC1);
        asm volatile("" ::: "memory");
    }
    STAMP(7);
    const rsrc_t rs_wd = mk_rsrc(P.wd, FF * DM * 2);
    const unsigned vwd = l8 * 1024 + wsw + hg * 128;
#define KS4(s) (11 * ((s) / 10) + (s) % 10 + 1 - ((s) >= 30 ? 1 : 0))
    const unsigned swd = ((w >> 2) * 32 + (w & 3) * 8) * 1024;
#define ISSUE_D2(s0) DMA(wk, (s0) * 4096, vwd, rs_wd, swd, KS4(s0) * 32768)
    const unsigned swd0 = (w & 3) * 8 * 1024;
#define ISSUE_S() do { DMA(wk, 30 * 4096, vwd, rs_wd, swd0 + (kh ? 11 : 0) * 32768, 0); DMA(wk, 32 * 4096, vwd, rs_wd, swd0 + (kh ? 36 : 22) * 32768, 0); } while (0)
    {
        const bool active = c < 15;
        WAIT_VM(0);
        BAR();
        if (tid == 0) __hip_atomic_store(flags + (FL_2 + c) * 32, epoch, __ATOMIC_RELAXED, __HIP_MEMORY_SCOPE_AGENT);
        if (active && w != 0) { ISSUE_B(0); ISSUE_B(1); ISSUE_B(2); ISSUE_B(3); }
        STAMP(8);
        unsigned aa0 = L3_A + (4 * m3) * 1024 + lane * 16 + m3 * 8192, aa1 = aa0 ^ 8192;
        unsigned ba0[3], ba1[3];
#pragma unroll
        for (int i = 0; i < 3; ++i) {
            const unsigned mat = p4 >> 1;
            unsigned pos = 3 * n3 + i + (g & 1) + 2 * mat; if (pos >= 12) pos -= 12;
            const unsigned a = L3_B + mat * 6144 + ((8 * g + q4) * 12 + pos) * 16 + 8 * (p4 & 1);
            ba0[i] = a + m3 * L3_BSLOT; ba1[i] = a + (1 - m3) * L3_BSLOT;
            PIN(ba0[i]); PIN(ba1[i]);
        }
        unsigned vl16 = lane * 16;
        const unsigned sa3 = WS_H + w * 16384, sst = WS_ST + w * 2048;
        PIN(aa0); PIN(aa1); PIN(vl16);
        if (w == 0) wait_flags(flags + FL_2 * 32, 16, epoch, lane);
        BAR();
        STAMP(9);
        if (active && w == 0) { ISSUE_B(0); ISSUE_B(1); ISSUE_B(2); ISSUE_B(3); }
        if (tid == 0 && c == 0) __hip_atomic_store(flags + FL_GEN * 32, epoch, __ATOMIC_RELAXED, __HIP_MEMORY_SCOPE_AGENT);
        if (active) {
#define ISSUE_A(pr) do { const unsigned sa_ = sa3 + (((pr) + rot3) & 7) * 2048; DMA_SC1(wk, L3_A + ((pr) % 3) * 16384, vl16, rs_ws, sa_, 0); DMA_SC1(wk, L3_A + ((pr) % 3) * 16384 + 8192, vl16, rs_ws, sa_, 1024); } while (0)
            ISSUE_A(0); ISSUE_A(1); ISSUE_A(2);
            DMA_SC1(wk + wk, L3_ST, vl16, rs_ws, sst, 0); DMA_SC1(wk + wk, L3_ST + 1024, vl16, rs_ws, sst, 1024);
            f32x4 acc[4][3], cs[3] = {Z4, Z4, Z4};
#pragma unroll
            for (int t = 0; t < 4; ++t)
#pragma unroll
                for (int i = 0; i < 3; ++i) acc[t][i] = Z4;
            f32x2 st[4];
            f32x4 cvp[3];
            u32x4 af[2][4], bf[2][3];
            unsigned sa = L3_ST + (tid >> 8) * 8192 + (tid & 3) * 2048 + ((tid >> 2) & 63) * 8, sf = L3_ST + 512 + m3 * 8192 + r16 * 8;
            PIN(sa); PIN(sf);
#define LOAD3(s) do { const int pr_ = (s) >> 1, k2_ = (s) & 1; \
                bf[k2_][0] = tr_frag((k2_ ? ba1[0] : ba0[0]) + (pr_ & 3) * 2 * L3_BSLOT, 4 * 12 * 16); \
                _Pragma("unroll") for (int t = 0; t < 4; ++t) af[k2_][t] = lds128((k2_ ? aa1 : aa0) + (pr_ % 3) * 16384 + t * 1024); \
                _Pragma("unroll") for (int i = 1; i < 3; ++i) bf[k2_][i] = tr_frag((k2_ ? ba1[i] : ba0[i]) + (pr_ & 3) * 2 * L3_BSLOT, 4 * 12 * 16); } while (0)
#define JOB3(po, j) do { \
                if ((po) + 3 < 8 && (j) < 4) DMA_SC1(wk, L3_A + (((po) + 3) % 3) * 16384 + ((j) & 1) * 8192 + ((j) >> 1) * 4096, vl16, rs_ws, sa3 + (((po) + 3 + rot3) & 7) * 2048, ((j) & 1) * 1024 + ((j) >> 1) * 65536); \
                if ((po) + 4 < 8 && (j) >= 4 && (j) < 7) DMA(mg0[((j) >= 4 && (j) < 7) ? (j) - 4 : 0], (((po) + 4) & 3) * 2 * L3_BSLOT, vg0[((j) >= 4 && (j) < 7) ? (j) - 4 : 0], rs_g, (((po) + 4 + rot3) & 7) * (64 * FF * 2), 0); \
                if ((po) + 4 < 8 && (j) >= 7 && (j) < 10) DMA(mg0[((j) >= 7 && (j) < 10) ? (j) - 7 : 0], (((po) + 4) & 3) * 2 * L3_BSLOT + 6144, vg1[((j) >= 7 && (j) < 10) ? (j) - 7 : 0], rs_u, (((po) + 4 + rot3) & 7) * (64 * FF * 2), 0); \
                if ((po) >= 4) { const int jd_ = (po) == 4 ? (j) - 4 : (j); if (jd_ >= 0 && jd_ < 6) { const int s0_ = 12 + 6 * ((po) - 4) + 2 * ((jd_ >= 0 && jd_ < 6) ? jd_ >> 1 : 0); \
                    DMA(wk, s0_ * 4096 + (jd_ & 1) * 4096, vwd, rs_wd, swd, KS4(s0_) * 32768 + (jd_ & 1) * 32768); } } } while (0)
#define SLOT3(po, sl) do { __builtin_amdgcn_sched_barrier(0); if ((po) >= 0 && (po) < 7) { if (w < 4) { JOB3((po) >= 0 && (po) < 7 ? (po) : 0, 2 * (sl)); JOB3((po) >= 0 && (po) < 7 ? (po) : 0, 2 * (sl) + 1); } } __builtin_amdgcn_sched_barrier(0); } while (0)
            WAIT_VM(6);
            BAR();
            STAMP(10);
            LOAD3(0);
#pragma unroll
            for (int s = 0; s < 16; ++s) {
                const int po = (s - 1) >> 1, sl0 = (s & 1) ? 0 : 3;
                if (s & 1) {
                    if (w < 4) { if (s == 1) WAIT_VM(4); else if (s == 3) WAIT_VM(12); else if (s < 13) WAIT_VM(16); else if (s == 13) WAIT_VM(12); }
                    else { if (s == 1) WAIT_VM(4); else if (s == 3) WAIT_VM(2); else if (s == 5) WAIT_VM(0); }
                    BAR();
                }
                if (s == 15) {
                    ISSUE_S(); ISSUE_D2(0); ISSUE_D2(2); ISSUE_D2(4);
#pragma unroll
                    for (int i = 0; i < 3; ++i) cvp[i] = lds128f(L3_ST + 1024 + (3 * (w ^ 4) + i) * 64 + g * 16);
                }
                const int n_ = s + 1, pr_ = n_ >> 1, k2_ = n_ & 1, cb = s & 1, nb = n_ & 1;
                f32x2 pp[4];
                if (s < 15) {
                    bf[nb][0] = tr_frag((k2_ ? ba1[0] : ba0[0]) + (pr_ & 3) * 2 * L3_BSLOT, 4 * 12 * 16);
                    af[nb][0] = lds128((k2_ ? aa1 : aa0) + (pr_ % 3) * 16384); af[nb][1] = lds128((k2_ ? aa1 : aa0) + (pr_ % 3) * 16384 + 1024);
                }
#pragma unroll
                for (int t = 0; t < 4; ++t) acc[t][0] = mfma32(bf[cb][0], af[cb][t], acc[t][0]);
                if ((s & 1) == 0) cs[0] = mfma32(bf[cb][0], ONES, cs[0]);
#pragma unroll
                for (int i = 0; i < 4; ++i) { __builtin_amdgcn_sched_group_barrier(0x8, 1, 0); __builtin_amdgcn_sched_group_barrier(0x100, 1, 0); }
                SLOT3(po, sl0);
                if (s < 15) {
                    af[nb][2] = lds128((k2_ ? aa1 : aa0) + (pr_ % 3) * 16384 + 2048); af[nb][3] = lds128((k2_ ? aa1 : aa0) + (pr_ % 3) * 16384 + 3072);
                    bf[nb][1] = tr_frag((k2_ ? ba1[1] : ba0[1]) + (pr_ & 3) * 2 * L3_BSLOT, 4 * 12 * 16);
                }
#pragma unroll
                for (int t = 0; t < 4; ++t) acc[t][1] = mfma32(bf[cb][1], af[cb][t], acc[t][1]);
                if ((s & 1) == 0) cs[1] = mfma32(bf[cb][1], ONES, cs[1]);
#pragma unroll
                for (int i = 0; i < 4; ++i) { __builtin_amdgcn_sched_group_barrier(0x8, 1, 0); __builtin_amdgcn_sched_group_barrier(0x100, 1, 0); }
                SLOT3(po, sl0 + 1);
                if (s < 15) bf[nb][2] = tr_frag((k2_ ? ba1[2] : ba0[2]) + (pr_ & 3) * 2 * L3_BSLOT, 4 * 12 * 16);
                if (s == 6) { pp[0] = lds64f(sa); pp[1] = lds64f(sa + 512); pp[2] = lds64f(sa + 1024); pp[3] = lds64f(sa + 1536); }
                if (s == 12) {
#pragma unroll
                    for (int t = 0; t < 4; ++t) st[t] = lds64f(sf + t * 128);
                }
#pragma unroll
                for (int t = 0; t < 4; ++t) acc[t][2] = mfma32(bf[cb][2], af[cb][t], acc[t][2]);
                if ((s & 1) == 0) cs[2] = mfma32(bf[cb][2], ONES, cs[2]);
#pragma unroll
                for (int i = 0; i < 4; ++i) { __builtin_amdgcn_sched_group_barrier(0x8, 1, 0); __builtin_amdgcn_sched_group_barrier(0x100, 2, 0); }
                __builtin_amdgcn_sched_barrier(0);
                if (s == 6) {
                    const f32x2 sm = (pp[0] + pp[1]) + (pp[2] + pp[3]);
                    float sx = sm.x, sq = sm.y;
                    sx += __builtin_amdgcn_update_dpp(0.f, sx, 0xB1, 0xf, 0xf, false); sq += __builtin_amdgcn_update_dpp(0.f, sq, 0xB1, 0xf, 0xf, false);
                    sx += __builtin_amdgcn_update_dpp(0.f, sx, 0x4E, 0xf, 0xf, false); sq += __builtin_amdgcn_update_dpp(0.f, sq, 0x4E, 0xf, 0xf, false);
                    if ((lane & 3) == 0) *(LAS f32x2*)(sa + 512) = ln_stats(sx, sq);
                }
                if (s == 14 && r16 == 0) {
#pragma unroll
                    for (int i = 0; i < 3; ++i) *(LAS f32x4*)(L3_ST + 1024 + (3 * w + i) * 64 + g * 16) = cs[i];
                }
                SLOT3(po, sl0 + 2);
            }
            STAMP(11);
            const unsigned hi = lane >> 5;
            const float r01 = hi ? st[1].y : st[0].y, r23 = hi ? st[3].y : st[2].y;
            const float rr01 = r01 * r01, rr23 = r23 * r23;
            const unsigned tau = 2 * (g & 1) + (g >> 1);
#pragma unroll
            for (int i = 0; i < 3; ++i) {
                const f32x4 cv = cs[i] + cvp[i];
                u32x2 hp[2];
#pragma unroll
                for (int pp = 0; pp < 2; ++pp) {
                    const f32x4 e0 = acc[2 * pp][i] - st[2 * pp].x * cv, e1 = acc[2 * pp + 1][i] - st[2 * pp + 1].x * cv;
                    float hv[4];
#pragma unroll
                    for (int r = 0; r < 4; ++r) {
                        auto sw = __builtin_amdgcn_permlane32_swap(f2u(e0[r]), f2u(e1[r]), false, false);
                        const unsigned x0 = sw[0], x1 = sw[1];
                        hv[r] = __builtin_fmaxf(u2f(x0), 0.0f) * (u2f(x1) * (pp ? rr23 : rr01));
                    }
                    hp[pp] = (u32x2){pk_bf16(hv[0], hv[1]), pk_bf16(hv[2], hv[3])};
                }
                auto s0 = __builtin_amdgcn_permlane16_swap(hp[0].x, hp[1].x, false, false);
                auto s1 = __builtin_amdgcn_permlane16_swap(hp[0].y, hp[1].y, false, false);
                const u32x4 hf4 = {s0[0], s1[0], s0[1], s1[1]};
                const unsigned U = 12 * c + 3 * n3 + i;
                const unsigned soff = WS_HID + ((4 * m3) * NKS4 + (U >> 2)) * 1024 + (U & 3) * 256;
                const unsigned off = U < 172 ? tau * (NKS4 * 1024) + r16 * 16 + soff : 0xFFFFFFF0u;
                __builtin_amdgcn_raw_buffer_store_b128(hf4, rs_ws, off, 0, AUX_SC1);
            }
            asm volatile("" ::: "memory");
        }
    }
    STAMP(12);
    {
        const bool active = c < 15;
        if (!active) {
            ISSUE_D2(12); ISSUE_D2(14); ISSUE_D2(16); ISSUE_D2(18); ISSUE_D2(20); ISSUE_D2(22); ISSUE_D2(24); ISSUE_D2(26); ISSUE_D2(28);
            ISSUE_S();
            ISSUE_D2(0); ISSUE_D2(2); ISSUE_D2(4);
        }
        WAIT_VM(0);
        BAR();
        if (tid == 0 && active) __hip_atomic_store(flags + (FL_3 + c) * 32, epoch, __ATOMIC_RELAXED, __HIP_MEMORY_SCOPE_AGENT);
        STAMP(13);
#define REST_D() do { ISSUE_D2(6); ISSUE_D2(8); ISSUE_D2(10); ISSUE_D2(34); ISSUE_D2(36); ISSUE_D2(38); } while (0)
        if (w != 0) REST_D();
        const unsigned wb0 = (8 * g + q4) * 128 + (fsw * 32) + 8 * p4;
        u32x4 bq[11][4];
#pragma unroll
        for (int ct = 0; ct < 4; ++ct) {
            const u32x4 t0 = tr_frag((wb0 ^ (ct * 32)) + (30 + (kq4 < 3 ? kq4 : 0)) * 4096, 512);
            bq[0][ct] = kq4 < 3 ? t0 : (u32x4){0u, 0u, 0u, 0u};
        }
        BAR();
#define LAST_D() do { ISSUE_D2(30); DMA(wk, 32 * 4096, vwd, rs_wd, swd, 35 * 32768); } while (0)
        if (w != 0) LAST_D();
        unsigned wbk = wb0 + kq4 * (10 * 4096);
        unsigned abase = lane * 16;
        const unsigned asoff = WS_HID + ((4 * hf + 2 * rp4) * NKS4 + 11 * kq4 - (kq4 == 3 ? 1 : 0)) * 1024;
        PIN(wbk); PIN(abase);
        if (w == 0) wait_flags(flags + FL_3 * 32, 15, epoch, lane);
        BAR();
        STAMP(14);
        u32x4 af[2][11];
#define LOADA4(k) do { af[0][k] = __builtin_amdgcn_raw_buffer_load_b128(rs_ws, abase + ((k) & 3) * 1024, asoff + ((k) >> 2) * 4096, AUX_SC1); \
                       af[1][k] = __builtin_amdgcn_raw_buffer_load_b128(rs_ws, abase + ((k) & 3) * 1024, asoff + NKS4 * 1024 + ((k) >> 2) * 4096, AUX_SC1); } while (0)
        if (w == 0) { REST_D(); LAST_D(); }
        LOADA4(0); LOADA4(1); LOADA4(2);
        WAIT_VM(6);
        BAR();
#define LOAD4(j) do { _Pragma("unroll") for (int ct = 0; ct < 4; ++ct) bq[j][ct] = tr_frag((wbk ^ (ct * 32)) + ((j) - 1) * 4096, 512); } while (0)
        LOAD4(1); LOAD4(2);
        __builtin_amdgcn_sched_barrier(0);
        f32x4 acc[2][4] = {{Z4, Z4, Z4, Z4}, {Z4, Z4, Z4, Z4}};
#pragma unroll
        for (int j = 0; j < 11; ++j) {
            if (j + 3 < 11) { LOADA4(j + 3); LOAD4(j + 3); }
#pragma unroll
            for (int rl = 0; rl < 2; ++rl)
#pragma unroll
                for (int ct = 0; ct < 4; ++ct) acc[rl][ct] = mfma32(bq[j][ct], af[rl][j], acc[rl][ct]);
#pragma unroll
            for (int i = 0; i < 8; ++i) { __builtin_amdgcn_sched_group_barrier(0x8, 1, 0); __builtin_amdgcn_sched_group_barrier(0x100, 1, 0); }
            __builtin_amdgcn_sched_barrier(0);
        }
        STAMP(15);
        BAR();
        const unsigned red = w * 8192 + lane * 16;
#pragma unroll
        for (int rl = 0; rl < 2; ++rl)
#pragma unroll
            for (int ct = 0; ct < 4; ++ct) *(LAS f32x4*)(red + (rl * 4 + ct) * 1024) = acc[rl][ct];
        BAR();
        f32x4 a0 = Z4, a1 = Z4;
#pragma unroll
        for (int o = 0; o < 4; ++o) {
            const unsigned ra = (2 * rp4 + (o & 1) + 4 * (o >> 1)) * 8192 + lane * 16 + ((rt4 & 1) * 4 + 2 * kh) * 1024;
            a0 += lds128f(ra); a1 += lds128f(ra + 1024);
        }
        auto s0 = __builtin_amdgcn_permlane16_swap(pk_bf16(a0[0], a0[1]), pk_bf16(a1[0], a1[1]), false, false);
        auto s1 = __builtin_amdgcn_permlane16_swap(pk_bf16(a0[2], a0[3]), pk_bf16(a1[2], a1[3]), false, false);
        const u32x4 at = {s0[0], s1[0], s0[1], s1[1]};
        u32x4 o;
#pragma unroll
        for (int i = 0; i < 4; ++i) o[i] = pk_bf16(bf_lo(at[i]) + bf_lo(h_keep[i]), bf_hi(at[i]) + bf_hi(h_keep[i]));
        *(u32x4*)((char*)P.out + tok_off) = o;
    }
    STAMP(16);
}

}

extern "C" void kernel_launch(void* const* d_in, const int* in_sizes, int n_in, void* d_out, int out_size, void* d_ws, size_t ws_size, hipStream_t stream) {
    Params P;
    P.x = (const u16*)d_in[0]; P.wqkv = (const u16*)d_in[1]; P.wo = (const u16*)d_in[2];
    P.wg = (const u16*)d_in[4]; P.wu = (const u16*)d_in[5]; P.wd = (const u16*)d_in[6];
    P.out = (u16*)d_out; P.ws = (unsigned char*)d_ws;
    (void)hipFuncSetAttribute((const void*)fused_block, hipFuncAttributeMaxDynamicSharedMemorySize, LDS_BYTES);
    hipLaunchKernelGGL(fused_block, dim3(256), dim3(512), LDS_BYTES, stream, P);
}
```

```cpp
#include <hip/hip_runtime.h>
#include <hip/hip_bf16.h>
#include <stdint.h>
#pragma clang diagnostic ignored "-Wint-to-pointer-cast"

#define LAS __attribute__((address_space(3)))
#define GAS __attribute__((address_space(1)))
typedef unsigned short u16;
typedef unsigned long long u64;
typedef short s16x4 __attribute__((ext_vector_type(4)));
typedef __bf16 bf16x8 __attribute__((ext_vector_type(8)));
typedef __bf16 bf16x2 __attribute__((ext_vector_type(2)));
typedef float f32x4 __attribute__((ext_vector_type(4)));
typedef float f32x2 __attribute__((ext_vector_type(2)));
typedef unsigned u32x4 __attribute__((ext_vector_type(4)));
typedef unsigned u32x2 __attribute__((ext_vector_type(2)));
typedef GAS u64 gu64;
typedef __amdgpu_buffer_rsrc_t rsrc_t;

namespace {

constexpr int SEQ = 2048, DM = 512, FF = 1376;
constexpr int NKS4 = FF / 32;
constexpr unsigned WS_FLAG = 0;
constexpr unsigned WS_ST = 16384;
constexpr unsigned WS_O = 40960;
constexpr unsigned WS_H = WS_O + 131072;
constexpr unsigned WS_HID = WS_H + 131072;
constexpr unsigned WS_SPARE = WS_HID + 8 * 43 * 1024;
constexpr unsigned WS_BYTES = WS_SPARE + 8192;
constexpr int FL_GEN = 0, FL_1 = 8, FL_2 = 24, FL_3 = 40;
constexpr int AUX_SC1 = 16;
constexpr unsigned kSpinCap = 1u << 16;
constexpr unsigned CODE_KB = 24;

constexpr int LDS_BYTES = 163840;
constexpr int L1_X = 0, L1_H = 65536, L1_W = 81920, L1_WSLOT = 24576, L1_SCR = 155648;
constexpr int L2_WO = 81920, L2_RED = 114688;
constexpr int L3_A = 0, L3_B = 49152, L3_BSLOT = 12288, L3_ST = 147456;

__device__ __forceinline__ constexpr int vm_imm(int n) { return (n & 0xF) | ((n >> 4) << 14) | (0x7 << 4) | (0xF << 8); }
#define WAIT_VM(n) asm volatile("s_waitcnt vmcnt(%0)" :: "i"(n) : "memory")
#define BAR() do { asm volatile("s_waitcnt lgkmcnt(0)" ::: "memory"); __builtin_amdgcn_s_barrier(); asm volatile("" ::: "memory"); } while (0)
#define PIN(x) asm volatile("" : "+v"(x))
#define DMA(m0b, ldsoff, voff, rs, sbase, soffimm) do { unsigned t_; asm volatile("s_add_u32 m0, %1, %2\n\ts_add_u32 %0, %5, %6\n\tbuffer_load_dwordx4 %3, %4, %0 offen lds" \
    : "=&s"(t_) : "s"(m0b), "i"(ldsoff), "v"(voff), "s"(rs), "s"(sbase), "i"(soffimm) : "scc", "memory"); } while (0)
#define DMA_SC1(m0b, ldsoff, voff, rs, sbase, soffimm) do { unsigned t_; asm volatile("s_add_u32 m0, %1, %2\n\ts_add_u32 %0, %5, %6\n\tbuffer_load_dwordx4 %3, %4, %0 offen sc1 lds" \
    : "=&s"(t_) : "s"(m0b), "i"(ldsoff), "v"(voff), "s"(rs), "s"(sbase), "i"(soffimm) : "scc", "memory"); } while (0)

__device__ __forceinline__ rsrc_t mk_rsrc(const void* p, unsigned bytes) { return __builtin_amdgcn_make_buffer_rsrc((void*)p, 0, bytes, 0x00020000); }
__device__ __forceinline__ float u2f(unsigned u) { return __builtin_bit_cast(float, u); }
__device__ __forceinline__ unsigned f2u(float f) { return __builtin_bit_cast(unsigned, f); }
__device__ __forceinline__ unsigned pk_bf16(float a, float b) { bf16x2 v; v[0] = (__bf16)a; v[1] = (__bf16)b; return __builtin_bit_cast(unsigned, v); }
__device__ __forceinline__ float bf_lo(unsigned u) { return u2f(u << 16); }
__device__ __forceinline__ float bf_hi(unsigned u) { return u2f(u & 0xffff0000u); }
__device__ __forceinline__ float rbf(float f) { return (float)(__bf16)f; }
__device__ __forceinline__ f32x4 mfma32(u32x4 a, u32x4 b, f32x4 c) {
    return __builtin_amdgcn_mfma_f32_16x16x32_bf16(__builtin_bit_cast(bf16x8, a), __builtin_bit_cast(bf16x8, b), c, 0, 0, 0);
}
__device__ __forceinline__ f32x4 mfma16(u32x2 a, u32x2 b, f32x4 c) {
    return __builtin_amdgcn_mfma_f32_16x16x16bf16_1k(__builtin_bit_cast(s16x4, a), __builtin_bit_cast(s16x4, b), c, 0, 0, 0);
}
__device__ __forceinline__ u32x4 lds128(unsigned a) { return *(LAS u32x4*)a; }
__device__ __forceinline__ f32x4 lds128f(unsigned a) { return *(LAS f32x4*)a; }
__device__ __forceinline__ f32x2 lds64f(unsigned a) { return *(LAS f32x2*)a; }
__device__ __forceinline__ u32x2 tr_read(unsigned a) { return __builtin_bit_cast(u32x2, __builtin_amdgcn_ds_read_tr16_b64_v4i16((LAS s16x4*)a)); }
__device__ __forceinline__ u32x4 tr_frag(unsigned a, int step) { const u32x2 x = tr_read(a), y = tr_read(a + step); return (u32x4){x.x, x.y, y.x, y.y}; }
__device__ __forceinline__ float swap16_lo(float v) {
    const unsigned a = f2u(v); auto r = __builtin_amdgcn_permlane16_swap(a, a, false, false); const unsigned x = r[0]; return u2f(x);
}
__device__ __forceinline__ float xmax16_32(float v) {
    unsigned a = f2u(v);
    auto r = __builtin_amdgcn_permlane16_swap(a, a, false, false);
    const unsigned r0 = r[0], r1 = r[1];
    v = __builtin_fmaxf(u2f(r0), u2f(r1));
    a = f2u(v);
    auto s = __builtin_amdgcn_permlane32_swap(a, a, false, false);
    const unsigned s0 = s[0], s1 = s[1];
    return __builtin_fmaxf(u2f(s0), u2f(s1));
}
__device__ __forceinline__ float bcast_row0(float v) { return __builtin_amdgcn_update_dpp(0.f, v, 0x150, 0xf, 0xf, false); }
__device__ __forceinline__ float diag4(f32x4 d, unsigned lane) { const float a = (lane & 1) ? d[1] : d[0], b = (lane & 1) ? d[3] : d[2]; return (lane & 2) ? b : a; }
__device__ __forceinline__ f32x2 ln_stats(float s, float q) {
    const float mean = s * (1.0f / DM);
    const float var = __builtin_fmaxf(q * (1.0f / DM) - mean * mean, 0.0f);
    const float sd = rbf(__builtin_amdgcn_sqrtf(rbf(rbf(var) + 1e-5f)));
    return (f32x2){rbf(mean), __builtin_amdgcn_rcpf(sd)};
}
__device__ __forceinline__ void wait_flags(gu64* base, unsigned n, u64 epoch, unsigned lane) {
    for (unsigned spins = 0; spins < kSpinCap; ++spins) {
        u64 v = epoch;
        if (lane < n) v = __hip_atomic_load(base + lane * 32, __ATOMIC_RELAXED, __HIP_MEMORY_SCOPE_AGENT);
        if (__all(v == epoch)) break;
    }
    __builtin_amdgcn_fence(__ATOMIC_ACQUIRE, "wavefront");
}

struct Params { const u16* x; const u16* wqkv; const u16* wo; const u16* wg; const u16* wu; const u16* wd; u16* out; unsigned char* ws; };

#define STAMP(i) do {} while (0)

__global__ void __launch_bounds__(512, 2) __attribute__((amdgpu_waves_per_eu(2, 2))) fused_block(Params P
) {
    const unsigned tid = threadIdx.x, lane = tid & 63;
    const unsigned w = __builtin_amdgcn_readfirstlane(tid >> 6);
    const unsigned b = blockIdx.x, xcd = b & 7, jj = b >> 3;
    const unsigned p = 4 * (xcd >> 1) + (jj >> 3);
    const unsigned hf = (jj >> 2) & 1, hg = 4 * (xcd & 1) + (jj & 3);
    const unsigned c = 8 * hf + hg;
    const unsigned r16 = lane & 15, g = lane >> 4, l8 = lane >> 3, l7 = lane & 7, q4 = r16 >> 2, p4 = lane & 3;
    STAMP(0);
    {
        u64 pc; asm volatile("s_getpc_b64 %0" : "=s"(pc));
        const char* cp = (const char*)(pc & ~(u64)1023) + ((jj * 8 + w) % CODE_KB) * 1024 + lane * 16;
        asm volatile("s_mov_b32 m0, %0\n\ts_nop 0\n\tglobal_load_lds_dwordx4 %1, off" :: "s"(L1_SCR + 6144), "v"(cp) : "memory");
    }
    const unsigned wk = w * 1024;
    const rsrc_t rs_x = mk_rsrc(P.x, SEQ * DM * 2);
    const rsrc_t rs_w = mk_rsrc(P.wqkv, DM * 3 * DM * 2);
    unsigned char* const wsp = P.ws + (size_t)p * WS_BYTES;
    gu64* const flags = (gu64*)(wsp + WS_FLAG);
    const unsigned T0 = 128 * p + 64 * hf;
    STAMP(18);
    u32x4 ONES = {0x3F803F80u, 0x3F803F80u, 0x3F803F80u, 0x3F803F80u};
    PIN(ONES);
    const f32x4 Z4 = {0.f, 0.f, 0.f, 0.f};
    const unsigned fsw = ((q4 >> 1) & 1) | ((g & 1) << 1);
    const unsigned wsw = (l7 ^ (((g & 1) | ((w & 1) << 1)) << 1)) * 16;
    u64 gen = 0;
    const unsigned rt4 = w & 3, kh = w >> 2, rp4 = rt4 >> 1, kq4 = (rt4 & 1) + 2 * kh;
    const unsigned tok_off = (128 * p + 64 * hf + 32 * (rt4 & 1) + (rt4 >> 1) + 2 * r16) * 1024 + (64 * hg + 32 * kh + 16 * (g & 1) + 8 * (g >> 1)) * 2;
    u32x4 xr;

    {
        const unsigned rot = (2 * (jj & 3) + (jj >> 2) + (xcd >> 1)) & 7;
        const unsigned hh = w & 3, par = w >> 2;
        const unsigned vh = l8 * 2048 + ((l7 ^ g) * 16);
        const unsigned vxr = vh ^ ((w & 1) * 64);
        const unsigned sxr = (T0 + 32 * ((w >> 1) & 1) + (w >> 2) + 16 * (w & 1)) * 1024;
        const unsigned vw = (8 * w + l8) * 3072 + wsw + hg * 128;
        {
            const unsigned kap = (w + rot) & 7;
            const unsigned sh = (T0 == 0 ? 0 : T0 - 16) * 1024 + kap * 128;
            const unsigned hb = L1_H + w * 2048;
            if (lane >= 8) { DMA(hb, 0, vh, rs_x, sh, 0); const unsigned vh1 = vh ^ 64; DMA(hb, 1024, vh1, rs_x, sh, 1024); }
            else { *(LAS u32x4*)(hb + lane * 16) = ONES; *(LAS u32x4*)(hb + 1024 + lane * 16) = ONES; }
        }
        unsigned kx[8];
#pragma unroll
        for (int q = 0; q < 8; ++q) kx[q] = (q + rot) & 7;
#define TOUCH(ptr) asm volatile("s_mov_b32 m0, %0\n\ts_nop 0\n\tglobal_load_lds_dwordx4 %1, off" :: "s"(L1_SCR + 6144), "v"(ptr) : "memory")
#define ISSUE_X(q) DMA(wk, L1_X + (q) * 8192, vxr, rs_x, sxr + kx[q] * 128, 0)
#define ISSUE_W1(q, mat) DMA(wk, L1_W + ((q) % 3) * L1_WSLOT + (mat) * 8192, vw, rs_w, kx[q] * 196608, (mat) * 1024)
#define ISSUE_W(q) do { ISSUE_W1(q, 0); ISSUE_W1(q, 1); ISSUE_W1(q, 2); } while (0)
        ISSUE_X(0); ISSUE_W(0);
        __builtin_amdgcn_s_barrier();
        ISSUE_X(1); ISSUE_W(1);
        u64 genv;
        { const gu64* gp = flags + FL_GEN * 32; asm volatile("global_load_dwordx2 %0, %1, off sc1" : "=v"(genv) : "v"(gp) : "memory"); }
#define NTOUCH 4
        STAMP(17);
        const unsigned xa = (r16 >> 3) * 1024 + (r16 & 7) * 128 + ((g ^ ((r16 >> 1) & 7)) * 16);
        const unsigned xc0 = xa + par * 4096, xc1 = xc0 ^ 64;
        unsigned xh0 = xa + L1_H, xh1 = xh0 ^ 64;
        PIN(xh0); PIN(xh1);
        unsigned wa = L1_W + (8 * g + q4) * 128 + ((hh ^ fsw) * 32) + 8 * p4, wa2 = wa + 2 * L1_WSLOT;
        PIN(wa); PIN(wa2);
        const unsigned sxa = (xa ^ ((hh & 1) * 64));
        unsigned stc = sxa + par * 4096 + (hh >> 1) * 8192, sth = sxa + L1_H + (hh >> 1) * 2048;
        PIN(stc); PIN(sth);
        f32x4 mh, mp, mc;
#pragma unroll
        for (int r = 0; r < 4; ++r) {
            const unsigned kap = 4 * g + r;
            mh[r] = (T0 > 0 && (kap >> 3) == par && (kap & 7) >= r16 + 1) ? 0.f : -1e30f;
            mp[r] = (kap >= r16 + 9) ? 0.f : -1e30f;
            mc[r] = (kap <= r16 && r16 - kap <= 7) ? 0.f : -1e30f;
        }
        PIN(mh); PIN(mp); PIN(mc);

        f32x4 aq[3] = {Z4, Z4, Z4}, ak[3] = {Z4, Z4, Z4}, av[3] = {Z4, Z4, Z4};
        f32x4 gr[3] = {Z4, Z4, Z4}, rs[3] = {Z4, Z4, Z4};
        f32x2 sL[3]; f32x4 sR[3][2];
        const unsigned part = L1_SCR, fin = L1_SCR + 2560 + w * 384;
        u32x4 xf[2][3], wf[2][3], sf[2][3];
        const rsrc_t rs_wo = mk_rsrc(P.wo, DM * DM * 2);
        const unsigned vwo = (8 * w + l8) * 1024 + wsw + hg * 128;
#define LOAD1(s) do { const int kq_ = (s) >> 1, kk_ = (s) & 1; \
            xf[(s) & 1][0] = lds128((kk_ ? xh1 : xh0) + kq_ * 2048); xf[(s) & 1][1] = lds128((kk_ ? xc1 : xc0) + kq_ * 8192); xf[(s) & 1][2] = lds128((kk_ ? xc1 : xc0) + kq_ * 8192 + 2048); \
            _Pragma("unroll") for (int mat = 0; mat < 3; ++mat) wf[(s) & 1][mat] = tr_frag((kq_ % 3 == 2 ? wa2 : wa + (kq_ % 3) * L1_WSLOT) + mat * 8192 + kk_ * 4096, 512); } while (0)
#define X2(q, h) DMA(wk, L1_X + (q) * 8192 + (h) * 4096, vxr, rs_x, sxr + kx[q] * 128, (h) * 1024)
#define W2(q, mat, h) DMA(wk, L1_W + ((q) % 3) * L1_WSLOT + (mat) * 8192 + (h) * 4096, vw, rs_w, kx[q] * 196608, (mat) * 1024 + (h) * 98304)
#define O2(lds, m, h) DMA(wk, (lds) + (h) * 4096, vwo, rs_wo, 0u, (m) * 65536 + (h) * 32768)
#define JOB1(it, j) do { const int pc_ = (j) >> 1, h_ = (j) & 1; \
            if ((it) == 0) { if (pc_ == 0) X2(2, h_); else if (pc_ < 4) W2(2, (pc_ >= 1 && pc_ < 4) ? pc_ - 1 : 0, h_); } \
            if ((it) >= 1 && (it) <= 5 && pc_ < 3) W2(((it) >= 1 && (it) <= 5) ? (it) + 2 : 0, pc_ < 3 ? pc_ : 0, h_); \
            if ((it) == 1 && pc_ >= 3 && pc_ < 6) X2((pc_ >= 3 && pc_ < 6) ? pc_ : 0, h_); \
            if ((it) == 2 && pc_ >= 3 && pc_ < 5) X2((pc_ >= 3 && pc_ < 5) ? pc_ + 3 : 0, h_); \
            if ((it) == 6) { if (pc_ < 2) O2(L2_WO + (6 + (pc_ < 2 ? pc_ : 0)) * 8192, 6 + (pc_ < 2 ? pc_ : 0), h_); else if (pc_ < 5) O2(((pc_ >= 2 && pc_ < 5) ? pc_ - 2 : 0) * 8192, 3 + ((pc_ >= 2 && pc_ < 5) ? pc_ - 2 : 0), h_); } \
            if ((it) == 7 && pc_ < 3) O2(L2_WO + (pc_ < 3 ? pc_ : 0) * 8192, pc_ < 3 ? pc_ : 0, h_); } while (0)
#define SLOT1(it, sl) do { __builtin_amdgcn_sched_barrier(0); if (w < 4) { JOB1(it, 2 * (sl)); JOB1(it, 2 * (sl) + 1); } __builtin_amdgcn_sched_barrier(0); } while (0)
        WAIT_VM(5);
        BAR();
        STAMP(1);
        LOAD1(0);
#pragma unroll
        for (int s = 0; s < 16; ++s) {
            const int it = (s + 1) / 2, sl0 = (s & 1) ? 0 : 3;
            if (s & 1) {
                if (w < 4) {
                    if (s == 1) WAIT_VM(9 + NTOUCH); else if (s == 3) WAIT_VM(12 + NTOUCH); else if (s == 5) WAIT_VM(14); else if (s == 7) WAIT_VM(10); else if (s < 13) WAIT_VM(6); else if (s == 13) WAIT_VM(11);
                } else {
                    if (s == 1) WAIT_VM(1 + NTOUCH); else if (s == 9) WAIT_VM(0);
                }
                if (s < 15) BAR();
                if (s == 9) { asm volatile("" : "+v"(genv)); gen = ((u64)(unsigned)__builtin_amdgcn_readfirstlane((unsigned)(genv >> 32)) << 32) | (unsigned)__builtin_amdgcn_readfirstlane((unsigned)genv); }
            }
            const int n_ = s + 1, kq_ = n_ >> 1, kk_ = n_ & 1, cb = s & 1, nb = n_ & 1;
            const unsigned wsl = (kq_ % 3 == 2 ? wa2 : wa + (kq_ % 3) * L1_WSLOT) + kk_ * 4096;
            if (s < 15) { xf[nb][0] = lds128((kk_ ? xh1 : xh0) + kq_ * 2048); xf[nb][1] = lds128((kk_ ? xc1 : xc0) + kq_ * 8192); xf[nb][2] = lds128((kk_ ? xc1 : xc0) + kq_ * 8192 + 2048); }
#pragma unroll
            for (int t = 0; t < 3; ++t) aq[t] = mfma32(wf[cb][0], xf[cb][t], aq[t]);
#pragma unroll
            for (int i = 0; i < 3; ++i) { __builtin_amdgcn_sched_group_barrier(0x8, 1, 0); __builtin_amdgcn_sched_group_barrier(0x100, 1, 0); }
            if (s == 0) { __builtin_amdgcn_sched_barrier(0); if (w < 4) { JOB1(0, 0); JOB1(0, 1); JOB1(0, 2); } __builtin_amdgcn_sched_barrier(0); } else SLOT1(it, sl0);
            if (s < 15) { wf[nb][0] = tr_frag(wsl, 512); wf[nb][1] = tr_frag(wsl + 8192, 512); }
#pragma unroll
            for (int t = 0; t < 3; ++t) ak[t] = mfma32(wf[cb][1], xf[cb][t], ak[t]);
#pragma unroll
            for (int i = 0; i < 3; ++i) { __builtin_amdgcn_sched_group_barrier(0x8, 1, 0); __builtin_amdgcn_sched_group_barrier(0x100, 2, 0); }
            if (s == 0) { __builtin_amdgcn_sched_barrier(0); if (w < 4) { JOB1(0, 3); JOB1(0, 4); JOB1(0, 5); } __builtin_amdgcn_sched_barrier(0); } else SLOT1(it, sl0 + 1);
            if (s < 15) wf[nb][2] = tr_frag(wsl + 16384, 512);
            if (s >= 6 && s <= 9) { const int j = s - 6; sf[j & 1][0] = lds128(sth + j * 4096); sf[j & 1][1] = lds128(stc + j * 16384); sf[j & 1][2] = lds128(stc + j * 16384 + 2048); }
            f32x2 pp[4];
            if (s == 11) {
                const unsigned pa = part + ((g == 0 ? 0 : (g == 3 ? 0 : 2 * par + g)) * 64 + r16) * 8;
                pp[0] = lds64f(pa); pp[1] = lds64f(pa + 128); pp[2] = lds64f(pa + 256); pp[3] = lds64f(pa + 384);
            }
            if (s == 13) {
#pragma unroll
                for (int t = 0; t < 3; ++t) { sL[t] = lds64f(fin + (t * 16 + r16) * 8); sR[t][0] = lds128f(fin + (t * 16 + 4 * g) * 8); sR[t][1] = lds128f(fin + (t * 16 + 4 * g) * 8 + 16); }
            }
#pragma unroll
            for (int t = 0; t < 3; ++t) av[t] = mfma32(xf[cb][t], wf[cb][2], av[t]);
            if (s >= 7 && s <= 10) {
#pragma unroll
                for (int t = 0; t < 3; ++t) { gr[t] = mfma32(sf[(s - 7) & 1][t], sf[(s - 7) & 1][t], gr[t]); rs[t] = mfma32(ONES, sf[(s - 7) & 1][t], rs[t]); }
            }
#pragma unroll
            for (int i = 0; i < 9; ++i) { __builtin_amdgcn_sched_group_barrier(0x8, 1, 0); __builtin_amdgcn_sched_group_barrier(0x100, 1, 0); }
            __builtin_amdgcn_sched_barrier(0);
            if (s == 10 && g == q4) {
                *(LAS f32x2*)(part + (hh * 16 + r16) * 8) = (f32x2){rs[0][0], diag4(gr[0], lane)};
                *(LAS f32x2*)(part + (((1 + 2 * par) * 4 + hh) * 16 + r16) * 8) = (f32x2){rs[1][0], diag4(gr[1], lane)};
                *(LAS f32x2*)(part + (((2 + 2 * par) * 4 + hh) * 16 + r16) * 8) = (f32x2){rs[2][0], diag4(gr[2], lane)};
            }
            if (s == 11) {
                const f32x2 sm = (pp[0] + pp[1]) + (pp[2] + pp[3]);
                const f32x2 res = ln_stats(sm.x, sm.y);
                if (g < 3) *(LAS f32x2*)(fin + (g * 16 + r16) * 8) = res;
            }
            if (s == 0) { __builtin_amdgcn_sched_barrier(0); if (w < 4) { JOB1(0, 6); JOB1(0, 7); } __builtin_amdgcn_sched_barrier(0); } else SLOT1(it, sl0 + 2);
            if (s == 0) {
            const char* t1 = lane < 22 ? (const char*)P.wg + lane * 65536 : (lane < 44 ? (const char*)P.wu + (lane - 22) * 65536 : (const char*)P.wd + (lane - 44) * 65536);
            const char* t2 = lane < 8 ? (const char*)P.wo + lane * 65536 : (lane < 32 ? (const char*)P.out + p * 131072 + (lane - 8) * 4096 : (const char*)P.wd + (20 + (lane & 1)) * 65536);
            const char* tw = w == 0 ? (const char*)wsp + WS_FLAG + lane * 256 : (w == 1 ? t1 : (w == 2 ? t2 : (const char*)wsp + ((w & 1) ? WS_SPARE : 32768u) + lane * 16));
            TOUCH(tw);
#pragma unroll
            for (int i = 0; i < 3; ++i) {
                unsigned q = b * 24 + w * 3 + i; if (q >= 4640) q -= 4640;
                const char* base = q < 512 ? (const char*)P.wo + q * 1024 : (q < 1888 ? (const char*)P.wg + (q - 512) * 1024 : (q < 3264 ? (const char*)P.wu + (q - 1888) * 1024 : (const char*)P.wd + (q - 3264) * 1024));
                const char* t3 = base + lane * 16;
                TOUCH(t3);
            }

        }
            if (s == 12) asm volatile("buffer_load_dwordx4 %0, %1, %2, 0 offen" : "=v"(xr) : "v"(tok_off), "s"(rs_x) : "memory");
        }
        STAMP(2);
        f32x4 csq, csk;
#pragma unroll
        for (int r = 0; r < 4; ++r) { csq[r] = bcast_row0(aq[0][r]); csk[r] = bcast_row0(ak[0][r]); }
        const float csv = swap16_lo(av[0][0]);
        u32x2 qb[2], kb[3], vb[3];
#pragma unroll
        for (int t = 0; t < 3; ++t) {
            const f32x4 kv = (ak[t] - sL[t].x * csk) * sL[t].y;
            kb[t] = (u32x2){pk_bf16(kv[0], kv[1]), pk_bf16(kv[2], kv[3])};
            if (t > 0) { const f32x4 qv = (aq[t] - sL[t].x * csq) * sL[t].y; qb[t - 1] = (u32x2){pk_bf16(qv[0], qv[1]), pk_bf16(qv[2], qv[3])}; }
            const f32x4 mn = {sR[t][0][0], sR[t][0][2], sR[t][1][0], sR[t][1][2]}, rd = {sR[t][0][1], sR[t][0][3], sR[t][1][1], sR[t][1][3]};
            const f32x4 vv = (av[t] - mn * csv) * rd;
            vb[t] = (u32x2){pk_bf16(vv[0], vv[1]), pk_bf16(vv[2], vv[3])};
        }
        const float SC = 0.25f * 1.44269504088896f;
        u32x2 ob[2];
#pragma unroll
        for (int t = 0; t < 2; ++t) {
            f32x4 sp = mfma16(kb[t], qb[t], t == 0 ? mh : mp), sc = mfma16(kb[t + 1], qb[t], mc);
            float mx = __builtin_fmaxf(__builtin_fmaxf(__builtin_fmaxf(sp[0], sp[1]), __builtin_fmaxf(sp[2], sp[3])), __builtin_fmaxf(__builtin_fmaxf(sc[0], sc[1]), __builtin_fmaxf(sc[2], sc[3])));
            mx = xmax16_32(mx);
            const float mb = -mx * SC;
#pragma unroll
            for (int r = 0; r < 4; ++r) { sp[r] = __builtin_amdgcn_exp2f(sp[r] * SC + mb); sc[r] = __builtin_amdgcn_exp2f(sc[r] * SC + mb); }
            const u32x4 pf = {pk_bf16(sp[0], sp[1]), pk_bf16(sp[2], sp[3]), pk_bf16(sc[0], sc[1]), pk_bf16(sc[2], sc[3])};
            const u32x4 vf = {vb[t].x, vb[t].y, vb[t + 1].x, vb[t + 1].y};
            const f32x4 sm = mfma32(ONES, pf, Z4);
            const f32x4 o = mfma32(vf, pf, Z4) * __builtin_amdgcn_rcpf(sm[0]);
            ob[t] = (u32x2){pk_bf16(o[0], o[1]), pk_bf16(o[2], o[3])};
        }
        auto s0 = __builtin_amdgcn_permlane16_swap(ob[0].x, ob[1].x, false, false);
        auto s1 = __builtin_amdgcn_permlane16_swap(ob[0].y, ob[1].y, false, false);
        const u32x4 o_frag = {s0[0], s1[0], s0[1], s1[1]};
        const rsrc_t rs_ws1 = mk_rsrc(wsp, WS_BYTES);
        const unsigned off = ((g & 1) * 16384) + ((2 * (hh & 1) + (g >> 1)) * 16 + r16) * 16;
        const unsigned soff = WS_O + ((4 * hf + 2 * par) * 16 + 2 * hg + (hh >> 1)) * 1024;
        __builtin_amdgcn_raw_buffer_store_b128(o_frag, rs_ws1, off + soff, 0, AUX_SC1);
        asm volatile("" ::: "memory");
    }
    STAMP(3);
    const rsrc_t rs_ws = mk_rsrc(wsp, WS_BYTES);
    const u64 epoch = gen + 1;
    const unsigned n3 = w & 3, m3 = w >> 2;
    const rsrc_t rs_g = mk_rsrc(P.wg, DM * FF * 2), rs_u = mk_rsrc(P.wu, DM * FF * 2);
    const rsrc_t rs_gu = mk_rsrc(m3 ? P.wu : P.wg, DM * FF * 2);
    unsigned vg0[3], vg1[3];
#pragma unroll
    for (int i = 0; i < 3; ++i) {
        const unsigned s = (n3 * 3 + i) * 64 + lane;
        const unsigned ks2 = s >= 384 ? 1 : 0, s2 = s - ks2 * 384, row = (s2 * 43691u) >> 19, pos = s2 - row * 12;
        const int umax = (int)(c < 15 ? 171 - 12 * c : 0);
        int u0 = (int)pos - (int)((row >> 3) & 1); if (u0 < 0) u0 += 12;
        int u1 = (int)pos - (int)(((row >> 3) & 1) + 2); if (u1 < 0) u1 += 12;
        const unsigned rb = (ks2 * 32 + row) * (FF * 2) + c * 192;
        vg0[i] = rb + min(u0, umax) * 16; vg1[i] = rb + min(u1, umax) * 16;
    }
    unsigned vgu[3];
#pragma unroll
    for (int i = 0; i < 3; ++i) vgu[i] = m3 ? vg1[i] : vg0[i];
    unsigned mg0[3], mgu[3];
#pragma unroll
    for (int i = 0; i < 3; ++i) { const unsigned pi = n3 * 3 + i; mg0[i] = L3_B + (pi >= 6 ? L3_BSLOT + (pi - 6) * 1024 : pi * 1024); mgu[i] = mg0[i] + m3 * 6144; }
    const unsigned rot3 = (2 * (jj >> 3) + (jj & 7)) & 7;
#define ISSUE_B(pr) do { const unsigned sb_ = (((pr) + rot3) & 7) * (64 * FF * 2); _Pragma("unroll") for (int i_ = 0; i_ < 3; ++i_) DMA(mgu[i_], ((pr) & 3) * 2 * L3_BSLOT, vgu[i_], rs_gu, sb_, 0); } while (0)

    u32x4 h_keep;
    {
        WAIT_VM(0);
        BAR();
        if (tid == 0) __hip_atomic_store(flags + (FL_1 + c) * 32, epoch, __ATOMIC_RELAXED, __HIP_MEMORY_SCOPE_AGENT);
        STAMP(4);
        asm volatile("" : "+v"(xr));
        const unsigned wl = (8 * g + q4) * 128 + (fsw * 32) + 8 * p4;
        const unsigned wb0 = wl + (kh ? 8192 : L2_WO), wb1 = wl + (kh ? L2_WO + 49152 : L2_WO + 16384), wb2 = wl + (kh ? L2_WO + 57344 : 0);
        unsigned abase = lane * 16;
        const unsigned asoff = WS_O + ((4 * hf + rt4) * 16 + 8 * kh) * 1024;
        PIN(abase);
        u32x4 bf[8][4];
#pragma unroll
        for (int k = 0; k < 8; ++k)
#pragma unroll
            for (int ct = 0; ct < 4; ++ct) bf[k][ct] = tr_frag(((k < 4 ? wb0 : (k < 6 ? wb1 : wb2)) ^ (ct * 32)) + (k < 4 ? k : (k < 6 ? k - 4 : k - 6)) * 4096, 512);
        __builtin_amdgcn_sched_barrier(0);
        STAMP(19);
        if (w == 0) wait_flags(flags + (FL_1 + 8 * hf) * 32, 8, epoch, lane);
        BAR();
        STAMP(5);
        u32x4 af[8];
#pragma unroll
        for (int k = 0; k < 4; ++k) af[k] = __builtin_amdgcn_raw_buffer_load_b128(rs_ws, abase + (k & 3) * 1024, asoff + (k >> 2) * 4096, AUX_SC1);
        __builtin_amdgcn_sched_barrier(0);
        f32x4 acc[4] = {Z4, Z4, Z4, Z4};
#pragma unroll
        for (int k = 0; k < 8; ++k) {
            if (k + 4 < 8) af[k + 4] = __builtin_amdgcn_raw_buffer_load_b128(rs_ws, abase + (k & 3) * 1024, asoff + 4096, AUX_SC1);
            __builtin_amdgcn_sched_barrier(0);
#pragma unroll
            for (int ct = 0; ct < 4; ++ct) acc[ct] = mfma32(bf[k][ct], af[k], acc[ct]);
            __builtin_amdgcn_sched_barrier(0);
        }
        STAMP(6);
        const unsigned red = L2_RED + w * 2048 + lane * 16;
        *(LAS f32x4*)red = kh ? acc[0] : acc[2];
        *(LAS f32x4*)(red + 1024) = kh ? acc[1] : acc[3];
        BAR();
        const f32x4 a0 = (kh ? acc[2] : acc[0]) + lds128f(red ^ 8192);
        const f32x4 a1 = (kh ? acc[3] : acc[1]) + lds128f((red ^ 8192) + 1024);
        auto s0 = __builtin_amdgcn_permlane16_swap(pk_bf16(a0[0], a0[1]), pk_bf16(a1[0], a1[1]), false, false);
        auto s1 = __builtin_amdgcn_permlane16_swap(pk_bf16(a0[2], a0[3]), pk_bf16(a1[2], a1[3]), false, false);
        const u32x4 at = {s0[0], s1[0], s0[1], s1[1]};
#pragma unroll
        for (int i = 0; i < 4; ++i) h_keep[i] = pk_bf16(bf_lo(at[i]) + bf_lo(xr[i]), bf_hi(at[i]) + bf_hi(xr[i]));
        const unsigned hoff = ((2 * (g & 1) + (g >> 1)) * 16 + r16) * 16;
        const unsigned hsoff = WS_H + ((4 * hf + rt4) * 16 + 2 * hg + kh) * 1024;
        __builtin_amdgcn_raw_buffer_store_b128(h_keep, rs_ws, hoff + hsoff, 0, AUX_SC1);
        const f32x4 gs = mfma32(h_keep, h_keep, Z4), sm = mfma32(ONES, h_keep, Z4);
        const u32x2 stv = {f2u(sm[0]), f2u(diag4(gs, lane))};
        const unsigned stoff = (g == q4) ? WS_ST + c * 1024 + w * 128 + r16 * 8 : 0xFFFFFFF0u;
        __builtin_amdgcn_raw_buffer_store_b64(stv, rs_ws, stoff, 0, AUX_SC1);
        asm volatile("" ::: "memory");
    }
    STAMP(7);
    const rsrc_t rs_wd = mk_rsrc(P.wd, FF * DM * 2);
    const unsigned vwd = l8 * 1024 + wsw + hg * 128;
#define KS4(s) (11 * ((s) / 10) + (s) % 10 + 1 - ((s) >= 30 ? 1 : 0))
    const unsigned swd = ((w >> 2) * 32 + (w & 3) * 8) * 1024;
#define ISSUE_D2(s0) DMA(wk, (s0) * 4096, vwd, rs_wd, swd, KS4(s0) * 32768)
    const unsigned swd0 = (w & 3) * 8 * 1024;
#define ISSUE_S() do { DMA(wk, 30 * 4096, vwd, rs_wd, swd0 + (kh ? 11 : 0) * 32768, 0); DMA(wk, 32 * 4096, vwd, rs_wd, swd0 + (kh ? 36 : 22) * 32768, 0); } while (0)
    {
        const bool active = c < 15;
        WAIT_VM(0);
        BAR();
        if (tid == 0) __hip_atomic_store(flags + (FL_2 + c) * 32, epoch, __ATOMIC_RELAXED, __HIP_MEMORY_SCOPE_AGENT);
        if (active && w != 0) { ISSUE_B(0); ISSUE_B(1); ISSUE_B(2); ISSUE_B(3); }
        STAMP(8);
        unsigned aa0 = L3_A + (4 * m3) * 1024 + lane * 16 + m3 * 8192, aa1 = aa0 ^ 8192;
        unsigned ba0[3], ba1[3];
#pragma unroll
        for (int i = 0; i < 3; ++i) {
            const unsigned mat = p4 >> 1;
            unsigned pos = 3 * n3 + i + (g & 1) + 2 * mat; if (pos >= 12) pos -= 12;
            const unsigned a = L3_B + mat * 6144 + ((8 * g + q4) * 12 + pos) * 16 + 8 * (p4 & 1);
            ba0[i] = a + m3 * L3_BSLOT; ba1[i] = a + (1 - m3) * L3_BSLOT;
            PIN(ba0[i]); PIN(ba1[i]);
        }
        unsigned vl16 = lane * 16;
        const unsigned sa3 = WS_H + w * 16384, sst = WS_ST + w * 2048;
        PIN(aa0); PIN(aa1); PIN(vl16);
        if (w == 0) wait_flags(flags + FL_2 * 32, 16, epoch, lane);
        BAR();
        STAMP(9);
        if (active && w == 0) { ISSUE_B(0); ISSUE_B(1); ISSUE_B(2); ISSUE_B(3); }
        if (tid == 0 && c == 0) __hip_atomic_store(flags + FL_GEN * 32, epoch, __ATOMIC_RELAXED, __HIP_MEMORY_SCOPE_AGENT);
        if (active) {
#define ISSUE_A(pr) do { const unsigned sa_ = sa3 + (((pr) + rot3) & 7) * 2048; DMA_SC1(wk, L3_A + ((pr) % 3) * 16384, vl16, rs_ws, sa_, 0); DMA_SC1(wk, L3_A + ((pr) % 3) * 16384 + 8192, vl16, rs_ws, sa_, 1024); } while (0)
            ISSUE_A(0); ISSUE_A(1); ISSUE_A(2);
            DMA_SC1(wk + wk, L3_ST, vl16, rs_ws, sst, 0); DMA_SC1(wk + wk, L3_ST + 1024, vl16, rs_ws, sst, 1024);
            f32x4 acc[4][3], cs[3] = {Z4, Z4, Z4};
#pragma unroll
            for (int t = 0; t < 4; ++t)
#pragma unroll
                for (int i = 0; i < 3; ++i) acc[t][i] = Z4;
            f32x2 st[4];
            f32x4 cvp[3];
            u32x4 af[2][4], bf[2][3];
            unsigned sa = L3_ST + (tid >> 8) * 8192 + (tid & 3) * 2048 + ((tid >> 2) & 63) * 8, sf = L3_ST + 512 + m3 * 8192 + r16 * 8;
            PIN(sa); PIN(sf);
#define LOAD3(s) do { const int pr_ = (s) >> 1, k2_ = (s) & 1; \
                bf[k2_][0] = tr_frag((k2_ ? ba1[0] : ba0[0]) + (pr_ & 3) * 2 * L3_BSLOT, 4 * 12 * 16); \
                _Pragma("unroll") for (int t = 0; t < 4; ++t) af[k2_][t] = lds128((k2_ ? aa1 : aa0) + (pr_ % 3) * 16384 + t * 1024); \
                _Pragma("unroll") for (int i = 1; i < 3; ++i) bf[k2_][i] = tr_frag((k2_ ? ba1[i] : ba0[i]) + (pr_ & 3) * 2 * L3_BSLOT, 4 * 12 * 16); } while (0)
#define JOB3(po, j) do { \
                if ((po) + 3 < 8 && (j) < 4) DMA_SC1(wk, L3_A + (((po) + 3) % 3) * 16384 + ((j) & 1) * 8192 + ((j) >> 1) * 4096, vl16, rs_ws, sa3 + (((po) + 3 + rot3) & 7) * 2048, ((j) & 1) * 1024 + ((j) >> 1) * 65536); \
                if ((po) + 4 < 8 && (j) >= 4 && (j) < 7) DMA(mg0[((j) >= 4 && (j) < 7) ? (j) - 4 : 0], (((po) + 4) & 3) * 2 * L3_BSLOT, vg0[((j) >= 4 && (j) < 7) ? (j) - 4 : 0], rs_g, (((po) + 4 + rot3) & 7) * (64 * FF * 2), 0); \
                if ((po) + 4 < 8 && (j) >= 7 && (j) < 10) DMA(mg0[((j) >= 7 && (j) < 10) ? (j) - 7 : 0], (((po) + 4) & 3) * 2 * L3_BSLOT + 6144, vg1[((j) >= 7 && (j) < 10) ? (j) - 7 : 0], rs_u, (((po) + 4 + rot3) & 7) * (64 * FF * 2), 0); \
                if ((po) >= 4) { const int jd_ = (po) == 4 ? (j) - 4 : (j); if (jd_ >= 0 && jd_ < 6) { const int s0_ = 12 + 6 * ((po) - 4) + 2 * ((jd_ >= 0 && jd_ < 6) ? jd_ >> 1 : 0); \
                    DMA(wk, s0_ * 4096 + (jd_ & 1) * 4096, vwd, rs_wd, swd, KS4(s0_) * 32768 + (jd_ & 1) * 32768); } } } while (0)
#define SLOT3(po, sl) do { __builtin_amdgcn_sched_barrier(0); if ((po) >= 0 && (po) < 7) { if (w < 4) { JOB3((po) >= 0 && (po) < 7 ? (po) : 0, 2 * (sl)); JOB3((po) >= 0 && (po) < 7 ? (po) : 0, 2 * (sl) + 1); } } __builtin_amdgcn_sched_barrier(0); } while (0)
            WAIT_VM(6);
            BAR();
            STAMP(10);
            LOAD3(0);
#pragma unroll
            for (int s = 0; s < 16; ++s) {
                const int po = (s - 1) >> 1, sl0 = (s & 1) ? 0 : 3;
                if (s & 1) {
                    if (w < 4) { if (s == 1) WAIT_VM(4); else if (s == 3) WAIT_VM(12); else if (s < 13) WAIT_VM(16); else if (s == 13) WAIT_VM(12); }
                    else { if (s == 1) WAIT_VM(4); else if (s == 3) WAIT_VM(2); else if (s == 5) WAIT_VM(0); }
                    BAR();
                }
                if (s == 15) {
                    ISSUE_S(); ISSUE_D2(0); ISSUE_D2(2); ISSUE_D2(4);
#pragma unroll
                    for (int i = 0; i < 3; ++i) cvp[i] = lds128f(L3_ST + 1024 + (3 * (w ^ 4) + i) * 64 + g * 16);
                }
                const int n_ = s + 1, pr_ = n_ >> 1, k2_ = n_ & 1, cb = s & 1, nb = n_ & 1;
                f32x2 pp[4];
                if (s < 15) {
                    bf[nb][0] = tr_frag((k2_ ? ba1[0] : ba0[0]) + (pr_ & 3) * 2 * L3_BSLOT, 4 * 12 * 16);
                    af[nb][0] = lds128((k2_ ? aa1 : aa0) + (pr_ % 3) * 16384); af[nb][1] = lds128((k2_ ? aa1 : aa0) + (pr_ % 3) * 16384 + 1024);
                }
#pragma unroll
                for (int t = 0; t < 4; ++t) acc[t][0] = mfma32(bf[cb][0], af[cb][t], acc[t][0]);
                if ((s & 1) == 0) cs[0] = mfma32(bf[cb][0], ONES, cs[0]);
#pragma unroll
                for (int i = 0; i < 4; ++i) { __builtin_amdgcn_sched_group_barrier(0x8, 1, 0); __builtin_amdgcn_sched_group_barrier(0x100, 1, 0); }
                SLOT3(po, sl0);
                if (s < 15) {
                    af[nb][2] = lds128((k2_ ? aa1 : aa0) + (pr_ % 3) * 16384 + 2048); af[nb][3] = lds128((k2_ ? aa1 : aa0) + (pr_ % 3) * 16384 + 3072);
                    bf[nb][1] = tr_frag((k2_ ? ba1[1] : ba0[1]) + (pr_ & 3) * 2 * L3_BSLOT, 4 * 12 * 16);
                }
#pragma unroll
                for (int t = 0; t < 4; ++t) acc[t][1] = mfma32(bf[cb][1], af[cb][t], acc[t][1]);
                if ((s & 1) == 0) cs[1] = mfma32(bf[cb][1], ONES, cs[1]);
#pragma unroll
                for (int i = 0; i < 4; ++i) { __builtin_amdgcn_sched_group_barrier(0x8, 1, 0); __builtin_amdgcn_sched_group_barrier(0x100, 1, 0); }
                SLOT3(po, sl0 + 1);
                if (s < 15) bf[nb][2] = tr_frag((k2_ ? ba1[2] : ba0[2]) + (pr_ & 3) * 2 * L3_BSLOT, 4 * 12 * 16);
                if (s == 6) { pp[0] = lds64f(sa); pp[1] = lds64f(sa + 512); pp[2] = lds64f(sa + 1024); pp[3] = lds64f(sa + 1536); }
                if (s == 12) {
#pragma unroll
                    for (int t = 0; t < 4; ++t) st[t] = lds64f(sf + t * 128);
                }
#pragma unroll
                for (int t = 0; t < 4; ++t) acc[t][2] = mfma32(bf[cb][2], af[cb][t], acc[t][2]);
                if ((s & 1) == 0) cs[2] = mfma32(bf[cb][2], ONES, cs[2]);
#pragma unroll
                for (int i = 0; i < 4; ++i) { __builtin_amdgcn_sched_group_barrier(0x8, 1, 0); __builtin_amdgcn_sched_group_barrier(0x100, 2, 0); }
                __builtin_amdgcn_sched_barrier(0);
                if (s == 6) {
                    const f32x2 sm = (pp[0] + pp[1]) + (pp[2] + pp[3]);
                    float sx = sm.x, sq = sm.y;
                    sx += __builtin_amdgcn_update_dpp(0.f, sx, 0xB1, 0xf, 0xf, false); sq += __builtin_amdgcn_update_dpp(0.f, sq, 0xB1, 0xf, 0xf, false);
                    sx += __builtin_amdgcn_update_dpp(0.f, sx, 0x4E, 0xf, 0xf, false); sq += __builtin_amdgcn_update_dpp(0.f, sq, 0x4E, 0xf, 0xf, false);
                    if ((lane & 3) == 0) *(LAS f32x2*)(sa + 512) = ln_stats(sx, sq);
                }
                if (s == 14 && r16 == 0) {
#pragma unroll
                    for (int i = 0; i < 3; ++i) *(LAS f32x4*)(L3_ST + 1024 + (3 * w + i) * 64 + g * 16) = cs[i];
                }
                SLOT3(po, sl0 + 2);
            }
            STAMP(11);
            const unsigned hi = lane >> 5;
            const float r01 = hi ? st[1].y : st[0].y, r23 = hi ? st[3].y : st[2].y;
            const float rr01 = r01 * r01, rr23 = r23 * r23;
            const unsigned tau = 2 * (g & 1) + (g >> 1);
#pragma unroll
            for (int i = 0; i < 3; ++i) {
                const f32x4 cv = cs[i] + cvp[i];
                u32x2 hp[2];
#pragma unroll
                for (int pp = 0; pp < 2; ++pp) {
                    const f32x4 e0 = acc[2 * pp][i] - st[2 * pp].x * cv, e1 = acc[2 * pp + 1][i] - st[2 * pp + 1].x * cv;
                    float hv[4];
#pragma unroll
                    for (int r = 0; r < 4; ++r) {
                        auto sw = __builtin_amdgcn_permlane32_swap(f2u(e0[r]), f2u(e1[r]), false, false);
                        const unsigned x0 = sw[0], x1 = sw[1];
                        hv[r] = __builtin_fmaxf(u2f(x0), 0.0f) * (u2f(x1) * (pp ? rr23 : rr01));
                    }
                    hp[pp] = (u32x2){pk_bf16(hv[0], hv[1]), pk_bf16(hv[2], hv[3])};
                }
                auto s0 = __builtin_amdgcn_permlane16_swap(hp[0].x, hp[1].x, false, false);
                auto s1 = __builtin_amdgcn_permlane16_swap(hp[0].y, hp[1].y, false, false);
                const u32x4 hf4 = {s0[0], s1[0], s0[1], s1[1]};
                const unsigned U = 12 * c + 3 * n3 + i;
                const unsigned soff = WS_HID + ((4 * m3) * NKS4 + (U >> 2)) * 1024 + (U & 3) * 256;
                const unsigned off = U < 172 ? tau * (NKS4 * 1024) + r16 * 16 + soff : 0xFFFFFFF0u;
                __builtin_amdgcn_raw_buffer_store_b128(hf4, rs_ws, off, 0, AUX_SC1);
            }
            asm volatile("" ::: "memory");
        }
    }
    STAMP(12);
    {
        const bool active = c < 15;
        if (!active) {
            ISSUE_D2(12); ISSUE_D2(14); ISSUE_D2(16); ISSUE_D2(18); ISSUE_D2(20); ISSUE_D2(22); ISSUE_D2(24); ISSUE_D2(26); ISSUE_D2(28);
            ISSUE_S();
            ISSUE_D2(0); ISSUE_D2(2); ISSUE_D2(4);
        }
        WAIT_VM(0);
        BAR();
        if (tid == 0 && active) __hip_atomic_store(flags + (FL_3 + c) * 32, epoch, __ATOMIC_RELAXED, __HIP_MEMORY_SCOPE_AGENT);
        STAMP(13);
#define REST_D() do { ISSUE_D2(6); ISSUE_D2(8); ISSUE_D2(10); ISSUE_D2(34); ISSUE_D2(36); ISSUE_D2(38); } while (0)
        if (w != 0) REST_D();
        const unsigned wb0 = (8 * g + q4) * 128 + (fsw * 32) + 8 * p4;
        u32x4 bq[11][4];
#pragma unroll
        for (int ct = 0; ct < 4; ++ct) {
            const u32x4 t0 = tr_frag((wb0 ^ (ct * 32)) + (30 + (kq4 < 3 ? kq4 : 0)) * 4096, 512);
            bq[0][ct] = kq4 < 3 ? t0 : (u32x4){0u, 0u, 0u, 0u};
        }
        BAR();
#define LAST_D() do { ISSUE_D2(30); DMA(wk, 32 * 4096, vwd, rs_wd, swd, 35 * 32768); } while (0)
        if (w != 0) LAST_D();
        unsigned wbk = wb0 + kq4 * (10 * 4096);
        unsigned abase = lane * 16;
        const unsigned asoff = WS_HID + ((4 * hf + 2 * rp4) * NKS4 + 11 * kq4 - (kq4 == 3 ? 1 : 0)) * 1024;
        PIN(wbk); PIN(abase);
        if (w == 0) wait_flags(flags + FL_3 * 32, 15, epoch, lane);
        BAR();
        STAMP(14);
        u32x4 af[2][11];
#define LOADA4(k) do { af[0][k] = __builtin_amdgcn_raw_buffer_load_b128(rs_ws, abase + ((k) & 3) * 1024, asoff + ((k) >> 2) * 4096, AUX_SC1); \
                       af[1][k] = __builtin_amdgcn_raw_buffer_load_b128(rs_ws, abase + ((k) & 3) * 1024, asoff + NKS4 * 1024 + ((k) >> 2) * 4096, AUX_SC1); } while (0)
        if (w == 0) { REST_D(); LAST_D(); }
        LOADA4(0); LOADA4(1); LOADA4(2);
        WAIT_VM(6);
        BAR();
#define LOAD4(j) do { _Pragma("unroll") for (int ct = 0; ct < 4; ++ct) bq[j][ct] = tr_frag((wbk ^ (ct * 32)) + ((j) - 1) * 4096, 512); } while (0)
        LOAD4(1); LOAD4(2);
        __builtin_amdgcn_sched_barrier(0);
        f32x4 acc[2][4] = {{Z4, Z4, Z4, Z4}, {Z4, Z4, Z4, Z4}};
#pragma unroll
        for (int j = 0; j < 11; ++j) {
            if (j + 3 < 11) { LOADA4(j + 3); LOAD4(j + 3); }
#pragma unroll
            for (int rl = 0; rl < 2; ++rl)
#pragma unroll
                for (int ct = 0; ct < 4; ++ct) acc[rl][ct] = mfma32(bq[j][ct], af[rl][j], acc[rl][ct]);
#pragma unroll
            for (int i = 0; i < 8; ++i) { __builtin_amdgcn_sched_group_barrier(0x8, 1, 0); __builtin_amdgcn_sched_group_barrier(0x100, 1, 0); }
            __builtin_amdgcn_sched_barrier(0);
        }
        STAMP(15);
        BAR();
        const unsigned red = w * 8192 + lane * 16;
#pragma unroll
        for (int rl = 0; rl < 2; ++rl)
#pragma unroll
            for (int ct = 0; ct < 4; ++ct) *(LAS f32x4*)(red + (rl * 4 + ct) * 1024) = acc[rl][ct];
        BAR();
        f32x4 a0 = Z4, a1 = Z4;
#pragma unroll
        for (int o = 0; o < 4; ++o) {
            const unsigned ra = (2 * rp4 + (o & 1) + 4 * (o >> 1)) * 8192 + lane * 16 + ((rt4 & 1) * 4 + 2 * kh) * 1024;
            a0 += lds128f(ra); a1 += lds128f(ra + 1024);
        }
        auto s0 = __builtin_amdgcn_permlane16_swap(pk_bf16(a0[0], a0[1]), pk_bf16(a1[0], a1[1]), false, false);
        auto s1 = __builtin_amdgcn_permlane16_swap(pk_bf16(a0[2], a0[3]), pk_bf16(a1[2], a1[3]), false, false);
        const u32x4 at = {s0[0], s1[0], s0[1], s1[1]};
        u32x4 o;
#pragma unroll
        for (int i = 0; i < 4; ++i) o[i] = pk_bf16(bf_lo(at[i]) + bf_lo(h_keep[i]), bf_hi(at[i]) + bf_hi(h_keep[i]));
        *(u32x4*)((char*)P.out + tok_off) = o;
    }
    STAMP(16);
}

}

extern "C" void kernel_launch(void* const* d_in, const int* in_sizes, int n_in, void* d_out, int out_size, void* d_ws, size_t ws_size, hipStream_t stream) {
    Params P;
    P.x = (const u16*)d_in[0]; P.wqkv = (const u16*)d_in[1]; P.wo = (const u16*)d_in[2];
    P.wg = (const u16*)d_in[4]; P.wu = (const u16*)d_in[5]; P.wd = (const u16*)d_in[6];
    P.out = (u16*)d_out; P.ws = (unsigned char*)d_ws;
    (void)hipFuncSetAttribute((const void*)fused_block, hipFuncAttributeMaxDynamicSharedMemorySize, LDS_BYTES);
    hipLaunchKernelGGL(fused_block, dim3(256), dim3(512), LDS_BYTES, stream, P);
}
```

```cpp
#include <hip/hip_runtime.h>
#include <hip/hip_bf16.h>
#include <stdint.h>
#pragma clang diagnostic ignored "-Wint-to-pointer-cast"

#define LAS __attribute__((address_space(3)))
#define GAS __attribute__((address_space(1)))
typedef unsigned short u16;
typedef unsigned long long u64;
typedef short s16x4 __attribute__((ext_vector_type(4)));
typedef __bf16 bf16x8 __attribute__((ext_vector_type(8)));
typedef __bf16 bf16x2 __attribute__((ext_vector_type(2)));
typedef float f32x4 __attribute__((ext_vector_type(4)));
typedef float f32x2 __attribute__((ext_vector_type(2)));
typedef unsigned u32x4 __attribute__((ext_vector_type(4)));
typedef unsigned u32x2 __attribute__((ext_vector_type(2)));
typedef GAS u64 gu64;
typedef __amdgpu_buffer_rsrc_t rsrc_t;

namespace {

constexpr int SEQ = 2048, DM = 512, FF = 1376;
constexpr int NKS4 = FF / 32;
constexpr unsigned WS_FLAG = 0;
constexpr unsigned WS_ST = 16384;
constexpr unsigned WS_O = 40960;
constexpr unsigned WS_H = WS_O + 131072;
constexpr unsigned WS_HID = WS_H + 131072;
constexpr unsigned WS_SPARE = WS_HID + 8 * 43 * 1024;
constexpr unsigned WS_BYTES = WS_SPARE + 8192;
constexpr int FL_GEN = 0, FL_1 = 8, FL_2 = 24, FL_3 = 40;
constexpr int AUX_SC1 = 16;
constexpr unsigned kSpinCap = 1u << 16;
#define TSTEP 4
constexpr unsigned CODE_KB = 24;

constexpr int LDS_BYTES = 163840;
constexpr int L1_X = 0, L1_H = 65536, L1_W = 81920, L1_WSLOT = 24576, L1_SCR = 155648;
constexpr int L2_WO = 81920, L2_RED = 114688;
constexpr int L3_A = 0, L3_B = 49152, L3_BSLOT = 12288, L3_ST = 147456;

__device__ __forceinline__ constexpr int vm_imm(int n) { return (n & 0xF) | ((n >> 4) << 14) | (0x7 << 4) | (0xF << 8); }
#define WAIT_VM(n) asm volatile("s_waitcnt vmcnt(%0)" :: "i"(n) : "memory")
#define BAR() do { asm volatile("s_waitcnt lgkmcnt(0)" ::: "memory"); __builtin_amdgcn_s_barrier(); asm volatile("" ::: "memory"); } while (0)
#define PIN(x) asm volatile("" : "+v"(x))
#define DMA(m0b, ldsoff, voff, rs, sbase, soffimm) do { unsigned t_; asm volatile("s_add_u32 m0, %1, %2\n\ts_add_u32 %0, %5, %6\n\tbuffer_load_dwordx4 %3, %4, %0 offen lds" \
    : "=&s"(t_) : "s"(m0b), "i"(ldsoff), "v"(voff), "s"(rs), "s"(sbase), "i"(soffimm) : "scc", "memory"); } while (0)
#define DMA_SC1(m0b, ldsoff, voff, rs, sbase, soffimm) do { unsigned t_; asm volatile("s_add_u32 m0, %1, %2\n\ts_add_u32 %0, %5, %6\n\tbuffer_load_dwordx4 %3, %4, %0 offen sc1 lds" \
    : "=&s"(t_) : "s"(m0b), "i"(ldsoff), "v"(voff), "s"(rs), "s"(sbase), "i"(soffimm) : "scc", "memory"); } while (0)

__device__ __forceinline__ rsrc_t mk_rsrc(const void* p, unsigned bytes) { return __builtin_amdgcn_make_buffer_rsrc((void*)p, 0, bytes, 0x00020000); }
__device__ __forceinline__ float u2f(unsigned u) { return __builtin_bit_cast(float, u); }
__device__ __forceinline__ unsigned f2u(float f) { return __builtin_bit_cast(unsigned, f); }
__device__ __forceinline__ unsigned pk_bf16(float a, float b) { bf16x2 v; v[0] = (__bf16)a; v[1] = (__bf16)b; return __builtin_bit_cast(unsigned, v); }
__device__ __forceinline__ float bf_lo(unsigned u) { return u2f(u << 16); }
__device__ __forceinline__ float bf_hi(unsigned u) { return u2f(u & 0xffff0000u); }
__device__ __forceinline__ float rbf(float f) { return (float)(__bf16)f; }
__device__ __forceinline__ f32x4 mfma32(u32x4 a, u32x4 b, f32x4 c) {
    return __builtin_amdgcn_mfma_f32_16x16x32_bf16(__builtin_bit_cast(bf16x8, a), __builtin_bit_cast(bf16x8, b), c, 0, 0, 0);
}
__device__ __forceinline__ f32x4 mfma16(u32x2 a, u32x2 b, f32x4 c) {
    return __builtin_amdgcn_mfma_f32_16x16x16bf16_1k(__builtin_bit_cast(s16x4, a), __builtin_bit_cast(s16x4, b), c, 0, 0, 0);
}
__device__ __forceinline__ u32x4 lds128(unsigned a) { return *(LAS u32x4*)a; }
__device__ __forceinline__ f32x4 lds128f(unsigned a) { return *(LAS f32x4*)a; }
__device__ __forceinline__ f32x2 lds64f(unsigned a) { return *(LAS f32x2*)a; }
__device__ __forceinline__ u32x2 tr_read(unsigned a) { return __builtin_bit_cast(u32x2, __builtin_amdgcn_ds_read_tr16_b64_v4i16((LAS s16x4*)a)); }
__device__ __forceinline__ u32x4 tr_frag(unsigned a, int step) { const u32x2 x = tr_read(a), y = tr_read(a + step); return (u32x4){x.x, x.y, y.x, y.y}; }
__device__ __forceinline__ float swap16_lo(float v) {
    const unsigned a = f2u(v); auto r = __builtin_amdgcn_permlane16_swap(a, a, false, false); const unsigned x = r[0]; return u2f(x);
}
__device__ __forceinline__ float xmax16_32(float v) {
    unsigned a = f2u(v);
    auto r = __builtin_amdgcn_permlane16_swap(a, a, false, false);
    const unsigned r0 = r[0], r1 = r[1];
    v = __builtin_fmaxf(u2f(r0), u2f(r1));
    a = f2u(v);
    auto s = __builtin_amdgcn_permlane32_swap(a, a, false, false);
    const unsigned s0 = s[0], s1 = s[1];
    return __builtin_fmaxf(u2f(s0), u2f(s1));
}
__device__ __forceinline__ float bcast_row0(float v) { return __builtin_amdgcn_update_dpp(0.f, v, 0x150, 0xf, 0xf, false); }
__device__ __forceinline__ float diag4(f32x4 d, unsigned lane) { const float a = (lane & 1) ? d[1] : d[0], b = (lane & 1) ? d[3] : d[2]; return (lane & 2) ? b : a; }
__device__ __forceinline__ f32x2 ln_stats(float s, float q) {
    const float mean = s * (1.0f / DM);
    const float var = __builtin_fmaxf(q * (1.0f / DM) - mean * mean, 0.0f);
    const float sd = rbf(__builtin_amdgcn_sqrtf(rbf(rbf(var) + 1e-5f)));
    return (f32x2){rbf(mean), __builtin_amdgcn_rcpf(sd)};
}
__device__ __forceinline__ void wait_flags(gu64* base, unsigned n, u64 epoch, unsigned lane) {
    for (unsigned spins = 0; spins < kSpinCap; ++spins) {
        u64 v = epoch;
        if (lane < n) v = __hip_atomic_load(base + lane * 32, __ATOMIC_RELAXED, __HIP_MEMORY_SCOPE_AGENT);
        if (__all(v == epoch)) break;
    }
    __builtin_amdgcn_fence(__ATOMIC_ACQUIRE, "wavefront");
}

struct Params { const u16* x; const u16* wqkv; const u16* wo; const u16* wg; const u16* wu; const u16* wd; u16* out; unsigned char* ws; };

#define STAMP(i) do {} while (0)

__global__ void __launch_bounds__(512, 2) __attribute__((amdgpu_waves_per_eu(2, 2))) fused_block(Params P
) {
    const unsigned tid = threadIdx.x, lane = tid & 63;
    const unsigned w = __builtin_amdgcn_readfirstlane(tid >> 6);
    const unsigned b = blockIdx.x, xcd = b & 7, jj = b >> 3;
    const unsigned p = 4 * (xcd >> 1) + (jj >> 3);
    const unsigned hf = (jj >> 2) & 1, hg = 4 * (xcd & 1) + (jj & 3);
    const unsigned c = 8 * hf + hg;
    const unsigned r16 = lane & 15, g = lane >> 4, l8 = lane >> 3, l7 = lane & 7, q4 = r16 >> 2, p4 = lane & 3;
    STAMP(0);
    {
        u64 pc; asm volatile("s_getpc_b64 %0" : "=s"(pc));
        const char* cp = (const char*)(pc & ~(u64)1023) + ((jj * 8 + w) % CODE_KB) * 1024 + lane * 16;
        asm volatile("s_mov_b32 m0, %0\n\ts_nop 0\n\tglobal_load_lds_dwordx4 %1, off" :: "s"(L1_SCR + 6144), "v"(cp) : "memory");
    }
    const unsigned wk = w * 1024;
    const rsrc_t rs_x = mk_rsrc(P.x, SEQ * DM * 2);
    const rsrc_t rs_w = mk_rsrc(P.wqkv, DM * 3 * DM * 2);
    unsigned char* const wsp = P.ws + (size_t)p * WS_BYTES;
    gu64* const flags = (gu64*)(wsp + WS_FLAG);
    const unsigned T0 = 128 * p + 64 * hf;
    STAMP(18);
    u32x4 ONES = {0x3F803F80u, 0x3F803F80u, 0x3F803F80u, 0x3F803F80u};
    PIN(ONES);
    const f32x4 Z4 = {0.f, 0.f, 0.f, 0.f};
    const unsigned fsw = ((q4 >> 1) & 1) | ((g & 1) << 1);
    const unsigned wsw = (l7 ^ (((g & 1) | ((w & 1) << 1)) << 1)) * 16;
    u64 gen = 0;
    const unsigned rt4 = w & 3, kh = w >> 2, rp4 = rt4 >> 1, kq4 = (rt4 & 1) + 2 * kh;
    const unsigned tok_off = (128 * p + 64 * hf + 32 * (rt4 & 1) + (rt4 >> 1) + 2 * r16) * 1024 + (64 * hg + 32 * kh + 16 * (g & 1) + 8 * (g >> 1)) * 2;
    u32x4 xr;

    {
        const unsigned rot = (2 * (jj & 3) + (jj >> 2) + (xcd >> 1)) & 7;
        const unsigned hh = w & 3, par = w >> 2;
        const unsigned vh = l8 * 2048 + ((l7 ^ g) * 16);
        const unsigned vxr = vh ^ ((w & 1) * 64);
        const unsigned sxr = (T0 + 32 * ((w >> 1) & 1) + (w >> 2) + 16 * (w & 1)) * 1024;
        const unsigned vw = (8 * w + l8) * 3072 + wsw + hg * 128;
        {
            const unsigned kap = (w + rot) & 7;
            const unsigned sh = (T0 == 0 ? 0 : T0 - 16) * 1024 + kap * 128;
            const unsigned hb = L1_H + w * 2048;
            if (lane >= 8) { DMA(hb, 0, vh, rs_x, sh, 0); const unsigned vh1 = vh ^ 64; DMA(hb, 1024, vh1, rs_x, sh, 1024); }
            else { *(LAS u32x4*)(hb + lane * 16) = ONES; *(LAS u32x4*)(hb + 1024 + lane * 16) = ONES; }
        }
        unsigned kx[8];
#pragma unroll
        for (int q = 0; q < 8; ++q) kx[q] = (q + rot) & 7;
#define TOUCH(ptr) asm volatile("s_mov_b32 m0, %0\n\ts_nop 0\n\tglobal_load_lds_dwordx4 %1, off" :: "s"(L1_SCR + 6144), "v"(ptr) : "memory")
#define ISSUE_X(q) DMA(wk, L1_X + (q) * 8192, vxr, rs_x, sxr + kx[q] * 128, 0)
#define ISSUE_W1(q, mat) DMA(wk, L1_W + ((q) % 3) * L1_WSLOT + (mat) * 8192, vw, rs_w, kx[q] * 196608, (mat) * 1024)
#define ISSUE_W(q) do { ISSUE_W1(q, 0); ISSUE_W1(q, 1); ISSUE_W1(q, 2); } while (0)
        ISSUE_X(0); ISSUE_W(0);
        __builtin_amdgcn_s_barrier();
        ISSUE_X(1); ISSUE_W(1);
        u64 genv;
        { const gu64* gp = flags + FL_GEN * 32; asm volatile("global_load_dwordx2 %0, %1, off sc1" : "=v"(genv) : "v"(gp) : "memory"); }
#define NTOUCH 4
        STAMP(17);
        const unsigned xa = (r16 >> 3) * 1024 + (r16 & 7) * 128 + ((g ^ ((r16 >> 1) & 7)) * 16);
        const unsigned xc0 = xa + par * 4096, xc1 = xc0 ^ 64;
        unsigned xh0 = xa + L1_H, xh1 = xh0 ^ 64;
        PIN(xh0); PIN(xh1);
        unsigned wa = L1_W + (8 * g + q4) * 128 + ((hh ^ fsw) * 32) + 8 * p4, wa2 = wa + 2 * L1_WSLOT;
        PIN(wa); PIN(wa2);
        const unsigned sxa = (xa ^ ((hh & 1) * 64));
        unsigned stc = sxa + par * 4096 + (hh >> 1) * 8192, sth = sxa + L1_H + (hh >> 1) * 2048;
        PIN(stc); PIN(sth);
        f32x4 mh, mp, mc;
#pragma unroll
        for (int r = 0; r < 4; ++r) {
            const unsigned kap = 4 * g + r;
            mh[r] = (T0 > 0 && (kap >> 3) == par && (kap & 7) >= r16 + 1) ? 0.f : -1e30f;
            mp[r] = (kap >= r16 + 9) ? 0.f : -1e30f;
            mc[r] = (kap <= r16 && r16 - kap <= 7) ? 0.f : -1e30f;
        }
        PIN(mh); PIN(mp); PIN(mc);

        f32x4 aq[3] = {Z4, Z4, Z4}, ak[3] = {Z4, Z4, Z4}, av[3] = {Z4, Z4, Z4};
        f32x4 gr[3] = {Z4, Z4, Z4}, rs[3] = {Z4, Z4, Z4};
        f32x2 sL[3]; f32x4 sR[3][2];
        const unsigned part = L1_SCR, fin = L1_SCR + 2560 + w * 384;
        u32x4 xf[2][3], wf[2][3], sf[2][3];
        const rsrc_t rs_wo = mk_rsrc(P.wo, DM * DM * 2);
        const unsigned vwo = (8 * w + l8) * 1024 + wsw + hg * 128;
#define LOAD1(s) do { const int kq_ = (s) >> 1, kk_ = (s) & 1; \
            xf[(s) & 1][0] = lds128((kk_ ? xh1 : xh0) + kq_ * 2048); xf[(s) & 1][1] = lds128((kk_ ? xc1 : xc0) + kq_ * 8192); xf[(s) & 1][2] = lds128((kk_ ? xc1 : xc0) + kq_ * 8192 + 2048); \
            _Pragma("unroll") for (int mat = 0; mat < 3; ++mat) wf[(s) & 1][mat] = tr_frag((kq_ % 3 == 2 ? wa2 : wa + (kq_ % 3) * L1_WSLOT) + mat * 8192 + kk_ * 4096, 512); } while (0)
#define X2(q, h) DMA(wk, L1_X + (q) * 8192 + (h) * 4096, vxr, rs_x, sxr + kx[q] * 128, (h) * 1024)
#define W2(q, mat, h) DMA(wk, L1_W + ((q) % 3) * L1_WSLOT + (mat) * 8192 + (h) * 4096, vw, rs_w, kx[q] * 196608, (mat) * 1024 + (h) * 98304)
#define O2(lds, m, h) DMA(wk, (lds) + (h) * 4096, vwo, rs_wo, 0u, (m) * 65536 + (h) * 32768)
#define JOB1(it, j) do { const int pc_ = (j) >> 1, h_ = (j) & 1; \
            if ((it) == 0) { if (pc_ == 0) X2(2, h_); else if (pc_ < 4) W2(2, (pc_ >= 1 && pc_ < 4) ? pc_ - 1 : 0, h_); } \
            if ((it) >= 1 && (it) <= 5 && pc_ < 3) W2(((it) >= 1 && (it) <= 5) ? (it) + 2 : 0, pc_ < 3 ? pc_ : 0, h_); \
            if ((it) == 1 && pc_ >= 3 && pc_ < 6) X2((pc_ >= 3 && pc_ < 6) ? pc_ : 0, h_); \
            if ((it) == 2 && pc_ >= 3 && pc_ < 5) X2((pc_ >= 3 && pc_ < 5) ? pc_ + 3 : 0, h_); \
            if ((it) == 6) { if (pc_ < 2) O2(L2_WO + (6 + (pc_ < 2 ? pc_ : 0)) * 8192, 6 + (pc_ < 2 ? pc_ : 0), h_); else if (pc_ < 5) O2(((pc_ >= 2 && pc_ < 5) ? pc_ - 2 : 0) * 8192, 3 + ((pc_ >= 2 && pc_ < 5) ? pc_ - 2 : 0), h_); } \
            if ((it) == 7 && pc_ < 3) O2(L2_WO + (pc_ < 3 ? pc_ : 0) * 8192, pc_ < 3 ? pc_ : 0, h_); } while (0)
#define SLOT1(it, sl) do { __builtin_amdgcn_sched_barrier(0); if (w < 4) { JOB1(it, 2 * (sl)); JOB1(it, 2 * (sl) + 1); } __builtin_amdgcn_sched_barrier(0); } while (0)
        WAIT_VM(5);
        BAR();
        STAMP(1);
        LOAD1(0);
#pragma unroll
        for (int s = 0; s < 16; ++s) {
            const int it = (s + 1) / 2, sl0 = (s & 1) ? 0 : 3;
            if (s & 1) {
#define TA(s_, q_) ((TSTEP < (s_) && TSTEP / 2 >= (q_)) ? NTOUCH : 0)
                if (w < 4) {
                    if (s == 1) WAIT_VM(9 + TA(1, -1)); else if (s == 3) WAIT_VM(12 + TA(3, 0)); else if (s == 5) WAIT_VM(14 + TA(5, 1)); else if (s == 7) WAIT_VM(10 + TA(7, 2));
                    else if (s == 9) WAIT_VM(6 + TA(9, 3)); else if (s == 11) WAIT_VM(6 + TA(11, 4)); else if (s == 13) WAIT_VM(11 + TA(13, 5));
                } else {
                    if (s == 1) WAIT_VM(1 + TA(1, -1)); else if (s == 9) WAIT_VM(TA(9, 3));
                }
                if (s < 15) BAR();
                if (s == 9) { asm volatile("" : "+v"(genv)); gen = ((u64)(unsigned)__builtin_amdgcn_readfirstlane((unsigned)(genv >> 32)) << 32) | (unsigned)__builtin_amdgcn_readfirstlane((unsigned)genv); }
            }
            const int n_ = s + 1, kq_ = n_ >> 1, kk_ = n_ & 1, cb = s & 1, nb = n_ & 1;
            const unsigned wsl = (kq_ % 3 == 2 ? wa2 : wa + (kq_ % 3) * L1_WSLOT) + kk_ * 4096;
            if (s < 15) { xf[nb][0] = lds128((kk_ ? xh1 : xh0) + kq_ * 2048); xf[nb][1] = lds128((kk_ ? xc1 : xc0) + kq_ * 8192); xf[nb][2] = lds128((kk_ ? xc1 : xc0) + kq_ * 8192 + 2048); }
#pragma unroll
            for (int t = 0; t < 3; ++t) aq[t] = mfma32(wf[cb][0], xf[cb][t], aq[t]);
#pragma unroll
            for (int i = 0; i < 3; ++i) { __builtin_amdgcn_sched_group_barrier(0x8, 1, 0); __builtin_amdgcn_sched_group_barrier(0x100, 1, 0); }
            if (s == 0) { __builtin_amdgcn_sched_barrier(0); if (w < 4) { JOB1(0, 0); JOB1(0, 1); JOB1(0, 2); } __builtin_amdgcn_sched_barrier(0); } else SLOT1(it, sl0);
            if (s < 15) { wf[nb][0] = tr_frag(wsl, 512); wf[nb][1] = tr_frag(wsl + 8192, 512); }
#pragma unroll
            for (int t = 0; t < 3; ++t) ak[t] = mfma32(wf[cb][1], xf[cb][t], ak[t]);
#pragma unroll
            for (int i = 0; i < 3; ++i) { __builtin_amdgcn_sched_group_barrier(0x8, 1, 0); __builtin_amdgcn_sched_group_barrier(0x100, 2, 0); }
            if (s == 0) { __builtin_amdgcn_sched_barrier(0); if (w < 4) { JOB1(0, 3); JOB1(0, 4); JOB1(0, 5); } __builtin_amdgcn_sched_barrier(0); } else SLOT1(it, sl0 + 1);
            if (s < 15) wf[nb][2] = tr_frag(wsl + 16384, 512);
            if (s >= 6 && s <= 9) { const int j = s - 6; sf[j & 1][0] = lds128(sth + j * 4096); sf[j & 1][1] = lds128(stc + j * 16384); sf[j & 1][2] = lds128(stc + j * 16384 + 2048); }
            f32x2 pp[4];
            if (s == 11) {
                const unsigned pa = part + ((g == 0 ? 0 : (g == 3 ? 0 : 2 * par + g)) * 64 + r16) * 8;
                pp[0] = lds64f(pa); pp[1] = lds64f(pa + 128); pp[2] = lds64f(pa + 256); pp[3] = lds64f(pa + 384);
            }
            if (s == 13) {
#pragma unroll
                for (int t = 0; t < 3; ++t) { sL[t] = lds64f(fin + (t * 16 + r16) * 8); sR[t][0] = lds128f(fin + (t * 16 + 4 * g) * 8); sR[t][1] = lds128f(fin + (t * 16 + 4 * g) * 8 + 16); }
            }
#pragma unroll
            for (int t = 0; t < 3; ++t) av[t] = mfma32(xf[cb][t], wf[cb][2], av[t]);
            if (s >= 7 && s <= 10) {
#pragma unroll
                for (int t = 0; t < 3; ++t) { gr[t] = mfma32(sf[(s - 7) & 1][t], sf[(s - 7) & 1][t], gr[t]); rs[t] = mfma32(ONES, sf[(s - 7) & 1][t], rs[t]); }
            }
#pragma unroll
            for (int i = 0; i < 9; ++i) { __builtin_amdgcn_sched_group_barrier(0x8, 1, 0); __builtin_amdgcn_sched_group_barrier(0x100, 1, 0); }
            __builtin_amdgcn_sched_barrier(0);
            if (s == 10 && g == q4) {
                *(LAS f32x2*)(part + (hh * 16 + r16) * 8) = (f32x2){rs[0][0], diag4(gr[0], lane)};
                *(LAS f32x2*)(part + (((1 + 2 * par) * 4 + hh) * 16 + r16) * 8) = (f32x2){rs[1][0], diag4(gr[1], lane)};
                *(LAS f32x2*)(part + (((2 + 2 * par) * 4 + hh) * 16 + r16) * 8) = (f32x2){rs[2][0], diag4(gr[2], lane)};
            }
            if (s == 11) {
                const f32x2 sm = (pp[0] + pp[1]) + (pp[2] + pp[3]);
                const f32x2 res = ln_stats(sm.x, sm.y);
                if (g < 3) *(LAS f32x2*)(fin + (g * 16 + r16) * 8) = res;
            }
            if (s == 0) { __builtin_amdgcn_sched_barrier(0); if (w < 4) { JOB1(0, 6); JOB1(0, 7); } __builtin_amdgcn_sched_barrier(0); } else SLOT1(it, sl0 + 2);
            if (s == TSTEP) {
            const char* t1 = lane < 22 ? (const char*)P.wg + lane * 65536 : (lane < 44 ? (const char*)P.wu + (lane - 22) * 65536 : (const char*)P.wd + (lane - 44) * 65536);
            const char* t2 = lane < 8 ? (const char*)P.wo + lane * 65536 : (lane < 32 ? (const char*)P.out + p * 131072 + (lane - 8) * 4096 : (const char*)P.wd + (20 + (lane & 1)) * 65536);
            const char* tw = w == 0 ? (const char*)wsp + WS_FLAG + lane * 256 : (w == 1 ? t1 : (w == 2 ? t2 : (const char*)wsp + ((w & 1) ? WS_SPARE : 32768u) + lane * 16));
            TOUCH(tw);
#pragma unroll
            for (int i = 0; i < 3; ++i) {
                unsigned q = b * 24 + w * 3 + i; if (q >= 4640) q -= 4640;
                const char* base = q < 512 ? (const char*)P.wo + q * 1024 : (q < 1888 ? (const char*)P.wg + (q - 512) * 1024 : (q < 3264 ? (const char*)P.wu + (q - 1888) * 1024 : (const char*)P.wd + (q - 3264) * 1024));
                const char* t3 = base + lane * 16;
                TOUCH(t3);
            }

        }
            if (s == 12) asm volatile("buffer_load_dwordx4 %0, %1, %2, 0 offen" : "=v"(xr) : "v"(tok_off), "s"(rs_x) : "memory");
        }
        STAMP(2);
        f32x4 csq, csk;
#pragma unroll
        for (int r = 0; r < 4; ++r) { csq[r] = bcast_row0(aq[0][r]); csk[r] = bcast_row0(ak[0][r]); }
        const float csv = swap16_lo(av[0][0]);
        u32x2 qb[2], kb[3], vb[3];
#pragma unroll
        for (int t = 0; t < 3; ++t) {
            const f32x4 kv = (ak[t] - sL[t].x * csk) * sL[t].y;
            kb[t] = (u32x2){pk_bf16(kv[0], kv[1]), pk_bf16(kv[2], kv[3])};
            if (t > 0) { const f32x4 qv = (aq[t] - sL[t].x * csq) * sL[t].y; qb[t - 1] = (u32x2){pk_bf16(qv[0], qv[1]), pk_bf16(qv[2], qv[3])}; }
            const f32x4 mn = {sR[t][0][0], sR[t][0][2], sR[t][1][0], sR[t][1][2]}, rd = {sR[t][0][1], sR[t][0][3], sR[t][1][1], sR[t][1][3]};
            const f32x4 vv = (av[t] - mn * csv) * rd;
            vb[t] = (u32x2){pk_bf16(vv[0], vv[1]), pk_bf16(vv[2], vv[3])};
        }
        const float SC = 0.25f * 1.44269504088896f;
        u32x2 ob[2];
#pragma unroll
        for (int t = 0; t < 2; ++t) {
            f32x4 sp = mfma16(kb[t], qb[t], t == 0 ? mh : mp), sc = mfma16(kb[t + 1], qb[t], mc);
            float mx = __builtin_fmaxf(__builtin_fmaxf(__builtin_fmaxf(sp[0], sp[1]), __builtin_fmaxf(sp[2], sp[3])), __builtin_fmaxf(__builtin_fmaxf(sc[0], sc[1]), __builtin_fmaxf(sc[2], sc[3])));
            mx = xmax16_32(mx);
            const float mb = -mx * SC;
#pragma unroll
            for (int r = 0; r < 4; ++r) { sp[r] = __builtin_amdgcn_exp2f(sp[r] * SC + mb); sc[r] = __builtin_amdgcn_exp2f(sc[r] * SC + mb); }
            const u32x4 pf = {pk_bf16(sp[0], sp[1]), pk_bf16(sp[2], sp[3]), pk_bf16(sc[0], sc[1]), pk_bf16(sc[2], sc[3])};
            const u32x4 vf = {vb[t].x, vb[t].y, vb[t + 1].x, vb[t + 1].y};
            const f32x4 sm = mfma32(ONES, pf, Z4);
            const f32x4 o = mfma32(vf, pf, Z4) * __builtin_amdgcn_rcpf(sm[0]);
            ob[t] = (u32x2){pk_bf16(o[0], o[1]), pk_bf16(o[2], o[3])};
        }
        auto s0 = __builtin_amdgcn_permlane16_swap(ob[0].x, ob[1].x, false, false);
        auto s1 = __builtin_amdgcn_permlane16_swap(ob[0].y, ob[1].y, false, false);
        const u32x4 o_frag = {s0[0], s1[0], s0[1], s1[1]};
        const rsrc_t rs_ws1 = mk_rsrc(wsp, WS_BYTES);
        const unsigned off = ((g & 1) * 16384) + ((2 * (hh & 1) + (g >> 1)) * 16 + r16) * 16;
        const unsigned soff = WS_O + ((4 * hf + 2 * par) * 16 + 2 * hg + (hh >> 1)) * 1024;
        __builtin_amdgcn_raw_buffer_store_b128(o_frag, rs_ws1, off + soff, 0, AUX_SC1);
        asm volatile("" ::: "memory");
    }
    STAMP(3);
    const rsrc_t rs_ws = mk_rsrc(wsp, WS_BYTES);
    const u64 epoch = gen + 1;
    const unsigned n3 = w & 3, m3 = w >> 2;
    const rsrc_t rs_g = mk_rsrc(P.wg, DM * FF * 2), rs_u = mk_rsrc(P.wu, DM * FF * 2);
    const rsrc_t rs_gu = mk_rsrc(m3 ? P.wu : P.wg, DM * FF * 2);
    unsigned vg0[3], vg1[3];
#pragma unroll
    for (int i = 0; i < 3; ++i) {
        const unsigned s = (n3 * 3 + i) * 64 + lane;
        const unsigned ks2 = s >= 384 ? 1 : 0, s2 = s - ks2 * 384, row = (s2 * 43691u) >> 19, pos = s2 - row * 12;
        const int umax = (int)(c < 15 ? 171 - 12 * c : 0);
        int u0 = (int)pos - (int)((row >> 3) & 1); if (u0 < 0) u0 += 12;
        int u1 = (int)pos - (int)(((row >> 3) & 1) + 2); if (u1 < 0) u1 += 12;
        const unsigned rb = (ks2 * 32 + row) * (FF * 2) + c * 192;
        vg0[i] = rb + min(u0, umax) * 16; vg1[i] = rb + min(u1, umax) * 16;
    }
    unsigned vgu[3];
#pragma unroll
    for (int i = 0; i < 3; ++i) vgu[i] = m3 ? vg1[i] : vg0[i];
    unsigned mg0[3], mgu[3];
#pragma unroll
    for (int i = 0; i < 3; ++i) { const unsigned pi = n3 * 3 + i; mg0[i] = L3_B + (pi >= 6 ? L3_BSLOT + (pi - 6) * 1024 : pi * 1024); mgu[i] = mg0[i] + m3 * 6144; }
    const unsigned rot3 = (2 * (jj >> 3) + (jj & 7)) & 7;
#define ISSUE_B(pr) do { const unsigned sb_ = (((pr) + rot3) & 7) * (64 * FF * 2); _Pragma("unroll") for (int i_ = 0; i_ < 3; ++i_) DMA(mgu[i_], ((pr) & 3) * 2 * L3_BSLOT, vgu[i_], rs_gu, sb_, 0); } while (0)

    u32x4 h_keep;
    {
        WAIT_VM(0);
        BAR();
        if (tid == 0) __hip_atomic_store(flags + (FL_1 + c) * 32, epoch, __ATOMIC_RELAXED, __HIP_MEMORY_SCOPE_AGENT);
        STAMP(4);
        asm volatile("" : "+v"(xr));
        const unsigned wl = (8 * g + q4) * 128 + (fsw * 32) + 8 * p4;
        const unsigned wb0 = wl + (kh ? 8192 : L2_WO), wb1 = wl + (kh ? L2_WO + 49152 : L2_WO + 16384), wb2 = wl + (kh ? L2_WO + 57344 : 0);
        unsigned abase = lane * 16;
        const unsigned asoff = WS_O + ((4 * hf + rt4) * 16 + 8 * kh) * 1024;
        PIN(abase);
        u32x4 bf[8][4];
#pragma unroll
        for (int k = 0; k < 8; ++k)
#pragma unroll
            for (int ct = 0; ct < 4; ++ct) bf[k][ct] = tr_frag(((k < 4 ? wb0 : (k < 6 ? wb1 : wb2)) ^ (ct * 32)) + (k < 4 ? k : (k < 6 ? k - 4 : k - 6)) * 4096, 512);
        __builtin_amdgcn_sched_barrier(0);
        STAMP(19);
        if (w == 0) wait_flags(flags + (FL_1 + 8 * hf) * 32, 8, epoch, lane);
        BAR();
        STAMP(5);
        u32x4 af[8];
#pragma unroll
        for (int k = 0; k < 4; ++k) af[k] = __builtin_amdgcn_raw_buffer_load_b128(rs_ws, abase + (k & 3) * 1024, asoff + (k >> 2) * 4096, AUX_SC1);
        __builtin_amdgcn_sched_barrier(0);
        f32x4 acc[4] = {Z4, Z4, Z4, Z4};
#pragma unroll
        for (int k = 0; k < 8; ++k) {
            if (k + 4 < 8) af[k + 4] = __builtin_amdgcn_raw_buffer_load_b128(rs_ws, abase + (k & 3) * 1024, asoff + 4096, AUX_SC1);
            __builtin_amdgcn_sched_barrier(0);
#pragma unroll
            for (int ct = 0; ct < 4; ++ct) acc[ct] = mfma32(bf[k][ct], af[k], acc[ct]);
            __builtin_amdgcn_sched_barrier(0);
        }
        STAMP(6);
        const unsigned red = L2_RED + w * 2048 + lane * 16;
        *(LAS f32x4*)red = kh ? acc[0] : acc[2];
        *(LAS f32x4*)(red + 1024) = kh ? acc[1] : acc[3];
        BAR();
        const f32x4 a0 = (kh ? acc[2] : acc[0]) + lds128f(red ^ 8192);
        const f32x4 a1 = (kh ? acc[3] : acc[1]) + lds128f((red ^ 8192) + 1024);
        auto s0 = __builtin_amdgcn_permlane16_swap(pk_bf16(a0[0], a0[1]), pk_bf16(a1[0], a1[1]), false, false);
        auto s1 = __builtin_amdgcn_permlane16_swap(pk_bf16(a0[2], a0[3]), pk_bf16(a1[2], a1[3]), false, false);
        const u32x4 at = {s0[0], s1[0], s0[1], s1[1]};
#pragma unroll
        for (int i = 0; i < 4; ++i) h_keep[i] = pk_bf16(bf_lo(at[i]) + bf_lo(xr[i]), bf_hi(at[i]) + bf_hi(xr[i]));
        const unsigned hoff = ((2 * (g & 1) + (g >> 1)) * 16 + r16) * 16;
        const unsigned hsoff = WS_H + ((4 * hf + rt4) * 16 + 2 * hg + kh) * 1024;
        __builtin_amdgcn_raw_buffer_store_b128(h_keep, rs_ws, hoff + hsoff, 0, AUX_SC1);
        const f32x4 gs = mfma32(h_keep, h_keep, Z4), sm = mfma32(ONES, h_keep, Z4);
        const u32x2 stv = {f2u(sm[0]), f2u(diag4(gs, lane))};
        const unsigned stoff = (g == q4) ? WS_ST + c * 1024 + w * 128 + r16 * 8 : 0xFFFFFFF0u;
        __builtin_amdgcn_raw_buffer_store_b64(stv, rs_ws, stoff, 0, AUX_SC1);
        asm volatile("" ::: "memory");
    }
    STAMP(7);
    const rsrc_t rs_wd = mk_rsrc(P.wd, FF * DM * 2);
    const unsigned vwd = l8 * 1024 + wsw + hg * 128;
#define KS4(s) (11 * ((s) / 10) + (s) % 10 + 1 - ((s) >= 30 ? 1 : 0))
    const unsigned swd = ((w >> 2) * 32 + (w & 3) * 8) * 1024;
#define ISSUE_D2(s0) DMA(wk, (s0) * 4096, vwd, rs_wd, swd, KS4(s0) * 32768)
    const unsigned swd0 = (w & 3) * 8 * 1024;
#define ISSUE_S() do { DMA(wk, 30 * 4096, vwd, rs_wd, swd0 + (kh ? 11 : 0) * 32768, 0); DMA(wk, 32 * 4096, vwd, rs_wd, swd0 + (kh ? 36 : 22) * 32768, 0); } while (0)
    {
        const bool active = c < 15;
        WAIT_VM(0);
        BAR();
        if (tid == 0) __hip_atomic_store(flags + (FL_2 + c) * 32, epoch, __ATOMIC_RELAXED, __HIP_MEMORY_SCOPE_AGENT);
        if (active && w != 0) { ISSUE_B(0); ISSUE_B(1); ISSUE_B(2); ISSUE_B(3); }
        STAMP(8);
        unsigned aa0 = L3_A + (4 * m3) * 1024 + lane * 16 + m3 * 8192, aa1 = aa0 ^ 8192;
        unsigned ba0[3], ba1[3];
#pragma unroll
        for (int i = 0; i < 3; ++i) {
            const unsigned mat = p4 >> 1;
            unsigned pos = 3 * n3 + i + (g & 1) + 2 * mat; if (pos >= 12) pos -= 12;
            const unsigned a = L3_B + mat * 6144 + ((8 * g + q4) * 12 + pos) * 16 + 8 * (p4 & 1);
            ba0[i] = a + m3 * L3_BSLOT; ba1[i] = a + (1 - m3) * L3_BSLOT;
            PIN(ba0[i]); PIN(ba1[i]);
        }
        unsigned vl16 = lane * 16;
        const unsigned sa3 = WS_H + w * 16384, sst = WS_ST + w * 2048;
        PIN(aa0); PIN(aa1); PIN(vl16);
        if (w == 0) wait_flags(flags + FL_2 * 32, 16, epoch, lane);
        BAR();
        STAMP(9);
        if (active && w == 0) { ISSUE_B(0); ISSUE_B(1); ISSUE_B(2); ISSUE_B(3); }
        if (tid == 0 && c == 0) __hip_atomic_store(flags + FL_GEN * 32, epoch, __ATOMIC_RELAXED, __HIP_MEMORY_SCOPE_AGENT);
        if (active) {
#define ISSUE_A(pr) do { const unsigned sa_ = sa3 + (((pr) + rot3) & 7) * 2048; DMA_SC1(wk, L3_A + ((pr) % 3) * 16384, vl16, rs_ws, sa_, 0); DMA_SC1(wk, L3_A + ((pr) % 3) * 16384 + 8192, vl16, rs_ws, sa_, 1024); } while (0)
            ISSUE_A(0); ISSUE_A(1); ISSUE_A(2);
            DMA_SC1(wk + wk, L3_ST, vl16, rs_ws, sst, 0); DMA_SC1(wk + wk, L3_ST + 1024, vl16, rs_ws, sst, 1024);
            f32x4 acc[4][3], cs[3] = {Z4, Z4, Z4};
#pragma unroll
            for (int t = 0; t < 4; ++t)
#pragma unroll
                for (int i = 0; i < 3; ++i) acc[t][i] = Z4;
            f32x2 st[4];
            f32x4 cvp[3];
            u32x4 af[2][4], bf[2][3];
            unsigned sa = L3_ST + (tid >> 8) * 8192 + (tid & 3) * 2048 + ((tid >> 2) & 63) * 8, sf = L3_ST + 512 + m3 * 8192 + r16 * 8;
            PIN(sa); PIN(sf);
#define LOAD3(s) do { const int pr_ = (s) >> 1, k2_ = (s) & 1; \
                bf[k2_][0] = tr_frag((k2_ ? ba1[0] : ba0[0]) + (pr_ & 3) * 2 * L3_BSLOT, 4 * 12 * 16); \
                _Pragma("unroll") for (int t = 0; t < 4; ++t) af[k2_][t] = lds128((k2_ ? aa1 : aa0) + (pr_ % 3) * 16384 + t * 1024); \
                _Pragma("unroll") for (int i = 1; i < 3; ++i) bf[k2_][i] = tr_frag((k2_ ? ba1[i] : ba0[i]) + (pr_ & 3) * 2 * L3_BSLOT, 4 * 12 * 16); } while (0)
#define JOB3(po, j) do { \
                if ((po) + 3 < 8 && (j) < 4) DMA_SC1(wk, L3_A + (((po) + 3) % 3) * 16384 + ((j) & 1) * 8192 + ((j) >> 1) * 4096, vl16, rs_ws, sa3 + (((po) + 3 + rot3) & 7) * 2048, ((j) & 1) * 1024 + ((j) >> 1) * 65536); \
                if ((po) + 4 < 8 && (j) >= 4 && (j) < 7) DMA(mg0[((j) >= 4 && (j) < 7) ? (j) - 4 : 0], (((po) + 4) & 3) * 2 * L3_BSLOT, vg0[((j) >= 4 && (j) < 7) ? (j) - 4 : 0], rs_g, (((po) + 4 + rot3) & 7) * (64 * FF * 2), 0); \
                if ((po) + 4 < 8 && (j) >= 7 && (j) < 10) DMA(mg0[((j) >= 7 && (j) < 10) ? (j) - 7 : 0], (((po) + 4) & 3) * 2 * L3_BSLOT + 6144, vg1[((j) >= 7 && (j) < 10) ? (j) - 7 : 0], rs_u, (((po) + 4 + rot3) & 7) * (64 * FF * 2), 0); \
                if ((po) >= 4) { const int jd_ = (po) == 4 ? (j) - 4 : (j); if (jd_ >= 0 && jd_ < 6) { const int s0_ = 12 + 6 * ((po) - 4) + 2 * ((jd_ >= 0 && jd_ < 6) ? jd_ >> 1 : 0); \
                    DMA(wk, s0_ * 4096 + (jd_ & 1) * 4096, vwd, rs_wd, swd, KS4(s0_) * 32768 + (jd_ & 1) * 32768); } } } while (0)
#define SLOT3(po, sl) do { __builtin_amdgcn_sched_barrier(0); if ((po) >= 0 && (po) < 7) { if (w < 4) { JOB3((po) >= 0 && (po) < 7 ? (po) : 0, 2 * (sl)); JOB3((po) >= 0 && (po) < 7 ? (po) : 0, 2 * (sl) + 1); } } __builtin_amdgcn_sched_barrier(0); } while (0)
            WAIT_VM(6);
            BAR();
            STAMP(10);
            LOAD3(0);
#pragma unroll
            for (int s = 0; s < 16; ++s) {
                const int po = (s - 1) >> 1, sl0 = (s & 1) ? 0 : 3;
                if (s & 1) {
                    if (w < 4) { if (s == 1) WAIT_VM(4); else if (s == 3) WAIT_VM(12); else if (s < 13) WAIT_VM(16); else if (s == 13) WAIT_VM(12); }
                    else { if (s == 1) WAIT_VM(4); else if (s == 3) WAIT_VM(2); else if (s == 5) WAIT_VM(0); }
                    BAR();
                }
                if (s == 15) {
                    ISSUE_S(); ISSUE_D2(0); ISSUE_D2(2); ISSUE_D2(4);
#pragma unroll
                    for (int i = 0; i < 3; ++i) cvp[i] = lds128f(L3_ST + 1024 + (3 * (w ^ 4) + i) * 64 + g * 16);
                }
                const int n_ = s + 1, pr_ = n_ >> 1, k2_ = n_ & 1, cb = s & 1, nb = n_ & 1;
                f32x2 pp[4];
                if (s < 15) {
                    bf[nb][0] = tr_frag((k2_ ? ba1[0] : ba0[0]) + (pr_ & 3) * 2 * L3_BSLOT, 4 * 12 * 16);
                    af[nb][0] = lds128((k2_ ? aa1 : aa0) + (pr_ % 3) * 16384); af[nb][1] = lds128((k2_ ? aa1 : aa0) + (pr_ % 3) * 16384 + 1024);
                }
#pragma unroll
                for (int t = 0; t < 4; ++t) acc[t][0] = mfma32(bf[cb][0], af[cb][t], acc[t][0]);
                if ((s & 1) == 0) cs[0] = mfma32(bf[cb][0], ONES, cs[0]);
#pragma unroll
                for (int i = 0; i < 4; ++i) { __builtin_amdgcn_sched_group_barrier(0x8, 1, 0); __builtin_amdgcn_sched_group_barrier(0x100, 1, 0); }
                SLOT3(po, sl0);
                if (s < 15) {
                    af[nb][2] = lds128((k2_ ? aa1 : aa0) + (pr_ % 3) * 16384 + 2048); af[nb][3] = lds128((k2_ ? aa1 : aa0) + (pr_ % 3) * 16384 + 3072);
                    bf[nb][1] = tr_frag((k2_ ? ba1[1] : ba0[1]) + (pr_ & 3) * 2 * L3_BSLOT, 4 * 12 * 16);
                }
#pragma unroll
                for (int t = 0; t < 4; ++t) acc[t][1] = mfma32(bf[cb][1], af[cb][t], acc[t][1]);
                if ((s & 1) == 0) cs[1] = mfma32(bf[cb][1], ONES, cs[1]);
#pragma unroll
                for (int i = 0; i < 4; ++i) { __builtin_amdgcn_sched_group_barrier(0x8, 1, 0); __builtin_amdgcn_sched_group_barrier(0x100, 1, 0); }
                SLOT3(po, sl0 + 1);
                if (s < 15) bf[nb][2] = tr_frag((k2_ ? ba1[2] : ba0[2]) + (pr_ & 3) * 2 * L3_BSLOT, 4 * 12 * 16);
                if (s == 6) { pp[0] = lds64f(sa); pp[1] = lds64f(sa + 512); pp[2] = lds64f(sa + 1024); pp[3] = lds64f(sa + 1536); }
                if (s == 12) {
#pragma unroll
                    for (int t = 0; t < 4; ++t) st[t] = lds64f(sf + t * 128);
                }
#pragma unroll
                for (int t = 0; t < 4; ++t) acc[t][2] = mfma32(bf[cb][2], af[cb][t], acc[t][2]);
                if ((s & 1) == 0) cs[2] = mfma32(bf[cb][2], ONES, cs[2]);
#pragma unroll
                for (int i = 0; i < 4; ++i) { __builtin_amdgcn_sched_group_barrier(0x8, 1, 0); __builtin_amdgcn_sched_group_barrier(0x100, 2, 0); }
                __builtin_amdgcn_sched_barrier(0);
                if (s == 6) {
                    const f32x2 sm = (pp[0] + pp[1]) + (pp[2] + pp[3]);
                    float sx = sm.x, sq = sm.y;
                    sx += __builtin_amdgcn_update_dpp(0.f, sx, 0xB1, 0xf, 0xf, false); sq += __builtin_amdgcn_update_dpp(0.f, sq, 0xB1, 0xf, 0xf, false);
                    sx += __builtin_amdgcn_update_dpp(0.f, sx, 0x4E, 0xf, 0xf, false); sq += __builtin_amdgcn_update_dpp(0.f, sq, 0x4E, 0xf, 0xf, false);
                    if ((lane & 3) == 0) *(LAS f32x2*)(sa + 512) = ln_stats(sx, sq);
                }
                if (s == 14 && r16 == 0) {
#pragma unroll
                    for (int i = 0; i < 3; ++i) *(LAS f32x4*)(L3_ST + 1024 + (3 * w + i) * 64 + g * 16) = cs[i];
                }
                SLOT3(po, sl0 + 2);
            }
            STAMP(11);
            const unsigned hi = lane >> 5;
            const float r01 = hi ? st[1].y : st[0].y, r23 = hi ? st[3].y : st[2].y;
            const float rr01 = r01 * r01, rr23 = r23 * r23;
            const unsigned tau = 2 * (g & 1) + (g >> 1);
#pragma unroll
            for (int i = 0; i < 3; ++i) {
                const f32x4 cv = cs[i] + cvp[i];
                u32x2 hp[2];
#pragma unroll
                for (int pp = 0; pp < 2; ++pp) {
                    const f32x4 e0 = acc[2 * pp][i] - st[2 * pp].x * cv, e1 = acc[2 * pp + 1][i] - st[2 * pp + 1].x * cv;
                    float hv[4];
#pragma unroll
                    for (int r = 0; r < 4; ++r) {
                        auto sw = __builtin_amdgcn_permlane32_swap(f2u(e0[r]), f2u(e1[r]), false, false);
                        const unsigned x0 = sw[0], x1 = sw[1];
                        hv[r] = __builtin_fmaxf(u2f(x0), 0.0f) * (u2f(x1) * (pp ? rr23 : rr01));
                    }
                    hp[pp] = (u32x2){pk_bf16(hv[0], hv[1]), pk_bf16(hv[2], hv[3])};
                }
                auto s0 = __builtin_amdgcn_permlane16_swap(hp[0].x, hp[1].x, false, false);
                auto s1 = __builtin_amdgcn_permlane16_swap(hp[0].y, hp[1].y, false, false);
                const u32x4 hf4 = {s0[0], s1[0], s0[1], s1[1]};
                const unsigned U = 12 * c + 3 * n3 + i;
                const unsigned soff = WS_HID + ((4 * m3) * NKS4 + (U >> 2)) * 1024 + (U & 3) * 256;
                const unsigned off = U < 172 ? tau * (NKS4 * 1024) + r16 * 16 + soff : 0xFFFFFFF0u;
                __builtin_amdgcn_raw_buffer_store_b128(hf4, rs_ws, off, 0, AUX_SC1);
            }
            asm volatile("" ::: "memory");
        }
    }
    STAMP(12);
    {
        const bool active = c < 15;
        if (!active) {
            ISSUE_D2(12); ISSUE_D2(14); ISSUE_D2(16); ISSUE_D2(18); ISSUE_D2(20); ISSUE_D2(22); ISSUE_D2(24); ISSUE_D2(26); ISSUE_D2(28);
            ISSUE_S();
            ISSUE_D2(0); ISSUE_D2(2); ISSUE_D2(4);
        }
        WAIT_VM(0);
        BAR();
        if (tid == 0 && active) __hip_atomic_store(flags + (FL_3 + c) * 32, epoch, __ATOMIC_RELAXED, __HIP_MEMORY_SCOPE_AGENT);
        STAMP(13);
#define REST_D() do { ISSUE_D2(6); ISSUE_D2(8); ISSUE_D2(10); ISSUE_D2(34); ISSUE_D2(36); ISSUE_D2(38); } while (0)
        if (w != 0) REST_D();
        const unsigned wb0 = (8 * g + q4) * 128 + (fsw * 32) + 8 * p4;
        u32x4 bq[11][4];
#pragma unroll
        for (int ct = 0; ct < 4; ++ct) {
            const u32x4 t0 = tr_frag((wb0 ^ (ct * 32)) + (30 + (kq4 < 3 ? kq4 : 0)) * 4096, 512);
            bq[0][ct] = kq4 < 3 ? t0 : (u32x4){0u, 0u, 0u, 0u};
        }
        BAR();
#define LAST_D() do { ISSUE_D2(30); DMA(wk, 32 * 4096, vwd, rs_wd, swd, 35 * 32768); } while (0)
        if (w != 0) LAST_D();
        unsigned wbk = wb0 + kq4 * (10 * 4096);
        unsigned abase = lane * 16;
        const unsigned asoff = WS_HID + ((4 * hf + 2 * rp4) * NKS4 + 11 * kq4 - (kq4 == 3 ? 1 : 0)) * 1024;
        PIN(wbk); PIN(abase);
        if (w == 0) wait_flags(flags + FL_3 * 32, 15, epoch, lane);
        BAR();
        STAMP(14);
        u32x4 af[2][11];
#define LOADA4(k) do { af[0][k] = __builtin_amdgcn_raw_buffer_load_b128(rs_ws, abase + ((k) & 3) * 1024, asoff + ((k) >> 2) * 4096, AUX_SC1); \
                       af[1][k] = __builtin_amdgcn_raw_buffer_load_b128(rs_ws, abase + ((k) & 3) * 1024, asoff + NKS4 * 1024 + ((k) >> 2) * 4096, AUX_SC1); } while (0)
        if (w == 0) { REST_D(); LAST_D(); }
        LOADA4(0); LOADA4(1); LOADA4(2);
        WAIT_VM(6);
        BAR();
#define LOAD4(j) do { _Pragma("unroll") for (int ct = 0; ct < 4; ++ct) bq[j][ct] = tr_frag((wbk ^ (ct * 32)) + ((j) - 1) * 4096, 512); } while (0)
        LOAD4(1); LOAD4(2);
        __builtin_amdgcn_sched_barrier(0);
        f32x4 acc[2][4] = {{Z4, Z4, Z4, Z4}, {Z4, Z4, Z4, Z4}};
#pragma unroll
        for (int j = 0; j < 11; ++j) {
            if (j + 3 < 11) { LOADA4(j + 3); LOAD4(j + 3); }
#pragma unroll
            for (int rl = 0; rl < 2; ++rl)
#pragma unroll
                for (int ct = 0; ct < 4; ++ct) acc[rl][ct] = mfma32(bq[j][ct], af[rl][j], acc[rl][ct]);
#pragma unroll
            for (int i = 0; i < 8; ++i) { __builtin_amdgcn_sched_group_barrier(0x8, 1, 0); __builtin_amdgcn_sched_group_barrier(0x100, 1, 0); }
            __builtin_amdgcn_sched_barrier(0);
        }
        STAMP(15);
        BAR();
        const unsigned red = w * 8192 + lane * 16;
#pragma unroll
        for (int rl = 0; rl < 2; ++rl)
#pragma unroll
            for (int ct = 0; ct < 4; ++ct) *(LAS f32x4*)(red + (rl * 4 + ct) * 1024) = acc[rl][ct];
        BAR();
        f32x4 a0 = Z4, a1 = Z4;
#pragma unroll
        for (int o = 0; o < 4; ++o) {
            const unsigned ra = (2 * rp4 + (o & 1) + 4 * (o >> 1)) * 8192 + lane * 16 + ((rt4 & 1) * 4 + 2 * kh) * 1024;
            a0 += lds128f(ra); a1 += lds128f(ra + 1024);
        }
        auto s0 = __builtin_amdgcn_permlane16_swap(pk_bf16(a0[0], a0[1]), pk_bf16(a1[0], a1[1]), false, false);
        auto s1 = __builtin_amdgcn_permlane16_swap(pk_bf16(a0[2], a0[3]), pk_bf16(a1[2], a1[3]), false, false);
        const u32x4 at = {s0[0], s1[0], s0[1], s1[1]};
        u32x4 o;
#pragma unroll
        for (int i = 0; i < 4; ++i) o[i] = pk_bf16(bf_lo(at[i]) + bf_lo(h_keep[i]), bf_hi(at[i]) + bf_hi(h_keep[i]));
        *(u32x4*)((char*)P.out + tok_off) = o;
    }
    STAMP(16);
}

}

extern "C" void kernel_launch(void* const* d_in, const int* in_sizes, int n_in, void* d_out, int out_size, void* d_ws, size_t ws_size, hipStream_t stream) {
    Params P;
    P.x = (const u16*)d_in[0]; P.wqkv = (const u16*)d_in[1]; P.wo = (const u16*)d_in[2];
    P.wg = (const u16*)d_in[4]; P.wu = (const u16*)d_in[5]; P.wd = (const u16*)d_in[6];
    P.out = (u16*)d_out; P.ws = (unsigned char*)d_ws;
    (void)hipFuncSetAttribute((const void*)fused_block, hipFuncAttributeMaxDynamicSharedMemorySize, LDS_BYTES);
    hipLaunchKernelGGL(fused_block, dim3(256), dim3(512), LDS_BYTES, stream, P);
}
```

```cpp
#include <hip/hip_runtime.h>
#include <hip/hip_bf16.h>
#include <stdint.h>
#pragma clang diagnostic ignored "-Wint-to-pointer-cast"

#define LAS __attribute__((address_space(3)))
#define GAS __attribute__((address_space(1)))
typedef unsigned short u16;
typedef unsigned long long u64;
typedef short s16x4 __attribute__((ext_vector_type(4)));
typedef __bf16 bf16x8 __attribute__((ext_vector_type(8)));
typedef __bf16 bf16x2 __attribute__((ext_vector_type(2)));
typedef float f32x4 __attribute__((ext_vector_type(4)));
typedef float f32x2 __attribute__((ext_vector_type(2)));
typedef unsigned u32x4 __attribute__((ext_vector_type(4)));
typedef unsigned u32x2 __attribute__((ext_vector_type(2)));
typedef GAS u64 gu64;
typedef __amdgpu_buffer_rsrc_t rsrc_t;

namespace {

constexpr int SEQ = 2048, DM = 512, FF = 1376;
constexpr int NKS4 = FF / 32;
constexpr unsigned WS_FLAG = 0;
constexpr unsigned WS_ST = 16384;
constexpr unsigned WS_O = 40960;
constexpr unsigned WS_H = WS_O + 131072;
constexpr unsigned WS_HID = WS_H + 131072;
constexpr unsigned WS_SPARE = WS_HID + 8 * 43 * 1024;
constexpr unsigned WS_BYTES = WS_SPARE + 8192;
constexpr int FL_GEN = 0, FL_1 = 8, FL_2 = 24, FL_3 = 40;
constexpr int AUX_SC1 = 16;
constexpr unsigned kSpinCap = 1u << 16;
#define TSTEP 4
constexpr unsigned CODE_KB = 22;

constexpr int LDS_BYTES = 163840;
constexpr int L1_X = 0, L1_H = 65536, L1_W = 81920, L1_WSLOT = 24576, L1_SCR = 155648;
constexpr int L2_WO = 81920, L2_RED = 114688;
constexpr int L3_A = 0, L3_B = 49152, L3_BSLOT = 12288, L3_ST = 147456;

__device__ __forceinline__ constexpr int vm_imm(int n) { return (n & 0xF) | ((n >> 4) << 14) | (0x7 << 4) | (0xF << 8); }
#define WAIT_VM(n) asm volatile("s_waitcnt vmcnt(%0)" :: "i"(n) : "memory")
#define BAR() do { asm volatile("s_waitcnt lgkmcnt(0)" ::: "memory"); __builtin_amdgcn_s_barrier(); asm volatile("" ::: "memory"); } while (0)
#define PIN(x) asm volatile("" : "+v"(x))
#define DMA(m0b, ldsoff, voff, rs, sbase, soffimm) do { unsigned t_; asm volatile("s_add_u32 m0, %1, %2\n\ts_add_u32 %0, %5, %6\n\tbuffer_load_dwordx4 %3, %4, %0 offen lds" \
    : "=&s"(t_) : "s"(m0b), "i"(ldsoff), "v"(voff), "s"(rs), "s"(sbase), "i"(soffimm) : "scc", "memory"); } while (0)
#define DMA_SC1(m0b, ldsoff, voff, rs, sbase, soffimm) do { unsigned t_; asm volatile("s_add_u32 m0, %1, %2\n\ts_add_u32 %0, %5, %6\n\tbuffer_load_dwordx4 %3, %4, %0 offen sc1 lds" \
    : "=&s"(t_) : "s"(m0b), "i"(ldsoff), "v"(voff), "s"(rs), "s"(sbase), "i"(soffimm) : "scc", "memory"); } while (0)

__device__ __forceinline__ rsrc_t mk_rsrc(const void* p, unsigned bytes) { return __builtin_amdgcn_make_buffer_rsrc((void*)p, 0, bytes, 0x00020000); }
__device__ __forceinline__ float u2f(unsigned u) { return __builtin_bit_cast(float, u); }
__device__ __forceinline__ unsigned f2u(float f) { return __builtin_bit_cast(unsigned, f); }
__device__ __forceinline__ unsigned pk_bf16(float a, float b) { bf16x2 v; v[0] = (__bf16)a; v[1] = (__bf16)b; return __builtin_bit_cast(unsigned, v); }
__device__ __forceinline__ float bf_lo(unsigned u) { return u2f(u << 16); }
__device__ __forceinline__ float bf_hi(unsigned u) { return u2f(u & 0xffff0000u); }
__device__ __forceinline__ float rbf(float f) { return (float)(__bf16)f; }
__device__ __forceinline__ f32x4 mfma32(u32x4 a, u32x4 b, f32x4 c) {
    return __builtin_amdgcn_mfma_f32_16x16x32_bf16(__builtin_bit_cast(bf16x8, a), __builtin_bit_cast(bf16x8, b), c, 0, 0, 0);
}
__device__ __forceinline__ f32x4 mfma16(u32x2 a, u32x2 b, f32x4 c) {
    return __builtin_amdgcn_mfma_f32_16x16x16bf16_1k(__builtin_bit_cast(s16x4, a), __builtin_bit_cast(s16x4, b), c, 0, 0, 0);
}
__device__ __forceinline__ u32x4 lds128(unsigned a) { return *(LAS u32x4*)a; }
__device__ __forceinline__ f32x4 lds128f(unsigned a) { return *(LAS f32x4*)a; }
__device__ __forceinline__ f32x2 lds64f(unsigned a) { return *(LAS f32x2*)a; }
__device__ __forceinline__ u32x2 tr_read(unsigned a) { return __builtin_bit_cast(u32x2, __builtin_amdgcn_ds_read_tr16_b64_v4i16((LAS s16x4*)a)); }
__device__ __forceinline__ u32x4 tr_frag(unsigned a, int step) { const u32x2 x = tr_read(a), y = tr_read(a + step); return (u32x4){x.x, x.y, y.x, y.y}; }
__device__ __forceinline__ float swap16_lo(float v) {
    const unsigned a = f2u(v); auto r = __builtin_amdgcn_permlane16_swap(a, a, false, false); const unsigned x = r[0]; return u2f(x);
}
__device__ __forceinline__ float xmax16_32(float v) {
    unsigned a = f2u(v);
    auto r = __builtin_amdgcn_permlane16_swap(a, a, false, false);
    const unsigned r0 = r[0], r1 = r[1];
    v = __builtin_fmaxf(u2f(r0), u2f(r1));
    a = f2u(v);
    auto s = __builtin_amdgcn_permlane32_swap(a, a, false, false);
    const unsigned s0 = s[0], s1 = s[1];
    return __builtin_fmaxf(u2f(s0), u2f(s1));
}
__device__ __forceinline__ float bcast_row0(float v) { return __builtin_amdgcn_update_dpp(0.f, v, 0x150, 0xf, 0xf, false); }
__device__ __forceinline__ float diag4(f32x4 d, unsigned lane) { const float a = (lane & 1) ? d[1] : d[0], b = (lane & 1) ? d[3] : d[2]; return (lane & 2) ? b : a; }
__device__ __forceinline__ f32x2 ln_stats(float s, float q) {
    const float mean = s * (1.0f / DM);
    const float var = __builtin_fmaxf(q * (1.0f / DM) - mean * mean, 0.0f);
    const float sd = rbf(__builtin_amdgcn_sqrtf(rbf(rbf(var) + 1e-5f)));
    return (f32x2){rbf(mean), __builtin_amdgcn_rcpf(sd)};
}
__device__ __forceinline__ void wait_flags(gu64* base, unsigned n, u64 epoch, unsigned lane) {
    for (unsigned spins = 0; spins < kSpinCap; ++spins) {
        u64 v = epoch;
        if (lane < n) v = __hip_atomic_load(base + lane * 32, __ATOMIC_RELAXED, __HIP_MEMORY_SCOPE_AGENT);
        if (__all(v == epoch)) break;
    }
    __builtin_amdgcn_fence(__ATOMIC_ACQUIRE, "wavefront");
}

struct Params { const u16* x; const u16* wqkv; const u16* wo; const u16* wg; const u16* wu; const u16* wd; u16* out; unsigned char* ws; };

#define STAMP(i) do {} while (0)

__global__ void __launch_bounds__(512, 2) __attribute__((amdgpu_waves_per_eu(2, 2))) fused_block(Params P
) {
    const unsigned tid = threadIdx.x, lane = tid & 63;
    const unsigned w = __builtin_amdgcn_readfirstlane(tid >> 6);
    const unsigned b = blockIdx.x, xcd = b & 7, jj = b >> 3;
    const unsigned p = 4 * (xcd >> 1) + (jj >> 3);
    const unsigned hf = (jj >> 2) & 1, hg = 4 * (xcd & 1) + (jj & 3);
    const unsigned c = 8 * hf + hg;
    const unsigned r16 = lane & 15, g = lane >> 4, l8 = lane >> 3, l7 = lane & 7, q4 = r16 >> 2, p4 = lane & 3;
    STAMP(0);
    {
        u64 pc; asm volatile("s_getpc_b64 %0" : "=s"(pc));
        const char* cp = (const char*)(pc & ~(u64)1023) + ((jj * 8 + w) % CODE_KB) * 1024 + lane * 16;
        asm volatile("s_mov_b32 m0, %0\n\ts_nop 0\n\tglobal_load_lds_dwordx4 %1, off" :: "s"(L1_SCR + 6144), "v"(cp) : "memory");
    }
    const unsigned wk = w * 1024;
    const rsrc_t rs_x = mk_rsrc(P.x, SEQ * DM * 2);
    const rsrc_t rs_w = mk_rsrc(P.wqkv, DM * 3 * DM * 2);
    unsigned char* const wsp = P.ws + (size_t)p * WS_BYTES;
    gu64* const flags = (gu64*)(wsp + WS_FLAG);
    const unsigned T0 = 128 * p + 64 * hf;
    STAMP(18);
    u32x4 ONES = {0x3F803F80u, 0x3F803F80u, 0x3F803F80u, 0x3F803F80u};
    PIN(ONES);
    const f32x4 Z4 = {0.f, 0.f, 0.f, 0.f};
    const unsigned fsw = ((q4 >> 1) & 1) | ((g & 1) << 1);
    const unsigned wsw = (l7 ^ (((g & 1) | ((w & 1) << 1)) << 1)) * 16;
    u64 gen = 0;
    const unsigned rt4 = w & 3, kh = w >> 2, rp4 = rt4 >> 1, kq4 = (rt4 & 1) + 2 * kh;
    const unsigned tok_off = (128 * p + 64 * hf + 32 * (rt4 & 1) + (rt4 >> 1) + 2 * r16) * 1024 + (64 * hg + 32 * kh + 16 * (g & 1) + 8 * (g >> 1)) * 2;
    u32x4 xr;

    {
        const unsigned rot = (2 * (jj & 3) + (jj >> 2) + (xcd >> 1)) & 7;
        const unsigned hh = w & 3, par = w >> 2;
        const unsigned vh = l8 * 2048 + ((l7 ^ g) * 16);
        const unsigned vxr = vh ^ ((w & 1) * 64);
        const unsigned sxr = (T0 + 32 * ((w >> 1) & 1) + (w >> 2) + 16 * (w & 1)) * 1024;
        const unsigned vw = (8 * w + l8) * 3072 + wsw + hg * 128;
        {
            const unsigned kap = (w + rot) & 7;
            const unsigned sh = (T0 == 0 ? 0 : T0 - 16) * 1024 + kap * 128;
            const unsigned hb = L1_H + w * 2048;
            if (lane >= 8) { DMA(hb, 0, vh, rs_x, sh, 0); const unsigned vh1 = vh ^ 64; DMA(hb, 1024, vh1, rs_x, sh, 1024); }
            else { *(LAS u32x4*)(hb + lane * 16) = ONES; *(LAS u32x4*)(hb + 1024 + lane * 16) = ONES; }
        }
        unsigned kx[8];
#pragma unroll
        for (int q = 0; q < 8; ++q) kx[q] = (q + rot) & 7;
#define TOUCH(ptr) asm volatile("s_mov_b32 m0, %0\n\ts_nop 0\n\tglobal_load_lds_dwordx4 %1, off" :: "s"(L1_SCR + 6144), "v"(ptr) : "memory")
#define ISSUE_X(q) DMA(wk, L1_X + (q) * 8192, vxr, rs_x, sxr + kx[q] * 128, 0)
#define ISSUE_W1(q, mat) DMA(wk, L1_W + ((q) % 3) * L1_WSLOT + (mat) * 8192, vw, rs_w, kx[q] * 196608, (mat) * 1024)
#define ISSUE_W(q) do { ISSUE_W1(q, 0); ISSUE_W1(q, 1); ISSUE_W1(q, 2); } while (0)
        ISSUE_X(0); ISSUE_W(0);
        __builtin_amdgcn_s_barrier();
        ISSUE_X(1); ISSUE_W(1);
        u64 genv;
        { const gu64* gp = flags + FL_GEN * 32; asm volatile("global_load_dwordx2 %0, %1, off sc1" : "=v"(genv) : "v"(gp) : "memory"); }
#define NTOUCH 4
        STAMP(17);
        const unsigned part = L1_SCR, fin = L1_SCR + 2560;
        const unsigned xa = (r16 >> 3) * 1024 + (r16 & 7) * 128 + ((g ^ ((r16 >> 1) & 7)) * 16);
        const rsrc_t rs_wo = mk_rsrc(P.wo, DM * DM * 2);
        WAIT_VM(5);
        BAR();
        STAMP(1);
        if (w < 4) {
            unsigned xc0 = xa, xc1 = xa ^ 64, xh0 = xa + L1_H, xh1 = xh0 ^ 64;
            unsigned wa = L1_W + (8 * g + q4) * 128 + ((hh ^ fsw) * 32) + 8 * p4, wa2 = wa + 2 * L1_WSLOT;
            PIN(xc0); PIN(xc1); PIN(xh0); PIN(xh1); PIN(wa); PIN(wa2);
            f32x4 aq[5], ak[5], av[5];
#pragma unroll
            for (int t = 0; t < 5; ++t) { aq[t] = Z4; ak[t] = Z4; av[t] = Z4; }
            u32x4 xf[2][5], wf[2][3];
#define LOADM1(s) do { const int kq_ = (s) >> 1, kk_ = (s) & 1; const unsigned wsl_ = (kq_ % 3 == 2 ? wa2 : wa + (kq_ % 3) * L1_WSLOT) + kk_ * 4096; \
                wf[kk_][0] = tr_frag(wsl_, 512); xf[kk_][0] = lds128((kk_ ? xh1 : xh0) + kq_ * 2048); \
                _Pragma("unroll") for (int t = 0; t < 4; ++t) xf[kk_][1 + t] = lds128((kk_ ? xc1 : xc0) + kq_ * 8192 + t * 2048); \
                wf[kk_][1] = tr_frag(wsl_ + 8192, 512); wf[kk_][2] = tr_frag(wsl_ + 16384, 512); } while (0)
            LOADM1(0);
#pragma unroll
            for (int s = 0; s < 16; ++s) {
                if ((s & 1) && s < 15) { if (s == 1) WAIT_VM(1); if (s == 9) WAIT_VM(0); BAR(); }
                if (s == 9) { asm volatile("" : "+v"(genv)); gen = ((u64)(unsigned)__builtin_amdgcn_readfirstlane((unsigned)(genv >> 32)) << 32) | (unsigned)__builtin_amdgcn_readfirstlane((unsigned)genv); }
                if (s < 15) LOADM1(s + 1);
#pragma unroll
                for (int t = 0; t < 5; ++t) aq[t] = mfma32(wf[s & 1][0], xf[s & 1][t], aq[t]);
#pragma unroll
                for (int t = 0; t < 5; ++t) ak[t] = mfma32(wf[s & 1][1], xf[s & 1][t], ak[t]);
#pragma unroll
                for (int t = 0; t < 5; ++t) av[t] = mfma32(xf[s & 1][t], wf[s & 1][2], av[t]);
#pragma unroll
                for (int i = 0; i < 11; ++i) { __builtin_amdgcn_sched_group_barrier(0x8, 1, 0); __builtin_amdgcn_sched_group_barrier(0x100, 1, 0); }
                __builtin_amdgcn_sched_group_barrier(0x8, 4, 0);
                __builtin_amdgcn_sched_barrier(0);
                if (s == 12) asm volatile("buffer_load_dwordx4 %0, %1, %2, 0 offen" : "=v"(xr) : "v"(tok_off), "s"(rs_x) : "memory");
            }
            STAMP(2);
            f32x4 csq, csk;
#pragma unroll
            for (int r = 0; r < 4; ++r) { csq[r] = bcast_row0(aq[0][r]); csk[r] = bcast_row0(ak[0][r]); }
            const float csv = swap16_lo(av[0][0]);
            u32x2 qb[5], kb[5], vb[5];
#pragma unroll
            for (int t = 0; t < 5; ++t) {
                const f32x2 sl = lds64f(fin + (t * 16 + r16) * 8);
                const f32x4 s01 = lds128f(fin + (t * 16 + 4 * g) * 8), s23 = lds128f(fin + (t * 16 + 4 * g) * 8 + 16);
                const f32x4 kv = (ak[t] - sl.x * csk) * sl.y;
                kb[t] = (u32x2){pk_bf16(kv[0], kv[1]), pk_bf16(kv[2], kv[3])};
                if (t > 0) { const f32x4 qv = (aq[t] - sl.x * csq) * sl.y; qb[t] = (u32x2){pk_bf16(qv[0], qv[1]), pk_bf16(qv[2], qv[3])}; }
                const f32x4 mn = {s01[0], s01[2], s23[0], s23[2]}, rd = {s01[1], s01[3], s23[1], s23[3]};
                const f32x4 vv = (av[t] - mn * csv) * rd;
                vb[t] = (u32x2){pk_bf16(vv[0], vv[1]), pk_bf16(vv[2], vv[3])};
            }
            f32x4 mh[2], mp, mc;
#pragma unroll
            for (int r = 0; r < 4; ++r) {
                const unsigned kap = 4 * g + r;
                mh[0][r] = (T0 > 0 && kap < 8 && kap >= r16 + 1) ? 0.f : -1e30f;
                mh[1][r] = (T0 > 0 && kap >= 8 && (kap & 7) >= r16 + 1) ? 0.f : -1e30f;
                mp[r] = (kap >= r16 + 9) ? 0.f : -1e30f;
                mc[r] = (kap <= r16 && r16 - kap <= 7) ? 0.f : -1e30f;
            }
            const float SC = 0.25f * 1.44269504088896f;
            const rsrc_t rs_ws1 = mk_rsrc(wsp, WS_BYTES);
#pragma unroll
            for (int pz = 0; pz < 2; ++pz) {
                u32x2 ob[2];
#pragma unroll
                for (int a = 0; a < 2; ++a) {
                    const int cur = 1 + 2 * pz + a, prev = a == 0 ? 0 : cur - 1;
                    f32x4 sp = mfma16(kb[prev], qb[cur], a == 0 ? mh[pz] : mp), sc = mfma16(kb[cur], qb[cur], mc);
                    float mx = __builtin_fmaxf(__builtin_fmaxf(__builtin_fmaxf(sp[0], sp[1]), __builtin_fmaxf(sp[2], sp[3])), __builtin_fmaxf(__builtin_fmaxf(sc[0], sc[1]), __builtin_fmaxf(sc[2], sc[3])));
                    mx = xmax16_32(mx);
                    const float mb = -mx * SC;
#pragma unroll
                    for (int r = 0; r < 4; ++r) { sp[r] = __builtin_amdgcn_exp2f(sp[r] * SC + mb); sc[r] = __builtin_amdgcn_exp2f(sc[r] * SC + mb); }
                    const u32x4 pf = {pk_bf16(sp[0], sp[1]), pk_bf16(sp[2], sp[3]), pk_bf16(sc[0], sc[1]), pk_bf16(sc[2], sc[3])};
                    const u32x4 vf = {vb[prev].x, vb[prev].y, vb[cur].x, vb[cur].y};
                    const f32x4 sm = mfma32(ONES, pf, Z4);
                    const f32x4 o = mfma32(vf, pf, Z4) * __builtin_amdgcn_rcpf(sm[0]);
                    ob[a] = (u32x2){pk_bf16(o[0], o[1]), pk_bf16(o[2], o[3])};
                }
                auto s0 = __builtin_amdgcn_permlane16_swap(ob[0].x, ob[1].x, false, false);
                auto s1 = __builtin_amdgcn_permlane16_swap(ob[0].y, ob[1].y, false, false);
                const u32x4 o_frag = {s0[0], s1[0], s0[1], s1[1]};
                const unsigned off = ((g & 1) * 16384) + ((2 * (hh & 1) + (g >> 1)) * 16 + r16) * 16;
                const unsigned soff = WS_O + ((4 * hf + 2 * pz) * 16 + 2 * hg + (hh >> 1)) * 1024;
                __builtin_amdgcn_raw_buffer_store_b128(o_frag, rs_ws1, off + soff, 0, AUX_SC1);
            }
            asm volatile("" ::: "memory");
        } else {
            const unsigned lw = w - 4, wk0 = lw * 1024;
            const unsigned sx0 = (T0 + 32 * ((lw >> 1) & 1) + 16 * (lw & 1)) * 1024;
            const unsigned vw0 = (8 * lw + l8) * 3072 + wsw + hg * 128, vwo0 = (8 * lw + l8) * 1024 + wsw + hg * 128;
#define X2(q) do { DMA(wk0, L1_X + (q) * 8192, vxr, rs_x, sx0 + kx[q] * 128, 0); DMA(wk0, L1_X + (q) * 8192 + 4096, vxr, rs_x, sx0 + kx[q] * 128, 1024); } while (0)
#define W6(q) do { _Pragma("unroll") for (int mat_ = 0; mat_ < 3; ++mat_) { DMA(wk0, L1_W + ((q) % 3) * L1_WSLOT + mat_ * 8192, vw0, rs_w, kx[q] * 196608, mat_ * 1024); \
                                                                            DMA(wk0, L1_W + ((q) % 3) * L1_WSLOT + mat_ * 8192 + 4096, vw0, rs_w, kx[q] * 196608, mat_ * 1024 + 98304); } } while (0)
#define O2(lds, m) do { DMA(wk0, (lds), vwo0, rs_wo, 0u, (m) * 65536); DMA(wk0, (lds) + 4096, vwo0, rs_wo, 0u, (m) * 65536 + 32768); } while (0)
            const unsigned sxa = xa ^ ((lw & 1) * 64);
            const unsigned stc = sxa + (lw >> 1) * 8192, sth = sxa + L1_H + (lw >> 1) * 2048;
            f32x4 gr[5], rs[5];
#pragma unroll
            for (int t = 0; t < 5; ++t) { gr[t] = Z4; rs[t] = Z4; }
#pragma unroll
            for (int s = 0; s < 16; ++s) {
                if ((s & 1) && s < 15) {
                    if (s == 1) WAIT_VM(9); else if (s == 3) WAIT_VM(12); else if (s == 5) WAIT_VM(14 + 2 * NTOUCH); else if (s == 7) WAIT_VM(10 + 2 * NTOUCH); else if (s < 13) WAIT_VM(6); else WAIT_VM(11);
                    BAR();
                    const int it = (s + 1) / 2;
                    if (it <= 5) W6(it + 2 > 7 ? 7 : it + 2);
                    if (it == 1) { X2(3); X2(4); X2(5); }
                    if (it == 2) { X2(6); X2(7); }
                    if (it == 6) { O2(L2_WO + 6 * 8192, 6); O2(L2_WO + 7 * 8192, 7); O2(0, 3); O2(8192, 4); O2(16384, 5); }
                    if (it == 7) { O2(L2_WO, 0); O2(L2_WO + 8192, 1); O2(L2_WO + 2 * 8192, 2); }
                    if (s == 9) { asm volatile("" : "+v"(genv)); gen = ((u64)(unsigned)__builtin_amdgcn_readfirstlane((unsigned)(genv >> 32)) << 32) | (unsigned)__builtin_amdgcn_readfirstlane((unsigned)genv); }
                }
                if (s == 0) { X2(2); W6(2); }
                if (s == 3) {
                    const char* t1 = lane < 22 ? (const char*)P.wg + lane * 65536 : (lane < 44 ? (const char*)P.wu + (lane - 22) * 65536 : (const char*)P.wd + (lane - 44) * 65536);
                    const char* t2 = lane < 8 ? (const char*)P.wo + lane * 65536 : (lane < 32 ? (const char*)P.out + p * 131072 + (lane - 8) * 4096 : (const char*)P.wd + (20 + (lane & 1)) * 65536);
                    const char* tw = lw == 0 ? (const char*)wsp + WS_FLAG + lane * 256 : (lw == 1 ? t1 : (lw == 2 ? t2 : (const char*)wsp + WS_SPARE + lane * 16));
                    TOUCH(tw);
                    const char* tz = (const char*)wsp + 32768u + lane * 16;
                    TOUCH(tz);
#pragma unroll
                    for (int i = 0; i < 6; ++i) {
                        unsigned qq = b * 24 + lw * 6 + i; if (qq >= 4640) qq -= 4640;
                        const char* base = qq < 512 ? (const char*)P.wo + qq * 1024 : (qq < 1888 ? (const char*)P.wg + (qq - 512) * 1024 : (qq < 3264 ? (const char*)P.wu + (qq - 1888) * 1024 : (const char*)P.wd + (qq - 3264) * 1024));
                        const char* t3 = base + lane * 16;
                        TOUCH(t3);
                    }
                }
                if (s == 5 || s == 6 || s == 7 || s == 9) {
                    const int j = s == 5 ? 0 : (s == 6 ? 1 : (s == 7 ? 2 : 3));
                    u32x4 sf[5];
                    sf[0] = lds128(sth + j * 4096);
#pragma unroll
                    for (int t = 0; t < 4; ++t) sf[1 + t] = lds128(stc + j * 16384 + t * 2048);
#pragma unroll
                    for (int t = 0; t < 5; ++t) { gr[t] = mfma32(sf[t], sf[t], gr[t]); rs[t] = mfma32(ONES, sf[t], rs[t]); }
                }
                if (s == 10 && g == q4) {
#pragma unroll
                    for (int t = 0; t < 5; ++t) *(LAS f32x2*)(part + ((t * 4 + lw) * 16 + r16) * 8) = (f32x2){rs[t][0], diag4(gr[t], lane)};
                }
                if (s == 11) {
                    const unsigned row = lw * 64 + lane < 80 ? lw * 64 + lane : 79;
                    const unsigned pa = part + ((row >> 4) * 64 + (row & 15)) * 8;
                    const f32x2 sm = (lds64f(pa) + lds64f(pa + 128)) + (lds64f(pa + 256) + lds64f(pa + 384));
                    *(LAS f32x2*)(fin + row * 8) = ln_stats(sm.x, sm.y);
                }
                if (s == 12) asm volatile("buffer_load_dwordx4 %0, %1, %2, 0 offen" : "=v"(xr) : "v"(tok_off), "s"(rs_x) : "memory");
            }
        }
    }
    STAMP(3);
    const rsrc_t rs_ws = mk_rsrc(wsp, WS_BYTES);
    const u64 epoch = gen + 1;
    const unsigned n3 = w & 3, m3 = w >> 2;
    const rsrc_t rs_g = mk_rsrc(P.wg, DM * FF * 2), rs_u = mk_rsrc(P.wu, DM * FF * 2);
    const rsrc_t rs_gu = mk_rsrc(m3 ? P.wu : P.wg, DM * FF * 2);
    unsigned vg0[3], vg1[3];
#pragma unroll
    for (int i = 0; i < 3; ++i) {
        const unsigned s = (n3 * 3 + i) * 64 + lane;
        const unsigned ks2 = s >= 384 ? 1 : 0, s2 = s - ks2 * 384, row = (s2 * 43691u) >> 19, pos = s2 - row * 12;
        const int umax = (int)(c < 15 ? 171 - 12 * c : 0);
        int u0 = (int)pos - (int)((row >> 3) & 1); if (u0 < 0) u0 += 12;
        int u1 = (int)pos - (int)(((row >> 3) & 1) + 2); if (u1 < 0) u1 += 12;
        const unsigned rb = (ks2 * 32 + row) * (FF * 2) + c * 192;
        vg0[i] = rb + min(u0, umax) * 16; vg1[i] = rb + min(u1, umax) * 16;
    }
    unsigned vgu[3], vgz[3];
#pragma unroll
    for (int i = 0; i < 3; ++i) {
        vgu[i] = m3 ? vg1[i] : vg0[i];
        const unsigned s = i * 64 + lane, row = (s * 43691u) >> 19, pos = s - row * 12;
        int u0 = (int)pos - (int)((row >> 3) & 1); if (u0 < 0) u0 += 12;
        vgz[i] = row * (FF * 2) + c * 192 + min(u0, (int)(c < 15 ? 171 - 12 * c : 0)) * 16;
    }
    unsigned mg0[3], mgu[3];
#pragma unroll
    for (int i = 0; i < 3; ++i) { const unsigned pi = n3 * 3 + i; mg0[i] = L3_B + (pi >= 6 ? L3_BSLOT + (pi - 6) * 1024 : pi * 1024); mgu[i] = mg0[i] + m3 * 6144; }
    const unsigned rot3 = (2 * (jj >> 3) + (jj & 7)) & 7;
#define ISSUE_B(pr) do { const unsigned sb_ = (((pr) + rot3) & 7) * (64 * FF * 2); _Pragma("unroll") for (int i_ = 0; i_ < 3; ++i_) DMA(mgu[i_], ((pr) & 3) * 2 * L3_BSLOT, vgu[i_], rs_gu, sb_, 0); } while (0)

    u32x4 h_keep;
    {
        WAIT_VM(0);
        BAR();
        if (tid == 0) __hip_atomic_store(flags + (FL_1 + c) * 32, epoch, __ATOMIC_RELAXED, __HIP_MEMORY_SCOPE_AGENT);
        STAMP(4);
        asm volatile("" : "+v"(xr));
        const unsigned wl = (8 * g + q4) * 128 + (fsw * 32) + 8 * p4;
        const unsigned wb0 = wl + (kh ? 8192 : L2_WO), wb1 = wl + (kh ? L2_WO + 49152 : L2_WO + 16384), wb2 = wl + (kh ? L2_WO + 57344 : 0);
        unsigned abase = lane * 16;
        const unsigned asoff = WS_O + ((4 * hf + rt4) * 16 + 8 * kh) * 1024;
        PIN(abase);
        u32x4 bf[8][4];
#pragma unroll
        for (int k = 0; k < 8; ++k)
#pragma unroll
            for (int ct = 0; ct < 4; ++ct) bf[k][ct] = tr_frag(((k < 4 ? wb0 : (k < 6 ? wb1 : wb2)) ^ (ct * 32)) + (k < 4 ? k : (k < 6 ? k - 4 : k - 6)) * 4096, 512);
        __builtin_amdgcn_sched_barrier(0);
        STAMP(19);
        if (w == 0) wait_flags(flags + (FL_1 + 8 * hf) * 32, 8, epoch, lane);
        BAR();
        STAMP(5);
        u32x4 af[8];
#pragma unroll
        for (int k = 0; k < 4; ++k) af[k] = __builtin_amdgcn_raw_buffer_load_b128(rs_ws, abase + (k & 3) * 1024, asoff + (k >> 2) * 4096, AUX_SC1);
        __builtin_amdgcn_sched_barrier(0);
        f32x4 acc[4] = {Z4, Z4, Z4, Z4};
#pragma unroll
        for (int k = 0; k < 8; ++k) {
            if (k + 4 < 8) af[k + 4] = __builtin_amdgcn_raw_buffer_load_b128(rs_ws, abase + (k & 3) * 1024, asoff + 4096, AUX_SC1);
            __builtin_amdgcn_sched_barrier(0);
#pragma unroll
            for (int ct = 0; ct < 4; ++ct) acc[ct] = mfma32(bf[k][ct], af[k], acc[ct]);
            __builtin_amdgcn_sched_barrier(0);
        }
        STAMP(6);
        const unsigned red = L2_RED + w * 2048 + lane * 16;
        *(LAS f32x4*)red = kh ? acc[0] : acc[2];
        *(LAS f32x4*)(red + 1024) = kh ? acc[1] : acc[3];
        BAR();
        const f32x4 a0 = (kh ? acc[2] : acc[0]) + lds128f(red ^ 8192);
        const f32x4 a1 = (kh ? acc[3] : acc[1]) + lds128f((red ^ 8192) + 1024);
        auto s0 = __builtin_amdgcn_permlane16_swap(pk_bf16(a0[0], a0[1]), pk_bf16(a1[0], a1[1]), false, false);
        auto s1 = __builtin_amdgcn_permlane16_swap(pk_bf16(a0[2], a0[3]), pk_bf16(a1[2], a1[3]), false, false);
        const u32x4 at = {s0[0], s1[0], s0[1], s1[1]};
#pragma unroll
        for (int i = 0; i < 4; ++i) h_keep[i] = pk_bf16(bf_lo(at[i]) + bf_lo(xr[i]), bf_hi(at[i]) + bf_hi(xr[i]));
        const unsigned hoff = ((2 * (g & 1) + (g >> 1)) * 16 + r16) * 16;
        const unsigned hsoff = WS_H + ((4 * hf + rt4) * 16 + 2 * hg + kh) * 1024;
        __builtin_amdgcn_raw_buffer_store_b128(h_keep, rs_ws, hoff + hsoff, 0, AUX_SC1);
        const f32x4 gs = mfma32(h_keep, h_keep, Z4), sm = mfma32(ONES, h_keep, Z4);
        const u32x2 stv = {f2u(sm[0]), f2u(diag4(gs, lane))};
        const unsigned stoff = (g == q4) ? WS_ST + c * 1024 + w * 128 + r16 * 8 : 0xFFFFFFF0u;
        __builtin_amdgcn_raw_buffer_store_b64(stv, rs_ws, stoff, 0, AUX_SC1);
        asm volatile("" ::: "memory");
    }
    STAMP(7);
    const rsrc_t rs_wd = mk_rsrc(P.wd, FF * DM * 2);
    const unsigned vwd = l8 * 1024 + wsw + hg * 128;
#define KS4(s) (11 * ((s) / 10) + (s) % 10 + 1 - ((s) >= 30 ? 1 : 0))
    const unsigned swd = ((w >> 2) * 32 + (w & 3) * 8) * 1024;
#define ISSUE_D2(s0) DMA(wk, (s0) * 4096, vwd, rs_wd, swd, KS4(s0) * 32768)
    const unsigned swd0 = (w & 3) * 8 * 1024;
#define ISSUE_S() do { DMA(wk, 30 * 4096, vwd, rs_wd, swd0 + (kh ? 11 : 0) * 32768, 0); DMA(wk, 32 * 4096, vwd, rs_wd, swd0 + (kh ? 36 : 22) * 32768, 0); } while (0)
    {
        const bool active = c < 15;
        WAIT_VM(0);
        BAR();
        if (tid == 0) __hip_atomic_store(flags + (FL_2 + c) * 32, epoch, __ATOMIC_RELAXED, __HIP_MEMORY_SCOPE_AGENT);
        if (active && w != 0) {
            ISSUE_B(0); ISSUE_B(1);
            if (w == 1 || w == 2) {
                const unsigned sb_ = ((w - 1 + rot3) & 7) * (64 * FF * 2), mz = L3_B + (w - 1) * 2 * L3_BSLOT;
                DMA(mz, 0, vgz[0], rs_g, sb_, 0); DMA(mz, 1024, vgz[1], rs_g, sb_, 0); DMA(mz, 2048, vgz[2], rs_g, sb_, 0);
            }
        }
        STAMP(8);
        unsigned aa = L3_A + lane * 16;
        unsigned ba[3];
#pragma unroll
        for (int i = 0; i < 3; ++i) {
            const unsigned mat = p4 >> 1;
            unsigned pos = 3 * n3 + i + (g & 1) + 2 * mat; if (pos >= 12) pos -= 12;
            ba[i] = L3_B + mat * 6144 + ((8 * g + q4) * 12 + pos) * 16 + 8 * (p4 & 1);
            PIN(ba[i]);
        }
        unsigned vl16 = lane * 16;
        const unsigned sa3 = WS_H + w * 16384;
        PIN(aa); PIN(vl16);
        if (w == 0) wait_flags(flags + FL_2 * 32, 16, epoch, lane);
        BAR();
        STAMP(9);
        if (tid == 0 && c == 0) __hip_atomic_store(flags + FL_GEN * 32, epoch, __ATOMIC_RELAXED, __HIP_MEMORY_SCOPE_AGENT);
        if (active) {
#define ISSUE_A(pr) do { const unsigned sa_ = sa3 + (((pr) + rot3) & 7) * 2048; DMA_SC1(wk, L3_A + ((pr) % 3) * 16384, vl16, rs_ws, sa_, 0); DMA_SC1(wk, L3_A + ((pr) % 3) * 16384 + 8192, vl16, rs_ws, sa_, 1024); } while (0)
            ISSUE_A(0); ISSUE_A(1);
            WAIT_VM(2);
            BAR();
            STAMP(10);
            if (w < 4) {
                f32x4 acc[8][3], cs[3] = {Z4, Z4, Z4};
#pragma unroll
                for (int t = 0; t < 8; ++t)
#pragma unroll
                    for (int i = 0; i < 3; ++i) acc[t][i] = Z4;
                u32x4 af[2][8], bf[2][3];
#define LOADM(s) do { const int pr_ = (s) >> 1, k2_ = (s) & 1; \
                    bf[k2_][0] = tr_frag(ba[0] + ((pr_ & 3) * 2 + k2_) * L3_BSLOT, 768); \
                    _Pragma("unroll") for (int t = 0; t < 8; ++t) af[k2_][t] = lds128(aa + (pr_ % 3) * 16384 + k2_ * 8192 + t * 1024); \
                    bf[k2_][1] = tr_frag(ba[1] + ((pr_ & 3) * 2 + k2_) * L3_BSLOT, 768); bf[k2_][2] = tr_frag(ba[2] + ((pr_ & 3) * 2 + k2_) * L3_BSLOT, 768); } while (0)
                LOADM(0);
#pragma unroll
                for (int s = 0; s < 16; ++s) {
                    if (s & 1) { if (s == 1) WAIT_VM(0); BAR(); }
                    if (s < 15) LOADM(s + 1);
#pragma unroll
                    for (int i = 0; i < 3; ++i) {
#pragma unroll
                        for (int t = 0; t < 8; ++t) acc[t][i] = mfma32(bf[s & 1][i], af[s & 1][t], acc[t][i]);
                        cs[i] = mfma32(bf[s & 1][i], ONES, cs[i]);
                    }
#pragma unroll
                    for (int i = 0; i < 14; ++i) { __builtin_amdgcn_sched_group_barrier(0x8, 1, 0); __builtin_amdgcn_sched_group_barrier(0x100, 1, 0); }
                    __builtin_amdgcn_sched_group_barrier(0x8, 13, 0);
                    __builtin_amdgcn_sched_barrier(0);
                }
                STAMP(11);
                const unsigned hi = lane >> 5;
                const unsigned tau = 2 * (g & 1) + (g >> 1);
#pragma unroll
                for (int m = 0; m < 2; ++m) {
                    f32x2 st[4];
#pragma unroll
                    for (int t = 0; t < 4; ++t) st[t] = lds64f(L3_ST + 512 + m * 8192 + t * 128 + r16 * 8);
                    const float r01 = hi ? st[1].y : st[0].y, r23 = hi ? st[3].y : st[2].y;
                    const float rr01 = r01 * r01, rr23 = r23 * r23;
#pragma unroll
                    for (int i = 0; i < 3; ++i) {
                        const f32x4 cv = cs[i];
                        u32x2 hp[2];
#pragma unroll
                        for (int pp = 0; pp < 2; ++pp) {
                            const f32x4 e0 = acc[4 * m + 2 * pp][i] - st[2 * pp].x * cv, e1 = acc[4 * m + 2 * pp + 1][i] - st[2 * pp + 1].x * cv;
                            float hv[4];
#pragma unroll
                            for (int r = 0; r < 4; ++r) {
                                auto sw = __builtin_amdgcn_permlane32_swap(f2u(e0[r]), f2u(e1[r]), false, false);
                                const unsigned x0 = sw[0], x1 = sw[1];
                                hv[r] = __builtin_fmaxf(u2f(x0), 0.0f) * (u2f(x1) * (pp ? rr23 : rr01));
                            }
                            hp[pp] = (u32x2){pk_bf16(hv[0], hv[1]), pk_bf16(hv[2], hv[3])};
                        }
                        auto s0 = __builtin_amdgcn_permlane16_swap(hp[0].x, hp[1].x, false, false);
                        auto s1 = __builtin_amdgcn_permlane16_swap(hp[0].y, hp[1].y, false, false);
                        const u32x4 hf4 = {s0[0], s1[0], s0[1], s1[1]};
                        const unsigned U = 12 * c + 3 * n3 + i;
                        const unsigned soff = WS_HID + ((4 * m) * NKS4 + (U >> 2)) * 1024 + (U & 3) * 256;
                        const unsigned off = U < 172 ? tau * (NKS4 * 1024) + r16 * 16 + soff : 0xFFFFFFF0u;
                        __builtin_amdgcn_raw_buffer_store_b128(hf4, rs_ws, off, 0, AUX_SC1);
                    }
                }
                asm volatile("" ::: "memory");
            } else {
                const unsigned lw = w - 4, wk0 = lw * 1024, sa30 = WS_H + lw * 16384;
#define A4(pr) do { const unsigned sa_ = sa30 + (((pr) + rot3) & 7) * 2048; const int o_ = L3_A + ((pr) % 3) * 16384; \
                    DMA_SC1(wk0, o_, vl16, rs_ws, sa_, 0); DMA_SC1(wk0, o_ + 8192, vl16, rs_ws, sa_, 1024); DMA_SC1(wk0, o_ + 4096, vl16, rs_ws, sa_, 65536); DMA_SC1(wk0, o_ + 12288, vl16, rs_ws, sa_, 65536 + 1024); } while (0)
#define B6(pr) do { const unsigned sb_ = (((pr) + rot3) & 7) * (64 * FF * 2); const int o_ = ((pr) & 3) * 2 * L3_BSLOT; \
                    _Pragma("unroll") for (int i_ = 0; i_ < 3; ++i_) DMA(mg0[i_], o_, vg0[i_], rs_g, sb_, 0); \
                    _Pragma("unroll") for (int i_ = 0; i_ < 3; ++i_) DMA(mg0[i_], o_ + 6144, vg1[i_], rs_u, sb_, 0); } while (0)
#define D4(s0) do { DMA(wk0, (s0) * 4096, vwd, rs_wd, swd0, KS4(s0) * 32768); DMA(wk0, (s0) * 4096 + 4096, vwd, rs_wd, swd0, KS4(s0) * 32768 + 32768); } while (0)
                const unsigned sa = L3_ST + (lane & 3) * 2048 + (lw * 16 + (lane >> 2)) * 8;
#pragma unroll
                for (int s = 0; s < 16; ++s) {
                    if (s & 1) {
                        if (s == 1 || s == 3) WAIT_VM(20); else if (s < 13) WAIT_VM(16); else if (s == 13) WAIT_VM(12);
                        BAR();
                        const int po = s >> 1;
                        if (po + 3 < 8) A4(po + 3 < 8 ? po + 3 : 7);
                        if (po + 4 < 8) B6(po + 4 < 8 ? po + 4 : 7);
                        if (po >= 4 && po < 7) { D4(12 + 6 * (po >= 4 ? po - 4 : 0)); D4(14 + 6 * (po >= 4 ? po - 4 : 0)); D4(16 + 6 * (po >= 4 ? po - 4 : 0)); }
                        if (s == 15) {
                            DMA(wk0, 30 * 4096, vwd, rs_wd, swd0, 0); DMA(wk0, 31 * 4096, vwd, rs_wd, swd0, 11 * 32768);
                            DMA(wk0, 32 * 4096, vwd, rs_wd, swd0, 22 * 32768); DMA(wk0, 33 * 4096, vwd, rs_wd, swd0, 36 * 32768);
                            D4(0); D4(2); D4(4);
                        }
                    }
                    if (s == 0) {
                        B6(2); A4(2); B6(3);
                        const unsigned sst = WS_ST + lw * 2048;
                        DMA_SC1(wk0 + wk0, L3_ST, vl16, rs_ws, sst, 0); DMA_SC1(wk0 + wk0, L3_ST + 1024, vl16, rs_ws, sst, 1024);
                        DMA_SC1(wk0 + wk0, L3_ST + 8192, vl16, rs_ws, sst, 8192); DMA_SC1(wk0 + wk0, L3_ST + 8192 + 1024, vl16, rs_ws, sst, 8192 + 1024);
                    }
                    if (s == 5) {
#pragma unroll
                        for (int hfz = 0; hfz < 2; ++hfz) {
                            const unsigned a = sa + hfz * 8192;
                            const f32x2 sm = (lds64f(a) + lds64f(a + 512)) + (lds64f(a + 1024) + lds64f(a + 1536));
                            float sx = sm.x, sq = sm.y;
                            sx += __builtin_amdgcn_update_dpp(0.f, sx, 0xB1, 0xf, 0xf, false); sq += __builtin_amdgcn_update_dpp(0.f, sq, 0xB1, 0xf, 0xf, false);
                            sx += __builtin_amdgcn_update_dpp(0.f, sx, 0x4E, 0xf, 0xf, false); sq += __builtin_amdgcn_update_dpp(0.f, sq, 0x4E, 0xf, 0xf, false);
                            *(LAS f32x2*)((a & ~(unsigned)6144) + 512) = ln_stats(sx, sq);
                        }
                    }
                }
            }
        }
    }
    STAMP(12);
    {
        const bool active = c < 15;
        if (!active) {
            ISSUE_D2(12); ISSUE_D2(14); ISSUE_D2(16); ISSUE_D2(18); ISSUE_D2(20); ISSUE_D2(22); ISSUE_D2(24); ISSUE_D2(26); ISSUE_D2(28);
            ISSUE_S();
            ISSUE_D2(0); ISSUE_D2(2); ISSUE_D2(4);
        }
        WAIT_VM(0);
        BAR();
        if (tid == 0 && active) __hip_atomic_store(flags + (FL_3 + c) * 32, epoch, __ATOMIC_RELAXED, __HIP_MEMORY_SCOPE_AGENT);
        STAMP(13);
#define REST_D() do { ISSUE_D2(6); ISSUE_D2(8); ISSUE_D2(10); ISSUE_D2(34); ISSUE_D2(36); ISSUE_D2(38); } while (0)
        if (w != 0) REST_D();
        const unsigned wb0 = (8 * g + q4) * 128 + (fsw * 32) + 8 * p4;
        u32x4 bq[11][4];
#pragma unroll
        for (int ct = 0; ct < 4; ++ct) {
            const u32x4 t0 = tr_frag((wb0 ^ (ct * 32)) + (30 + (kq4 < 3 ? kq4 : 0)) * 4096, 512);
            bq[0][ct] = kq4 < 3 ? t0 : (u32x4){0u, 0u, 0u, 0u};
        }
        BAR();
#define LAST_D() do { ISSUE_D2(30); DMA(wk, 32 * 4096, vwd, rs_wd, swd, 35 * 32768); } while (0)
        if (w != 0) LAST_D();
        unsigned wbk = wb0 + kq4 * (10 * 4096);
        unsigned abase = lane * 16;
        const unsigned asoff = WS_HID + ((4 * hf + 2 * rp4) * NKS4 + 11 * kq4 - (kq4 == 3 ? 1 : 0)) * 1024;
        PIN(wbk); PIN(abase);
        if (w == 0) wait_flags(flags + FL_3 * 32, 15, epoch, lane);
        BAR();
        STAMP(14);
        u32x4 af[2][11];
#define LOADA4(k) do { af[0][k] = __builtin_amdgcn_raw_buffer_load_b128(rs_ws, abase + ((k) & 3) * 1024, asoff + ((k) >> 2) * 4096, AUX_SC1); \
                       af[1][k] = __builtin_amdgcn_raw_buffer_load_b128(rs_ws, abase + ((k) & 3) * 1024, asoff + NKS4 * 1024 + ((k) >> 2) * 4096, AUX_SC1); } while (0)
        if (w == 0) { REST_D(); LAST_D(); }
        LOADA4(0); LOADA4(1); LOADA4(2);
        WAIT_VM(6);
        BAR();
#define LOAD4(j) do { _Pragma("unroll") for (int ct = 0; ct < 4; ++ct) bq[j][ct] = tr_frag((wbk ^ (ct * 32)) + ((j) - 1) * 4096, 512); } while (0)
        LOAD4(1); LOAD4(2);
        __builtin_amdgcn_sched_barrier(0);
        f32x4 acc[2][4] = {{Z4, Z4, Z4, Z4}, {Z4, Z4, Z4, Z4}};
#pragma unroll
        for (int j = 0; j < 11; ++j) {
            if (j + 3 < 11) { LOADA4(j + 3); LOAD4(j + 3); }
#pragma unroll
            for (int rl = 0; rl < 2; ++rl)
#pragma unroll
                for (int ct = 0; ct < 4; ++ct) acc[rl][ct] = mfma32(bq[j][ct], af[rl][j], acc[rl][ct]);
#pragma unroll
            for (int i = 0; i < 8; ++i) { __builtin_amdgcn_sched_group_barrier(0x8, 1, 0); __builtin_amdgcn_sched_group_barrier(0x100, 1, 0); }
            __builtin_amdgcn_sched_barrier(0);
        }
        STAMP(15);
        BAR();
        const unsigned red = w * 8192 + lane * 16;
#pragma unroll
        for (int rl = 0; rl < 2; ++rl)
#pragma unroll
            for (int ct = 0; ct < 4; ++ct) *(LAS f32x4*)(red + (rl * 4 + ct) * 1024) = acc[rl][ct];
        BAR();
        f32x4 a0 = Z4, a1 = Z4;
#pragma unroll
        for (int o = 0; o < 4; ++o) {
            const unsigned ra = (2 * rp4 + (o & 1) + 4 * (o >> 1)) * 8192 + lane * 16 + ((rt4 & 1) * 4 + 2 * kh) * 1024;
            a0 += lds128f(ra); a1 += lds128f(ra + 1024);
        }
        auto s0 = __builtin_amdgcn_permlane16_swap(pk_bf16(a0[0], a0[1]), pk_bf16(a1[0], a1[1]), false, false);
        auto s1 = __builtin_amdgcn_permlane16_swap(pk_bf16(a0[2], a0[3]), pk_bf16(a1[2], a1[3]), false, false);
        const u32x4 at = {s0[0], s1[0], s0[1], s1[1]};
        u32x4 o;
#pragma unroll
        for (int i = 0; i < 4; ++i) o[i] = pk_bf16(bf_lo(at[i]) + bf_lo(h_keep[i]), bf_hi(at[i]) + bf_hi(h_keep[i]));
        *(u32x4*)((char*)P.out + tok_off) = o;
    }
    STAMP(16);
}

}

extern "C" void kernel_launch(void* const* d_in, const int* in_sizes, int n_in, void* d_out, int out_size, void* d_ws, size_t ws_size, hipStream_t stream) {
    Params P;
    P.x = (const u16*)d_in[0]; P.wqkv = (const u16*)d_in[1]; P.wo = (const u16*)d_in[2];
    P.wg = (const u16*)d_in[4]; P.wu = (const u16*)d_in[5]; P.wd = (const u16*)d_in[6];
    P.out = (u16*)d_out; P.ws = (unsigned char*)d_ws;
    (void)hipFuncSetAttribute((const void*)fused_block, hipFuncAttributeMaxDynamicSharedMemorySize, LDS_BYTES);
    hipLaunchKernelGGL(fused_block, dim3(256), dim3(512), LDS_BYTES, stream, P);
}
```

```cpp
#include <hip/hip_runtime.h>
#include <hip/hip_bf16.h>
#include <stdint.h>
#pragma clang diagnostic ignored "-Wint-to-pointer-cast"

#define LAS __attribute__((address_space(3)))
#define GAS __attribute__((address_space(1)))
typedef unsigned short u16;
typedef unsigned long long u64;
typedef short s16x4 __attribute__((ext_vector_type(4)));
typedef __bf16 bf16x8 __attribute__((ext_vector_type(8)));
typedef __bf16 bf16x2 __attribute__((ext_vector_type(2)));
typedef float f32x4 __attribute__((ext_vector_type(4)));
typedef float f32x2 __attribute__((ext_vector_type(2)));
typedef unsigned u32x4 __attribute__((ext_vector_type(4)));
typedef unsigned u32x2 __attribute__((ext_vector_type(2)));
typedef GAS u64 gu64;
typedef __amdgpu_buffer_rsrc_t rsrc_t;

namespace {

constexpr int SEQ = 2048, DM = 512, FF = 1376;
constexpr int NKS4 = FF / 32;
constexpr unsigned WS_FLAG = 0;
constexpr unsigned WS_ST = 16384;
constexpr unsigned WS_O = 40960;
constexpr unsigned WS_H = WS_O + 131072;
constexpr unsigned WS_HID = WS_H + 131072;
constexpr unsigned WS_SPARE = WS_HID + 8 * 43 * 1024;
constexpr unsigned WS_BYTES = WS_SPARE + 8192;
constexpr int FL_GEN = 0, FL_1 = 8, FL_2 = 24, FL_3 = 40;
constexpr int AUX_SC1 = 16;
constexpr unsigned kSpinCap = 1u << 16;
constexpr unsigned CODE_KB = 29;

constexpr int LDS_BYTES = 163840;
constexpr int L1_X = 0, L1_H = 65536, L1_W = 81920, L1_WSLOT = 24576, L1_SCR = 155648;
constexpr int L2_WO = 81920, L2_RED = 114688;
constexpr int L3_A = 0, L3_B = 49152, L3_BSLOT = 12288, L3_ST = 147456;

#define WAIT_VM(n) asm volatile("s_waitcnt vmcnt(%0)" :: "i"(n) : "memory")
#define BAR() do { asm volatile("s_waitcnt lgkmcnt(0)" ::: "memory"); __builtin_amdgcn_s_barrier(); asm volatile("" ::: "memory"); } while (0)
#define PIN(x) asm volatile("" : "+v"(x))
#define DMA(m0b, ldsoff, voff, rs, sbase, soffimm) do { unsigned t_; asm volatile("s_add_u32 m0, %1, %2\n\ts_add_u32 %0, %5, %6\n\tbuffer_load_dwordx4 %3, %4, %0 offen lds" \
    : "=&s"(t_) : "s"(m0b), "i"(ldsoff), "v"(voff), "s"(rs), "s"(sbase), "i"(soffimm) : "scc", "memory"); } while (0)
#define DMA_SC1(m0b, ldsoff, voff, rs, sbase, soffimm) do { unsigned t_; asm volatile("s_add_u32 m0, %1, %2\n\ts_add_u32 %0, %5, %6\n\tbuffer_load_dwordx4 %3, %4, %0 offen sc1 lds" \
    : "=&s"(t_) : "s"(m0b), "i"(ldsoff), "v"(voff), "s"(rs), "s"(sbase), "i"(soffimm) : "scc", "memory"); } while (0)

__device__ __forceinline__ rsrc_t mk_rsrc(const void* p, unsigned bytes) { return __builtin_amdgcn_make_buffer_rsrc((void*)p, 0, bytes, 0x00020000); }
__device__ __forceinline__ float u2f(unsigned u) { return __builtin_bit_cast(float, u); }
__device__ __forceinline__ unsigned f2u(float f) { return __builtin_bit_cast(unsigned, f); }
__device__ __forceinline__ unsigned pk_bf16(float a, float b) { bf16x2 v; v[0] = (__bf16)a; v[1] = (__bf16)b; return __builtin_bit_cast(unsigned, v); }
__device__ __forceinline__ float bf_lo(unsigned u) { return u2f(u << 16); }
__device__ __forceinline__ float bf_hi(unsigned u) { return u2f(u & 0xffff0000u); }
__device__ __forceinline__ float rbf(float f) { return (float)(__bf16)f; }
__device__ __forceinline__ f32x4 mfma32(u32x4 a, u32x4 b, f32x4 c) {
    return __builtin_amdgcn_mfma_f32_16x16x32_bf16(__builtin_bit_cast(bf16x8, a), __builtin_bit_cast(bf16x8, b), c, 0, 0, 0);
}
__device__ __forceinline__ f32x4 mfma16(u32x2 a, u32x2 b, f32x4 c) {
    return __builtin_amdgcn_mfma_f32_16x16x16bf16_1k(__builtin_bit_cast(s16x4, a), __builtin_bit_cast(s16x4, b), c, 0, 0, 0);
}
__device__ __forceinline__ u32x4 lds128(unsigned a) { return *(LAS u32x4*)a; }
__device__ __forceinline__ f32x4 lds128f(unsigned a) { return *(LAS f32x4*)a; }
__device__ __forceinline__ f32x2 lds64f(unsigned a) { return *(LAS f32x2*)a; }
__device__ __forceinline__ u32x2 tr_read(unsigned a) { return __builtin_bit_cast(u32x2, __builtin_amdgcn_ds_read_tr16_b64_v4i16((LAS s16x4*)a)); }
__device__ __forceinline__ u32x4 tr_frag(unsigned a, int step) { const u32x2 x = tr_read(a), y = tr_read(a + step); return (u32x4){x.x, x.y, y.x, y.y}; }
__device__ __forceinline__ float swap16_lo(float v) {
    const unsigned a = f2u(v); auto r = __builtin_amdgcn_permlane16_swap(a, a, false, false); const unsigned x = r[0]; return u2f(x);
}
__device__ __forceinline__ float xmax16_32(float v) {
    unsigned a = f2u(v);
    auto r = __builtin_amdgcn_permlane16_swap(a, a, false, false);
    const unsigned r0 = r[0], r1 = r[1];
    v = __builtin_fmaxf(u2f(r0), u2f(r1));
    a = f2u(v);
    auto s = __builtin_amdgcn_permlane32_swap(a, a, false, false);
    const unsigned s0 = s[0], s1 = s[1];
    return __builtin_fmaxf(u2f(s0), u2f(s1));
}
__device__ __forceinline__ float bcast_row0(float v) { return __builtin_amdgcn_update_dpp(0.f, v, 0x150, 0xf, 0xf, false); }
__device__ __forceinline__ float diag4(f32x4 d, unsigned lane) { const float a = (lane & 1) ? d[1] : d[0], b = (lane & 1) ? d[3] : d[2]; return (lane & 2) ? b : a; }
__device__ __forceinline__ f32x2 ln_stats(float s, float q) {
    const float mean = s * (1.0f / DM);
    const float var = __builtin_fmaxf(q * (1.0f / DM) - mean * mean, 0.0f);
    const float sd = rbf(__builtin_amdgcn_sqrtf(rbf(rbf(var) + 1e-5f)));
    return (f32x2){rbf(mean), __builtin_amdgcn_rcpf(sd)};
}
__device__ __forceinline__ void wait_flags(gu64* base, unsigned n, u64 epoch, unsigned lane) {
    for (unsigned spins = 0; spins < kSpinCap; ++spins) {
        u64 v = epoch;
        if (lane < n) v = __hip_atomic_load(base + lane * 32, __ATOMIC_RELAXED, __HIP_MEMORY_SCOPE_AGENT);
        if (__all(v == epoch)) break;
    }
    __builtin_amdgcn_fence(__ATOMIC_ACQUIRE, "wavefront");
}

struct Params { const u16* x; const u16* wqkv; const u16* wo; const u16* wg; const u16* wu; const u16* wd; u16* out; unsigned char* ws; };

__global__ void __launch_bounds__(512, 2) __attribute__((amdgpu_waves_per_eu(2, 2))) fused_block(Params P) {
    const unsigned tid = threadIdx.x, lane = tid & 63;
    const unsigned w = __builtin_amdgcn_readfirstlane(tid >> 6);
    const unsigned b = blockIdx.x, xcd = b & 7, jj = b >> 3;
    const unsigned p = 4 * (xcd >> 1) + (jj >> 3);
    const unsigned hf = (jj >> 2) & 1, hg = 4 * (xcd & 1) + (jj & 3);
    const unsigned c = 8 * hf + hg;
    const unsigned r16 = lane & 15, g = lane >> 4, l8 = lane >> 3, l7 = lane & 7, q4 = r16 >> 2, p4 = lane & 3;
    {
        u64 pc; asm volatile("s_getpc_b64 %0" : "=s"(pc));
        const char* cp = (const char*)(pc & ~(u64)1023) + ((jj * 8 + w) % CODE_KB) * 1024 + lane * 16;
        asm volatile("s_mov_b32 m0, %0\n\ts_nop 0\n\tglobal_load_lds_dwordx4 %1, off" :: "s"(L1_SCR + 6144), "v"(cp) : "memory");
    }
    const unsigned wk = w * 1024;
    const rsrc_t rs_x = mk_rsrc(P.x, SEQ * DM * 2);
    const rsrc_t rs_w = mk_rsrc(P.wqkv, DM * 3 * DM * 2);
    unsigned char* const wsp = P.ws + (size_t)p * WS_BYTES;
    gu64* const flags = (gu64*)(wsp + WS_FLAG);
    const unsigned T0 = 128 * p + 64 * hf;
    u32x4 ONES = {0x3F803F80u, 0x3F803F80u, 0x3F803F80u, 0x3F803F80u};
    PIN(ONES);
    const f32x4 Z4 = {0.f, 0.f, 0.f, 0.f};
    const unsigned fsw = ((q4 >> 1) & 1) | ((g & 1) << 1);
    const unsigned wsw = (l7 ^ (((g & 1) | ((w & 1) << 1)) << 1)) * 16;
    u64 gen = 0;
    const unsigned rt4 = w & 3, kh = w >> 2, rp4 = rt4 >> 1, kq4 = (rt4 & 1) + 2 * kh;
    const unsigned tok_off = (128 * p + 64 * hf + 32 * (rt4 & 1) + (rt4 >> 1) + 2 * r16) * 1024 + (64 * hg + 32 * kh + 16 * (g & 1) + 8 * (g >> 1)) * 2;

    {
        const unsigned rot = (2 * (jj & 3) + (jj >> 2) + (xcd >> 1)) & 7;
        const unsigned hh = w & 3;
        const unsigned vh = l8 * 2048 + ((l7 ^ g) * 16);
        const unsigned vxr = vh ^ ((w & 1) * 64);
        const unsigned sxr = (T0 + 32 * ((w >> 1) & 1) + (w >> 2) + 16 * (w & 1)) * 1024;
        const unsigned vw = (8 * w + l8) * 3072 + wsw + hg * 128;
        {
            const unsigned kap = (w + rot) & 7;
            const unsigned sh = (T0 == 0 ? 0 : T0 - 16) * 1024 + kap * 128;
            const unsigned hb = L1_H + w * 2048;
            if (lane >= 8) { DMA(hb, 0, vh, rs_x, sh, 0); const unsigned vh1 = vh ^ 64; DMA(hb, 1024, vh1, rs_x, sh, 1024); }
            else { *(LAS u32x4*)(hb + lane * 16) = ONES; *(LAS u32x4*)(hb + 1024 + lane * 16) = ONES; }
        }
        unsigned kx[8];
#pragma unroll
        for (int q = 0; q < 8; ++q) kx[q] = (q + rot) & 7;
#define TOUCH(ptr) asm volatile("s_mov_b32 m0, %0\n\ts_nop 0\n\tglobal_load_lds_dwordx4 %1, off" :: "s"(L1_SCR + 6144), "v"(ptr) : "memory")
#define ISSUE_X(q) DMA(wk, L1_X + (q) * 8192, vxr, rs_x, sxr + kx[q] * 128, 0)
#define ISSUE_W1(q, mat) DMA(wk, L1_W + ((q) % 3) * L1_WSLOT + (mat) * 8192, vw, rs_w, kx[q] * 196608, (mat) * 1024)
#define ISSUE_W(q) do { ISSUE_W1(q, 0); ISSUE_W1(q, 1); ISSUE_W1(q, 2); } while (0)
        ISSUE_X(0); ISSUE_W(0);
        __builtin_amdgcn_s_barrier();
        ISSUE_X(1); ISSUE_W(1);
        { const char* gp = (const char*)wsp + WS_FLAG + lane * 16; asm volatile("s_mov_b32 m0, %0\n\ts_nop 0\n\tglobal_load_lds_dwordx4 %1, off sc1" :: "s"(L1_SCR + 7168), "v"(gp) : "memory"); }
#define READ_GEN() do { const u32x2 gv_ = *(LAS u32x2*)(L1_SCR + 7168 + FL_GEN * 256); gen = ((u64)(unsigned)__builtin_amdgcn_readfirstlane(gv_.y) << 32) | (unsigned)__builtin_amdgcn_readfirstlane(gv_.x); } while (0)
#define NTOUCH 4
        const unsigned part = L1_SCR, fin = L1_SCR + 2560;
        const unsigned xa = (r16 >> 3) * 1024 + (r16 & 7) * 128 + ((g ^ ((r16 >> 1) & 7)) * 16);
        const rsrc_t rs_wo = mk_rsrc(P.wo, DM * DM * 2);
        WAIT_VM(5);
        BAR();
        if (w < 4) {
            unsigned xc0 = xa, xc1 = xa ^ 64, xh0 = xa + L1_H, xh1 = xh0 ^ 64;
            unsigned wa = L1_W + (8 * g + q4) * 128 + ((hh ^ fsw) * 32) + 8 * p4, wa2 = wa + 2 * L1_WSLOT;
            PIN(xc0); PIN(xc1); PIN(xh0); PIN(xh1); PIN(wa); PIN(wa2);
            f32x4 aq[5], ak[5], av[5];
#pragma unroll
            for (int t = 0; t < 5; ++t) { aq[t] = Z4; ak[t] = Z4; av[t] = Z4; }
            u32x4 xf[2][5], wf[2][3];
#define LOADM1(s) do { const int kq_ = (s) >> 1, kk_ = (s) & 1; const unsigned wsl_ = (kq_ % 3 == 2 ? wa2 : wa + (kq_ % 3) * L1_WSLOT) + kk_ * 4096; \
                wf[kk_][0] = tr_frag(wsl_, 512); xf[kk_][0] = lds128((kk_ ? xh1 : xh0) + kq_ * 2048); \
                _Pragma("unroll") for (int t = 0; t < 4; ++t) xf[kk_][1 + t] = lds128((kk_ ? xc1 : xc0) + kq_ * 8192 + t * 2048); \
                wf[kk_][1] = tr_frag(wsl_ + 8192, 512); wf[kk_][2] = tr_frag(wsl_ + 16384, 512); } while (0)
            LOADM1(0);
#pragma unroll
            for (int s = 0; s < 16; ++s) {
                if ((s & 1) && s < 15) { if (s == 1) WAIT_VM(1); if (s == 9) WAIT_VM(0); BAR(); }
                if (s == 9) READ_GEN();
                if (s < 15) LOADM1(s + 1);
#pragma unroll
                for (int t = 0; t < 5; ++t) aq[t] = mfma32(wf[s & 1][0], xf[s & 1][t], aq[t]);
#pragma unroll
                for (int t = 0; t < 5; ++t) ak[t] = mfma32(wf[s & 1][1], xf[s & 1][t], ak[t]);
#pragma unroll
                for (int t = 0; t < 5; ++t) av[t] = mfma32(xf[s & 1][t], wf[s & 1][2], av[t]);
#pragma unroll
                for (int i = 0; i < 11; ++i) { __builtin_amdgcn_sched_group_barrier(0x8, 1, 0); __builtin_amdgcn_sched_group_barrier(0x100, 1, 0); }
                __builtin_amdgcn_sched_group_barrier(0x8, 4, 0);
                __builtin_amdgcn_sched_barrier(0);
            }
            f32x4 csq, csk;
#pragma unroll
            for (int r = 0; r < 4; ++r) { csq[r] = bcast_row0(aq[0][r]); csk[r] = bcast_row0(ak[0][r]); }
            const float csv = swap16_lo(av[0][0]);
            u32x2 qb[5], kb[5], vb[5];
#pragma unroll
            for (int t = 0; t < 5; ++t) {
                const f32x2 sl = lds64f(fin + (t * 16 + r16) * 8);
                const f32x4 s01 = lds128f(fin + (t * 16 + 4 * g) * 8), s23 = lds128f(fin + (t * 16 + 4 * g) * 8 + 16);
                const f32x4 kv = (ak[t] - sl.x * csk) * sl.y;
                kb[t] = (u32x2){pk_bf16(kv[0], kv[1]), pk_bf16(kv[2], kv[3])};
                if (t > 0) { const f32x4 qv = (aq[t] - sl.x * csq) * sl.y; qb[t] = (u32x2){pk_bf16(qv[0], qv[1]), pk_bf16(qv[2], qv[3])}; }
                const f32x4 mn = {s01[0], s01[2], s23[0], s23[2]}, rd = {s01[1], s01[3], s23[1], s23[3]};
                const f32x4 vv = (av[t] - mn * csv) * rd;
                vb[t] = (u32x2){pk_bf16(vv[0], vv[1]), pk_bf16(vv[2], vv[3])};
            }
            f32x4 mh[2], mp, mc;
#pragma unroll
            for (int r = 0; r < 4; ++r) {
                const unsigned kap = 4 * g + r;
                mh[0][r] = (T0 > 0 && kap < 8 && kap >= r16 + 1) ? 0.f : -1e30f;
                mh[1][r] = (T0 > 0 && kap >= 8 && (kap & 7) >= r16 + 1) ? 0.f : -1e30f;
                mp[r] = (kap >= r16 + 9) ? 0.f : -1e30f;
                mc[r] = (kap <= r16 && r16 - kap <= 7) ? 0.f : -1e30f;
            }
            const float SC = 0.25f * 1.44269504088896f;
            const rsrc_t rs_ws1 = mk_rsrc(wsp, WS_BYTES);
#pragma unroll
            for (int pz = 0; pz < 2; ++pz) {
                u32x2 ob[2];
#pragma unroll
                for (int a = 0; a < 2; ++a) {
                    const int cur = 1 + 2 * pz + a, prev = a == 0 ? 0 : cur - 1;
                    f32x4 sp = mfma16(kb[prev], qb[cur], a == 0 ? mh[pz] : mp), sc = mfma16(kb[cur], qb[cur], mc);
                    float mx = __builtin_fmaxf(__builtin_fmaxf(__builtin_fmaxf(sp[0], sp[1]), __builtin_fmaxf(sp[2], sp[3])), __builtin_fmaxf(__builtin_fmaxf(sc[0], sc[1]), __builtin_fmaxf(sc[2], sc[3])));
                    mx = xmax16_32(mx);
                    const float mb = -mx * SC;
#pragma unroll
                    for (int r = 0; r < 4; ++r) { sp[r] = __builtin_amdgcn_exp2f(sp[r] * SC + mb); sc[r] = __builtin_amdgcn_exp2f(sc[r] * SC + mb); }
                    const u32x4 pf = {pk_bf16(sp[0], sp[1]), pk_bf16(sp[2], sp[3]), pk_bf16(sc[0], sc[1]), pk_bf16(sc[2], sc[3])};
                    const u32x4 vf = {vb[prev].x, vb[prev].y, vb[cur].x, vb[cur].y};
                    const f32x4 sm = mfma32(ONES, pf, Z4);
                    const f32x4 o = mfma32(vf, pf, Z4) * __builtin_amdgcn_rcpf(sm[0]);
                    ob[a] = (u32x2){pk_bf16(o[0], o[1]), pk_bf16(o[2], o[3])};
                }
                auto s0 = __builtin_amdgcn_permlane16_swap(ob[0].x, ob[1].x, false, false);
                auto s1 = __builtin_amdgcn_permlane16_swap(ob[0].y, ob[1].y, false, false);
                const u32x4 o_frag = {s0[0], s1[0], s0[1], s1[1]};
                const unsigned off = ((g & 1) * 16384) + ((2 * (hh & 1) + (g >> 1)) * 16 + r16) * 16;
                const unsigned soff = WS_O + ((4 * hf + 2 * pz) * 16 + 2 * hg + (hh >> 1)) * 1024;
                __builtin_amdgcn_raw_buffer_store_b128(o_frag, rs_ws1, off + soff, 0, AUX_SC1);
            }
            asm volatile("" ::: "memory");
        } else {
            const unsigned lw = w - 4, wk0 = lw * 1024;
            const unsigned sx0 = (T0 + 32 * ((lw >> 1) & 1) + 16 * (lw & 1)) * 1024;
            const unsigned vw0 = (8 * lw + l8) * 3072 + wsw + hg * 128, vwo0 = (8 * lw + l8) * 1024 + wsw + hg * 128;
#define X2(q) do { DMA(wk0, L1_X + (q) * 8192, vxr, rs_x, sx0 + kx[q] * 128, 0); DMA(wk0, L1_X + (q) * 8192 + 4096, vxr, rs_x, sx0 + kx[q] * 128, 1024); } while (0)
#define W6(q) do { _Pragma("unroll") for (int mat_ = 0; mat_ < 3; ++mat_) { DMA(wk0, L1_W + ((q) % 3) * L1_WSLOT + mat_ * 8192, vw0, rs_w, kx[q] * 196608, mat_ * 1024); \
                                                                            DMA(wk0, L1_W + ((q) % 3) * L1_WSLOT + mat_ * 8192 + 4096, vw0, rs_w, kx[q] * 196608, mat_ * 1024 + 98304); } } while (0)
#define O2(lds, m) do { DMA(wk0, (lds), vwo0, rs_wo, 0u, (m) * 65536); DMA(wk0, (lds) + 4096, vwo0, rs_wo, 0u, (m) * 65536 + 32768); } while (0)
            const unsigned sxa = xa ^ ((lw & 1) * 64);
            const unsigned stc = sxa + (lw >> 1) * 8192, sth = sxa + L1_H + (lw >> 1) * 2048;
            f32x4 gr[5], rs[5];
#pragma unroll
            for (int t = 0; t < 5; ++t) { gr[t] = Z4; rs[t] = Z4; }
#pragma unroll
            for (int s = 0; s < 16; ++s) {
                if ((s & 1) && s < 15) {
                    if (s == 1) WAIT_VM(9); else if (s == 3) WAIT_VM(12); else if (s == 5) WAIT_VM(14 + 2 * NTOUCH); else if (s == 7) WAIT_VM(10 + 2 * NTOUCH); else if (s < 13) WAIT_VM(6); else WAIT_VM(10);
                    BAR();
                    const int it = (s + 1) / 2;
                    if (it <= 5) W6(it + 2 > 7 ? 7 : it + 2);
                    if (it == 1) { X2(3); X2(4); X2(5); }
                    if (it == 2) { X2(6); X2(7); }
                    if (it == 6) { O2(L2_WO + 6 * 8192, 6); O2(L2_WO + 7 * 8192, 7); O2(0, 3); O2(8192, 4); O2(16384, 5); }
                    if (it == 7) { O2(L2_WO, 0); O2(L2_WO + 8192, 1); O2(L2_WO + 2 * 8192, 2); }
                    if (s == 9) READ_GEN();
                }
                if (s == 0) { X2(2); W6(2); }
                if (s == 3) {
                    const char* t1 = lane < 22 ? (const char*)P.wg + lane * 65536 : (lane < 44 ? (const char*)P.wu + (lane - 22) * 65536 : (const char*)P.wd + (lane - 44) * 65536);
                    const char* t2 = lane < 8 ? (const char*)P.wo + lane * 65536 : (lane < 32 ? (const char*)P.out + p * 131072 + (lane - 8) * 4096 : (const char*)P.wd + (20 + (lane & 1)) * 65536);
                    const char* tw = lw == 0 ? (const char*)wsp + WS_FLAG + lane * 256 : (lw == 1 ? t1 : (lw == 2 ? t2 : (const char*)wsp + WS_SPARE + lane * 16));
                    TOUCH(tw);
                    const char* tz = (const char*)wsp + 32768u + lane * 16;
                    TOUCH(tz);
#pragma unroll
                    for (int i = 0; i < 6; ++i) {
                        unsigned qq = b * 24 + lw * 6 + i; if (qq >= 4640) qq -= 4640;
                        const char* base = qq < 512 ? (const char*)P.wo + qq * 1024 : (qq < 1888 ? (const char*)P.wg + (qq - 512) * 1024 : (qq < 3264 ? (const char*)P.wu + (qq - 1888) * 1024 : (const char*)P.wd + (qq - 3264) * 1024));
                        const char* t3 = base + lane * 16;
                        TOUCH(t3);
                    }
                }
                if (s == 5 || s == 6 || s == 7 || s == 9) {
                    const int j = s == 5 ? 0 : (s == 6 ? 1 : (s == 7 ? 2 : 3));
                    u32x4 sf[5];
                    sf[0] = lds128(sth + j * 4096);
#pragma unroll
                    for (int t = 0; t < 4; ++t) sf[1 + t] = lds128(stc + j * 16384 + t * 2048);
#pragma unroll
                    for (int t = 0; t < 5; ++t) { gr[t] = mfma32(sf[t], sf[t], gr[t]); rs[t] = mfma32(ONES, sf[t], rs[t]); }
                }
                if (s == 10 && g == q4) {
#pragma unroll
                    for (int t = 0; t < 5; ++t) *(LAS f32x2*)(part + ((t * 4 + lw) * 16 + r16) * 8) = (f32x2){rs[t][0], diag4(gr[t], lane)};
                }
                if (s == 11) {
                    const unsigned row = lw * 64 + lane < 80 ? lw * 64 + lane : 79;
                    const unsigned pa = part + ((row >> 4) * 64 + (row & 15)) * 8;
                    const f32x2 sm = (lds64f(pa) + lds64f(pa + 128)) + (lds64f(pa + 256) + lds64f(pa + 384));
                    *(LAS f32x2*)(fin + row * 8) = ln_stats(sm.x, sm.y);
                }
            }
        }
    }
    const rsrc_t rs_ws = mk_rsrc(wsp, WS_BYTES);
    const u64 epoch = gen + 1;
    const unsigned n3 = w & 3, m3 = w >> 2;
    const rsrc_t rs_g = mk_rsrc(P.wg, DM * FF * 2), rs_u = mk_rsrc(P.wu, DM * FF * 2);
    const rsrc_t rs_gu = mk_rsrc(m3 ? P.wu : P.wg, DM * FF * 2);
    unsigned vg0[3], vg1[3];
#pragma unroll
    for (int i = 0; i < 3; ++i) {
        const unsigned s = (n3 * 3 + i) * 64 + lane;
        const unsigned ks2 = s >= 384 ? 1 : 0, s2 = s - ks2 * 384, row = (s2 * 43691u) >> 19, pos = s2 - row * 12;
        const int umax = (int)(c < 15 ? 171 - 12 * c : 0);
        int u0 = (int)pos - (int)((row >> 3) & 1); if (u0 < 0) u0 += 12;
        int u1 = (int)pos - (int)(((row >> 3) & 1) + 2); if (u1 < 0) u1 += 12;
        const unsigned rb = (ks2 * 32 + row) * (FF * 2) + c * 192;
        vg0[i] = rb + min(u0, umax) * 16; vg1[i] = rb + min(u1, umax) * 16;
    }
    unsigned vgu[3], vgz[3];
#pragma unroll
    for (int i = 0; i < 3; ++i) {
        vgu[i] = m3 ? vg1[i] : vg0[i];
        const unsigned s = i * 64 + lane, row = (s * 43691u) >> 19, pos = s - row * 12;
        int u0 = (int)pos - (int)((row >> 3) & 1); if (u0 < 0) u0 += 12;
        vgz[i] = row * (FF * 2) + c * 192 + min(u0, (int)(c < 15 ? 171 - 12 * c : 0)) * 16;
    }
    unsigned mg0[3], mgu[3];
#pragma unroll
    for (int i = 0; i < 3; ++i) { const unsigned pi = n3 * 3 + i; mg0[i] = L3_B + (pi >= 6 ? L3_BSLOT + (pi - 6) * 1024 : pi * 1024); mgu[i] = mg0[i] + m3 * 6144; }
    const unsigned rot3 = (2 * (jj >> 3) + (jj & 7)) & 7;
#define ISSUE_B(pr) do { const unsigned sb_ = (((pr) + rot3) & 7) * (64 * FF * 2); _Pragma("unroll") for (int i_ = 0; i_ < 3; ++i_) DMA(mgu[i_], ((pr) & 3) * 2 * L3_BSLOT, vgu[i_], rs_gu, sb_, 0); } while (0)

    u32x4 h_keep;
    {
        WAIT_VM(0);
        BAR();
        if (tid == 0) __hip_atomic_store(flags + (FL_1 + c) * 32, epoch, __ATOMIC_RELAXED, __HIP_MEMORY_SCOPE_AGENT);
        const u32x4 xr = __builtin_amdgcn_raw_buffer_load_b128(rs_x, tok_off, 0, 0);
        const unsigned wl = (8 * g + q4) * 128 + (fsw * 32) + 8 * p4;
        const unsigned wb0 = wl + (kh ? 8192 : L2_WO), wb1 = wl + (kh ? L2_WO + 49152 : L2_WO + 16384), wb2 = wl + (kh ? L2_WO + 57344 : 0);
        unsigned abase = lane * 16;
        const unsigned asoff = WS_O + ((4 * hf + rt4) * 16 + 8 * kh) * 1024;
        PIN(abase);
        u32x4 bf[8][4];
#pragma unroll
        for (int k = 0; k < 8; ++k)
#pragma unroll
            for (int ct = 0; ct < 4; ++ct) bf[k][ct] = tr_frag(((k < 4 ? wb0 : (k < 6 ? wb1 : wb2)) ^ (ct * 32)) + (k < 4 ? k : (k < 6 ? k - 4 : k - 6)) * 4096, 512);
        __builtin_amdgcn_sched_barrier(0);
        if (w == 0) wait_flags(flags + (FL_1 + 8 * hf) * 32, 8, epoch, lane);
        BAR();
        u32x4 af[8];
#pragma unroll
        for (int k = 0; k < 4; ++k) af[k] = __builtin_amdgcn_raw_buffer_load_b128(rs_ws, abase + (k & 3) * 1024, asoff + (k >> 2) * 4096, AUX_SC1);
        __builtin_amdgcn_sched_barrier(0);
        f32x4 acc[4] = {Z4, Z4, Z4, Z4};
#pragma unroll
        for (int k = 0; k < 8; ++k) {
            if (k + 4 < 8) af[k + 4] = __builtin_amdgcn_raw_buffer_load_b128(rs_ws, abase + (k & 3) * 1024, asoff + 4096, AUX_SC1);
            __builtin_amdgcn_sched_barrier(0);
#pragma unroll
            for (int ct = 0; ct < 4; ++ct) acc[ct] = mfma32(bf[k][ct], af[k], acc[ct]);
            __builtin_amdgcn_sched_barrier(0);
        }
        const unsigned red = L2_RED + w * 2048 + lane * 16;
        *(LAS f32x4*)red = kh ? acc[0] : acc[2];
        *(LAS f32x4*)(red + 1024) = kh ? acc[1] : acc[3];
        BAR();
        const f32x4 a0 = (kh ? acc[2] : acc[0]) + lds128f(red ^ 8192);
        const f32x4 a1 = (kh ? acc[3] : acc[1]) + lds128f((red ^ 8192) + 1024);
        auto s0 = __builtin_amdgcn_permlane16_swap(pk_bf16(a0[0], a0[1]), pk_bf16(a1[0], a1[1]), false, false);
        auto s1 = __builtin_amdgcn_permlane16_swap(pk_bf16(a0[2], a0[3]), pk_bf16(a1[2], a1[3]), false, false);
        const u32x4 at = {s0[0], s1[0], s0[1], s1[1]};
#pragma unroll
        for (int i = 0; i < 4; ++i) h_keep[i] = pk_bf16(bf_lo(at[i]) + bf_lo(xr[i]), bf_hi(at[i]) + bf_hi(xr[i]));
        const unsigned hoff = ((2 * (g & 1) + (g >> 1)) * 16 + r16) * 16;
        const unsigned hsoff = WS_H + ((4 * hf + rt4) * 16 + 2 * hg + kh) * 1024;
        __builtin_amdgcn_raw_buffer_store_b128(h_keep, rs_ws, hoff + hsoff, 0, AUX_SC1);
        const f32x4 gs = mfma32(h_keep, h_keep, Z4), sm = mfma32(ONES, h_keep, Z4);
        const u32x2 stv = {f2u(sm[0]), f2u(diag4(gs, lane))};
        const unsigned stoff = (g == q4) ? WS_ST + c * 1024 + w * 128 + r16 * 8 : 0xFFFFFFF0u;
        __builtin_amdgcn_raw_buffer_store_b64(stv, rs_ws, stoff, 0, AUX_SC1);
        asm volatile("" ::: "memory");
    }
    const rsrc_t rs_wd = mk_rsrc(P.wd, FF * DM * 2);
    const unsigned vwd = l8 * 1024 + wsw + hg * 128;
#define KS4(s) (11 * ((s) / 10) + (s) % 10 + 1 - ((s) >= 30 ? 1 : 0))
    const unsigned swd = ((w >> 2) * 32 + (w & 3) * 8) * 1024;
#define ISSUE_D2(s0) DMA(wk, (s0) * 4096, vwd, rs_wd, swd, KS4(s0) * 32768)
    const unsigned swd0 = (w & 3) * 8 * 1024;
#define ISSUE_S() do { DMA(wk, 30 * 4096, vwd, rs_wd, swd0 + (kh ? 11 : 0) * 32768, 0); DMA(wk, 32 * 4096, vwd, rs_wd, swd0 + (kh ? 36 : 22) * 32768, 0); } while (0)
    {
        const bool active = c < 15;
        WAIT_VM(0);
        BAR();
        if (tid == 0) __hip_atomic_store(flags + (FL_2 + c) * 32, epoch, __ATOMIC_RELAXED, __HIP_MEMORY_SCOPE_AGENT);
        if (active && w != 0) {
            ISSUE_B(0); ISSUE_B(1);
            if (w == 1 || w == 2) {
                const unsigned sb_ = ((w - 1 + rot3) & 7) * (64 * FF * 2), mz = L3_B + (w - 1) * 2 * L3_BSLOT;
                DMA(mz, 0, vgz[0], rs_g, sb_, 0); DMA(mz, 1024, vgz[1], rs_g, sb_, 0); DMA(mz, 2048, vgz[2], rs_g, sb_, 0);
            }
        }
        unsigned aa = L3_A + lane * 16;
        unsigned ba[3];
#pragma unroll
        for (int i = 0; i < 3; ++i) {
            const unsigned mat = p4 >> 1;
            unsigned pos = 3 * n3 + i + (g & 1) + 2 * mat; if (pos >= 12) pos -= 12;
            ba[i] = L3_B + mat * 6144 + ((8 * g + q4) * 12 + pos) * 16 + 8 * (p4 & 1);
            PIN(ba[i]);
        }
        unsigned vl16 = lane * 16;
        const unsigned sa3 = WS_H + w * 16384;
        PIN(aa); PIN(vl16);
        if (w == 0) wait_flags(flags + FL_2 * 32, 16, epoch, lane);
        BAR();
        if (tid == 0 && c == 0) __hip_atomic_store(flags + FL_GEN * 32, epoch, __ATOMIC_RELAXED, __HIP_MEMORY_SCOPE_AGENT);
        if (active) {
#define ISSUE_A(pr) do { const unsigned sa_ = sa3 + (((pr) + rot3) & 7) * 2048; DMA_SC1(wk, L3_A + ((pr) % 3) * 16384, vl16, rs_ws, sa_, 0); DMA_SC1(wk, L3_A + ((pr) % 3) * 16384 + 8192, vl16, rs_ws, sa_, 1024); } while (0)
            ISSUE_A(0); ISSUE_A(1);
            WAIT_VM(2);
            BAR();
            if (w < 4) {
                f32x4 acc[8][3], cs[3] = {Z4, Z4, Z4};
#pragma unroll
                for (int t = 0; t < 8; ++t)
#pragma unroll
                    for (int i = 0; i < 3; ++i) acc[t][i] = Z4;
                u32x4 af[2][8], bf[2][3];
#define LOADM(s) do { const int pr_ = (s) >> 1, k2_ = (s) & 1; \
                    bf[k2_][0] = tr_frag(ba[0] + ((pr_ & 3) * 2 + k2_) * L3_BSLOT, 768); \
                    _Pragma("unroll") for (int t = 0; t < 8; ++t) af[k2_][t] = lds128(aa + (pr_ % 3) * 16384 + k2_ * 8192 + t * 1024); \
                    bf[k2_][1] = tr_frag(ba[1] + ((pr_ & 3) * 2 + k2_) * L3_BSLOT, 768); bf[k2_][2] = tr_frag(ba[2] + ((pr_ & 3) * 2 + k2_) * L3_BSLOT, 768); } while (0)
                LOADM(0);
#pragma unroll
                for (int s = 0; s < 16; ++s) {
                    if (s & 1) { if (s == 1) WAIT_VM(0); BAR(); }
                    if (s < 15) LOADM(s + 1);
#pragma unroll
                    for (int i = 0; i < 3; ++i) {
#pragma unroll
                        for (int t = 0; t < 8; ++t) acc[t][i] = mfma32(bf[s & 1][i], af[s & 1][t], acc[t][i]);
                        cs[i] = mfma32(bf[s & 1][i], ONES, cs[i]);
                    }
#pragma unroll
                    for (int i = 0; i < 14; ++i) { __builtin_amdgcn_sched_group_barrier(0x8, 1, 0); __builtin_amdgcn_sched_group_barrier(0x100, 1, 0); }
                    __builtin_amdgcn_sched_group_barrier(0x8, 13, 0);
                    __builtin_amdgcn_sched_barrier(0);
                }
                const unsigned hi = lane >> 5;
                const unsigned tau = 2 * (g & 1) + (g >> 1);
#pragma unroll
                for (int m = 0; m < 2; ++m) {
                    f32x2 st[4];
#pragma unroll
                    for (int t = 0; t < 4; ++t) st[t] = lds64f(L3_ST + 512 + m * 8192 + t * 128 + r16 * 8);
                    const float r01 = hi ? st[1].y : st[0].y, r23 = hi ? st[3].y : st[2].y;
                    const float rr01 = r01 * r01, rr23 = r23 * r23;
#pragma unroll
                    for (int i = 0; i < 3; ++i) {
                        const f32x4 cv = cs[i];
                        u32x2 hp[2];
#pragma unroll
                        for (int pp = 0; pp < 2; ++pp) {
                            const f32x4 e0 = acc[4 * m + 2 * pp][i] - st[2 * pp].x * cv, e1 = acc[4 * m + 2 * pp + 1][i] - st[2 * pp + 1].x * cv;
                            float hv[4];
#pragma unroll
                            for (int r = 0; r < 4; ++r) {
                                auto sw = __builtin_amdgcn_permlane32_swap(f2u(e0[r]), f2u(e1[r]), false, false);
                                const unsigned x0 = sw[0], x1 = sw[1];
                                hv[r] = __builtin_fmaxf(u2f(x0), 0.0f) * (u2f(x1) * (pp ? rr23 : rr01));
                            }
                            hp[pp] = (u32x2){pk_bf16(hv[0], hv[1]), pk_bf16(hv[2], hv[3])};
                        }
                        auto s0 = __builtin_amdgcn_permlane16_swap(hp[0].x, hp[1].x, false, false);
                        auto s1 = __builtin_amdgcn_permlane16_swap(hp[0].y, hp[1].y, false, false);
                        const u32x4 hf4 = {s0[0], s1[0], s0[1], s1[1]};
                        const unsigned U = 12 * c + 3 * n3 + i;
                        const unsigned soff = WS_HID + ((4 * m) * NKS4 + (U >> 2)) * 1024 + (U & 3) * 256;
                        const unsigned off = U < 172 ? tau * (NKS4 * 1024) + r16 * 16 + soff : 0xFFFFFFF0u;
                        __builtin_amdgcn_raw_buffer_store_b128(hf4, rs_ws, off, 0, AUX_SC1);
                    }
                }
                asm volatile("" ::: "memory");
            } else {
                const unsigned lw = w - 4, wk0 = lw * 1024, sa30 = WS_H + lw * 16384;
#define A4(pr) do { const unsigned sa_ = sa30 + (((pr) + rot3) & 7) * 2048; const int o_ = L3_A + ((pr) % 3) * 16384; \
                    DMA_SC1(wk0, o_, vl16, rs_ws, sa_, 0); DMA_SC1(wk0, o_ + 8192, vl16, rs_ws, sa_, 1024); DMA_SC1(wk0, o_ + 4096, vl16, rs_ws, sa_, 65536); DMA_SC1(wk0, o_ + 12288, vl16, rs_ws, sa_, 65536 + 1024); } while (0)
#define B6(pr) do { const unsigned sb_ = (((pr) + rot3) & 7) * (64 * FF * 2); const int o_ = ((pr) & 3) * 2 * L3_BSLOT; \
                    _Pragma("unroll") for (int i_ = 0; i_ < 3; ++i_) DMA(mg0[i_], o_, vg0[i_], rs_g, sb_, 0); \
                    _Pragma("unroll") for (int i_ = 0; i_ < 3; ++i_) DMA(mg0[i_], o_ + 6144, vg1[i_], rs_u, sb_, 0); } while (0)
#define D4(s0) do { DMA(wk0, (s0) * 4096, vwd, rs_wd, swd0, KS4(s0) * 32768); DMA(wk0, (s0) * 4096 + 4096, vwd, rs_wd, swd0, KS4(s0) * 32768 + 32768); } while (0)
                const unsigned sa = L3_ST + (lane & 3) * 2048 + (lw * 16 + (lane >> 2)) * 8;
#pragma unroll
                for (int s = 0; s < 16; ++s) {
                    if (s & 1) {
                        if (s == 1 || s == 3) WAIT_VM(20); else if (s < 13) WAIT_VM(16); else if (s == 13) WAIT_VM(12);
                        BAR();
                        const int po = s >> 1;
                        if (po + 3 < 8) A4(po + 3 < 8 ? po + 3 : 7);
                        if (po + 4 < 8) B6(po + 4 < 8 ? po + 4 : 7);
                        if (po >= 4 && po < 7) { D4(12 + 6 * (po >= 4 ? po - 4 : 0)); D4(14 + 6 * (po >= 4 ? po - 4 : 0)); D4(16 + 6 * (po >= 4 ? po - 4 : 0)); }
                        if (s == 15) {
                            DMA(wk0, 30 * 4096, vwd, rs_wd, swd0, 0); DMA(wk0, 31 * 4096, vwd, rs_wd, swd0, 11 * 32768);
                            DMA(wk0, 32 * 4096, vwd, rs_wd, swd0, 22 * 32768); DMA(wk0, 33 * 4096, vwd, rs_wd, swd0, 36 * 32768);
                            D4(0); D4(2); D4(4);
                        }
                    }
                    if (s == 0) {
                        B6(2); A4(2); B6(3);
                        const unsigned sst = WS_ST + lw * 2048;
                        DMA_SC1(wk0 + wk0, L3_ST, vl16, rs_ws, sst, 0); DMA_SC1(wk0 + wk0, L3_ST + 1024, vl16, rs_ws, sst, 1024);
                        DMA_SC1(wk0 + wk0, L3_ST + 8192, vl16, rs_ws, sst, 8192); DMA_SC1(wk0 + wk0, L3_ST + 8192 + 1024, vl16, rs_ws, sst, 8192 + 1024);
                    }
                    if (s == 5) {
#pragma unroll
                        for (int hfz = 0; hfz < 2; ++hfz) {
                            const unsigned a = sa + hfz * 8192;
                            const f32x2 sm = (lds64f(a) + lds64f(a + 512)) + (lds64f(a + 1024) + lds64f(a + 1536));
                            float sx = sm.x, sq = sm.y;
                            sx += __builtin_amdgcn_update_dpp(0.f, sx, 0xB1, 0xf, 0xf, false); sq += __builtin_amdgcn_update_dpp(0.f, sq, 0xB1, 0xf, 0xf, false);
                            sx += __builtin_amdgcn_update_dpp(0.f, sx, 0x4E, 0xf, 0xf, false); sq += __builtin_amdgcn_update_dpp(0.f, sq, 0x4E, 0xf, 0xf, false);
                            *(LAS f32x2*)((a & ~(unsigned)6144) + 512) = ln_stats(sx, sq);
                        }
                    }
                }
            }
        }
    }
    {
        const bool active = c < 15;
        if (!active) {
            ISSUE_D2(12); ISSUE_D2(14); ISSUE_D2(16); ISSUE_D2(18); ISSUE_D2(20); ISSUE_D2(22); ISSUE_D2(24); ISSUE_D2(26); ISSUE_D2(28);
            ISSUE_S();
            ISSUE_D2(0); ISSUE_D2(2); ISSUE_D2(4);
        }
        WAIT_VM(0);
        BAR();
        if (tid == 0 && active) __hip_atomic_store(flags + (FL_3 + c) * 32, epoch, __ATOMIC_RELAXED, __HIP_MEMORY_SCOPE_AGENT);
#define REST_D() do { ISSUE_D2(6); ISSUE_D2(8); ISSUE_D2(10); ISSUE_D2(34); ISSUE_D2(36); ISSUE_D2(38); } while (0)
        if (w != 0) REST_D();
        const unsigned wb0 = (8 * g + q4) * 128 + (fsw * 32) + 8 * p4;
        u32x4 bq[11][4];
#pragma unroll
        for (int ct = 0; ct < 4; ++ct) {
            const u32x4 t0 = tr_frag((wb0 ^ (ct * 32)) + (30 + (kq4 < 3 ? kq4 : 0)) * 4096, 512);
            bq[0][ct] = kq4 < 3 ? t0 : (u32x4){0u, 0u, 0u, 0u};
        }
        BAR();
#define LAST_D() do { ISSUE_D2(30); DMA(wk, 32 * 4096, vwd, rs_wd, swd, 35 * 32768); } while (0)
        if (w != 0) LAST_D();
        unsigned wbk = wb0 + kq4 * (10 * 4096);
        unsigned abase = lane * 16;
        const unsigned asoff = WS_HID + ((4 * hf + 2 * rp4) * NKS4 + 11 * kq4 - (kq4 == 3 ? 1 : 0)) * 1024;
        PIN(wbk); PIN(abase);
        if (w == 0) wait_flags(flags + FL_3 * 32, 15, epoch, lane);
        BAR();
        u32x4 af[2][11];
#define LOADA4(k) do { af[0][k] = __builtin_amdgcn_raw_buffer_load_b128(rs_ws, abase + ((k) & 3) * 1024, asoff + ((k) >> 2) * 4096, AUX_SC1); \
                       af[1][k] = __builtin_amdgcn_raw_buffer_load_b128(rs_ws, abase + ((k) & 3) * 1024, asoff + NKS4 * 1024 + ((k) >> 2) * 4096, AUX_SC1); } while (0)
        if (w == 0) { REST_D(); LAST_D(); }
        LOADA4(0); LOADA4(1); LOADA4(2);
        WAIT_VM(6);
        BAR();
#define LOAD4(j) do { _Pragma("unroll") for (int ct = 0; ct < 4; ++ct) bq[j][ct] = tr_frag((wbk ^ (ct * 32)) + ((j) - 1) * 4096, 512); } while (0)
        LOAD4(1); LOAD4(2);
        __builtin_amdgcn_sched_barrier(0);
        f32x4 acc[2][4] = {{Z4, Z4, Z4, Z4}, {Z4, Z4, Z4, Z4}};
#pragma unroll
        for (int j = 0; j < 11; ++j) {
            if (j + 3 < 11) { LOADA4(j + 3); LOAD4(j + 3); }
#pragma unroll
            for (int rl = 0; rl < 2; ++rl)
#pragma unroll
                for (int ct = 0; ct < 4; ++ct) acc[rl][ct] = mfma32(bq[j][ct], af[rl][j], acc[rl][ct]);
#pragma unroll
            for (int i = 0; i < 8; ++i) { __builtin_amdgcn_sched_group_barrier(0x8, 1, 0); __builtin_amdgcn_sched_group_barrier(0x100, 1, 0); }
            __builtin_amdgcn_sched_barrier(0);
        }
        BAR();
        const unsigned red = w * 8192 + lane * 16;
#pragma unroll
        for (int rl = 0; rl < 2; ++rl)
#pragma unroll
            for (int ct = 0; ct < 4; ++ct) *(LAS f32x4*)(red + (rl * 4 + ct) * 1024) = acc[rl][ct];
        BAR();
        f32x4 a0 = Z4, a1 = Z4;
#pragma unroll
        for (int o = 0; o < 4; ++o) {
            const unsigned ra = (2 * rp4 + (o & 1) + 4 * (o >> 1)) * 8192 + lane * 16 + ((rt4 & 1) * 4 + 2 * kh) * 1024;
            a0 += lds128f(ra); a1 += lds128f(ra + 1024);
        }
        auto s0 = __builtin_amdgcn_permlane16_swap(pk_bf16(a0[0], a0[1]), pk_bf16(a1[0], a1[1]), false, false);
        auto s1 = __builtin_amdgcn_permlane16_swap(pk_bf16(a0[2], a0[3]), pk_bf16(a1[2], a1[3]), false, false);
        const u32x4 at = {s0[0], s1[0], s0[1], s1[1]};
        u32x4 o;
#pragma unroll
        for (int i = 0; i < 4; ++i) o[i] = pk_bf16(bf_lo(at[i]) + bf_lo(h_keep[i]), bf_hi(at[i]) + bf_hi(h_keep[i]));
        *(u32x4*)((char*)P.out + tok_off) = o;
    }
}

}

extern "C" void kernel_launch(void* const* d_in, const int* in_sizes, int n_in, void* d_out, int out_size, void* d_ws, size_t ws_size, hipStream_t stream) {
    Params P;
    P.x = (const u16*)d_in[0]; P.wqkv = (const u16*)d_in[1]; P.wo = (const u16*)d_in[2];
    P.wg = (const u16*)d_in[4]; P.wu = (const u16*)d_in[5]; P.wd = (const u16*)d_in[6];
    P.out = (u16*)d_out; P.ws = (unsigned char*)d_ws;
    (void)hipFuncSetAttribute((const void*)fused_block, hipFuncAttributeMaxDynamicSharedMemorySize, LDS_BYTES);
    hipLaunchKernelGGL(fused_block, dim3(256), dim3(512), LDS_BYTES, stream, P);
}
```

```cpp
#include <hip/hip_runtime.h>
#include <hip/hip_bf16.h>
#include <stdint.h>
#pragma clang diagnostic ignored "-Wint-to-pointer-cast"

#define LAS __attribute__((address_space(3)))
#define GAS __attribute__((address_space(1)))
typedef unsigned short u16;
typedef unsigned long long u64;
typedef short s16x4 __attribute__((ext_vector_type(4)));
typedef __bf16 bf16x8 __attribute__((ext_vector_type(8)));
typedef __bf16 bf16x2 __attribute__((ext_vector_type(2)));
typedef float f32x4 __attribute__((ext_vector_type(4)));
typedef float f32x2 __attribute__((ext_vector_type(2)));
typedef unsigned u32x4 __attribute__((ext_vector_type(4)));
typedef unsigned u32x2 __attribute__((ext_vector_type(2)));
typedef GAS u64 gu64;
typedef __amdgpu_buffer_rsrc_t rsrc_t;

namespace {

constexpr int SEQ = 2048, DM = 512, FF = 1376;
constexpr int NKS4 = FF / 32;
constexpr unsigned WS_FLAG = 0;
constexpr unsigned WS_ST = 16384;
constexpr unsigned WS_O = 40960;
constexpr unsigned WS_H = WS_O + 131072;
constexpr unsigned WS_HID = WS_H + 131072;
constexpr unsigned WS_SPARE = WS_HID + 8 * 43 * 1024;
constexpr unsigned WS_BYTES = WS_SPARE + 8192;
constexpr int FL_GEN = 0, FL_1 = 8, FL_2 = 24, FL_3 = 40;
constexpr int AUX_SC1 = 16;
constexpr unsigned kSpinCap = 1u << 16;
constexpr unsigned CODE_KB = 29;

constexpr int LDS_BYTES = 163840;
constexpr int L1_X = 0, L1_H = 65536, L1_W = 81920, L1_WSLOT = 24576, L1_SCR = 155648;
constexpr int L2_WO = 81920, L2_RED = 114688;
constexpr int L3_A = 0, L3_B = 49152, L3_BSLOT = 12288, L3_ST = 147456;

#define WAIT_VM(n) asm volatile("s_waitcnt vmcnt(%0)" :: "i"(n) : "memory")
#define BAR() do { asm volatile("s_waitcnt lgkmcnt(0)" ::: "memory"); __builtin_amdgcn_s_barrier(); asm volatile("" ::: "memory"); } while (0)
#define PIN(x) asm volatile("" : "+v"(x))
#define DMA(m0b, ldsoff, voff, rs, sbase, soffimm) do { unsigned t_; asm volatile("s_add_u32 m0, %1, %2\n\ts_add_u32 %0, %5, %6\n\tbuffer_load_dwordx4 %3, %4, %0 offen lds" \
    : "=&s"(t_) : "s"(m0b), "i"(ldsoff), "v"(voff), "s"(rs), "s"(sbase), "i"(soffimm) : "scc", "memory"); } while (0)
#define DMA_SC1(m0b, ldsoff, voff, rs, sbase, soffimm) do { unsigned t_; asm volatile("s_add_u32 m0, %1, %2\n\ts_add_u32 %0, %5, %6\n\tbuffer_load_dwordx4 %3, %4, %0 offen sc1 lds" \
    : "=&s"(t_) : "s"(m0b), "i"(ldsoff), "v"(voff), "s"(rs), "s"(sbase), "i"(soffimm) : "scc", "memory"); } while (0)

__device__ __forceinline__ rsrc_t mk_rsrc(const void* p, unsigned bytes) { return __builtin_amdgcn_make_buffer_rsrc((void*)p, 0, bytes, 0x00020000); }
__device__ __forceinline__ float u2f(unsigned u) { return __builtin_bit_cast(float, u); }
__device__ __forceinline__ unsigned f2u(float f) { return __builtin_bit_cast(unsigned, f); }
__device__ __forceinline__ unsigned pk_bf16(float a, float b) { bf16x2 v; v[0] = (__bf16)a; v[1] = (__bf16)b; return __builtin_bit_cast(unsigned, v); }
__device__ __forceinline__ float bf_lo(unsigned u) { return u2f(u << 16); }
__device__ __forceinline__ float bf_hi(unsigned u) { return u2f(u & 0xffff0000u); }
__device__ __forceinline__ float rbf(float f) { return (float)(__bf16)f; }
__device__ __forceinline__ f32x4 mfma32(u32x4 a, u32x4 b, f32x4 c) {
    return __builtin_amdgcn_mfma_f32_16x16x32_bf16(__builtin_bit_cast(bf16x8, a), __builtin_bit_cast(bf16x8, b), c, 0, 0, 0);
}
__device__ __forceinline__ f32x4 mfma16(u32x2 a, u32x2 b, f32x4 c) {
    return __builtin_amdgcn_mfma_f32_16x16x16bf16_1k(__builtin_bit_cast(s16x4, a), __builtin_bit_cast(s16x4, b), c, 0, 0, 0);
}
__device__ __forceinline__ u32x4 lds128(unsigned a) { return *(LAS u32x4*)a; }
__device__ __forceinline__ f32x4 lds128f(unsigned a) { return *(LAS f32x4*)a; }
__device__ __forceinline__ f32x2 lds64f(unsigned a) { return *(LAS f32x2*)a; }
__device__ __forceinline__ u32x2 tr_read(unsigned a) { return __builtin_bit_cast(u32x2, __builtin_amdgcn_ds_read_tr16_b64_v4i16((LAS s16x4*)a)); }
__device__ __forceinline__ u32x4 tr_frag(unsigned a, int step) { const u32x2 x = tr_read(a), y = tr_read(a + step); return (u32x4){x.x, x.y, y.x, y.y}; }
__device__ __forceinline__ float swap16_lo(float v) {
    const unsigned a = f2u(v); auto r = __builtin_amdgcn_permlane16_swap(a, a, false, false); const unsigned x = r[0]; return u2f(x);
}
__device__ __forceinline__ float max2(float a, float b) { return __builtin_amdgcn_fmed3f(a, b, __builtin_inff()); }
__device__ __forceinline__ float xmax16_32(float v) {
    unsigned a = f2u(v);
    auto r = __builtin_amdgcn_permlane16_swap(a, a, false, false);
    const unsigned r0 = r[0], r1 = r[1];
    v = max2(u2f(r0), u2f(r1));
    a = f2u(v);
    auto s = __builtin_amdgcn_permlane32_swap(a, a, false, false);
    const unsigned s0 = s[0], s1 = s[1];
    return max2(u2f(s0), u2f(s1));
}
__device__ __forceinline__ float bcast_row0(float v) { return __builtin_amdgcn_update_dpp(0.f, v, 0x150, 0xf, 0xf, false); }
__device__ __forceinline__ float diag4(f32x4 d, unsigned lane) { const float a = (lane & 1) ? d[1] : d[0], b = (lane & 1) ? d[3] : d[2]; return (lane & 2) ? b : a; }
__device__ __forceinline__ f32x2 ln_stats(float s, float q) {
    const float mean = s * (1.0f / DM);
    const float var = __builtin_fmaxf(q * (1.0f / DM) - mean * mean, 0.0f);
    const float sd = rbf(__builtin_amdgcn_sqrtf(rbf(rbf(var) + 1e-5f)));
    return (f32x2){rbf(mean), __builtin_amdgcn_rcpf(sd)};
}
__device__ __forceinline__ void wait_flags(gu64* base, unsigned n, u64 epoch, unsigned lane) {
    for (unsigned spins = 0; spins < kSpinCap; ++spins) {
        u64 v = epoch;
        if (lane < n) v = __hip_atomic_load(base + lane * 32, __ATOMIC_RELAXED, __HIP_MEMORY_SCOPE_AGENT);
        if (__all(v == epoch)) break;
    }
    __builtin_amdgcn_fence(__ATOMIC_ACQUIRE, "wavefront");
}

struct Params { const u16* x; const u16* wqkv; const u16* wo; const u16* wg; const u16* wu; const u16* wd; u16* out; unsigned char* ws; };

__global__ void __launch_bounds__(512, 2) __attribute__((amdgpu_waves_per_eu(2, 2))) fused_block(Params P) {
    const unsigned tid = threadIdx.x, lane = tid & 63;
    const unsigned w = __builtin_amdgcn_readfirstlane(tid >> 6);
    const unsigned b = blockIdx.x, xcd = b & 7, jj = b >> 3;
    const unsigned p = 4 * (xcd >> 1) + (jj >> 3);
    const unsigned hf = (jj >> 2) & 1, hg = 4 * (xcd & 1) + (jj & 3);
    const unsigned c = 8 * hf + hg;
    const unsigned r16 = lane & 15, g = lane >> 4, l8 = lane >> 3, l7 = lane & 7, q4 = r16 >> 2, p4 = lane & 3;
    {
        u64 pc; asm volatile("s_getpc_b64 %0" : "=s"(pc));
        const char* cp = (const char*)(pc & ~(u64)1023) + ((jj * 8 + w) % CODE_KB) * 1024 + lane * 16;
        asm volatile("s_mov_b32 m0, %0\n\ts_nop 0\n\tglobal_load_lds_dwordx4 %1, off" :: "s"(L1_SCR + 6144), "v"(cp) : "memory");
    }
    const unsigned wk = w * 1024;
    const rsrc_t rs_x = mk_rsrc(P.x, SEQ * DM * 2);
    const rsrc_t rs_w = mk_rsrc(P.wqkv, DM * 3 * DM * 2);
    unsigned char* const wsp = P.ws + (size_t)p * WS_BYTES;
    gu64* const flags = (gu64*)(wsp + WS_FLAG);
    const unsigned T0 = 128 * p + 64 * hf;
    u32x4 ONES = {0x3F803F80u, 0x3F803F80u, 0x3F803F80u, 0x3F803F80u};
    PIN(ONES);
    const f32x4 Z4 = {0.f, 0.f, 0.f, 0.f};
    const unsigned fsw = ((q4 >> 1) & 1) | ((g & 1) << 1);
    const unsigned wsw = (l7 ^ (((g & 1) | ((w & 1) << 1)) << 1)) * 16;
    u64 gen = 0;
    const unsigned rt4 = w & 3, kh = w >> 2, rp4 = rt4 >> 1, kq4 = (rt4 & 1) + 2 * kh;
    const unsigned tok_off = (128 * p + 64 * hf + 32 * (rt4 & 1) + (rt4 >> 1) + 2 * r16) * 1024 + (64 * hg + 32 * kh + 16 * (g & 1) + 8 * (g >> 1)) * 2;

    {
        const unsigned rot = (2 * (jj & 3) + (jj >> 2) + (xcd >> 1)) & 7;
        const unsigned hh = w & 3;
        const unsigned vh = l8 * 2048 + ((l7 ^ g) * 16);
        const unsigned vxr = vh ^ ((w & 1) * 64);
        const unsigned sxr = (T0 + 32 * ((w >> 1) & 1) + (w >> 2) + 16 * (w & 1)) * 1024;
        const unsigned vw = (8 * w + l8) * 3072 + wsw + hg * 128;
        {
            const unsigned kap = (w + rot) & 7;
            const unsigned sh = (T0 == 0 ? 0 : T0 - 16) * 1024 + kap * 128;
            const unsigned hb = L1_H + w * 2048;
            if (lane >= 8) { DMA(hb, 0, vh, rs_x, sh, 0); const unsigned vh1 = vh ^ 64; DMA(hb, 1024, vh1, rs_x, sh, 1024); }
            else { *(LAS u32x4*)(hb + lane * 16) = ONES; *(LAS u32x4*)(hb + 1024 + lane * 16) = ONES; }
        }
        unsigned kx[8];
#pragma unroll
        for (int q = 0; q < 8; ++q) kx[q] = (q + rot) & 7;
#define TOUCH(ptr) asm volatile("s_mov_b32 m0, %0\n\ts_nop 0\n\tglobal_load_lds_dwordx4 %1, off" :: "s"(L1_SCR + 6144), "v"(ptr) : "memory")
#define ISSUE_X(q) DMA(wk, L1_X + (q) * 8192, vxr, rs_x, sxr + kx[q] * 128, 0)
#define ISSUE_W1(q, mat) DMA(wk, L1_W + ((q) % 3) * L1_WSLOT + (mat) * 8192, vw, rs_w, kx[q] * 196608, (mat) * 1024)
#define ISSUE_W(q) do { ISSUE_W1(q, 0); ISSUE_W1(q, 1); ISSUE_W1(q, 2); } while (0)
        ISSUE_X(0); ISSUE_W(0);
        __builtin_amdgcn_s_barrier();
        ISSUE_X(1); ISSUE_W(1);
        { const char* gp = (const char*)wsp + WS_FLAG + lane * 16; asm volatile("s_mov_b32 m0, %0\n\ts_nop 0\n\tglobal_load_lds_dwordx4 %1, off sc1" :: "s"(L1_SCR + 7168), "v"(gp) : "memory"); }
#define READ_GEN() do { const u32x2 gv_ = *(LAS u32x2*)(L1_SCR + 7168 + FL_GEN * 256); gen = ((u64)(unsigned)__builtin_amdgcn_readfirstlane(gv_.y) << 32) | (unsigned)__builtin_amdgcn_readfirstlane(gv_.x); } while (0)
#define NTOUCH 4
        const unsigned part = L1_SCR, fin = L1_SCR + 2560;
        const unsigned xa = (r16 >> 3) * 1024 + (r16 & 7) * 128 + ((g ^ ((r16 >> 1) & 7)) * 16);
        const rsrc_t rs_wo = mk_rsrc(P.wo, DM * DM * 2);
        WAIT_VM(5);
        BAR();
        if (w < 4) {
            unsigned xc0 = xa, xc1 = xa ^ 64, xh0 = xa + L1_H, xh1 = xh0 ^ 64;
            unsigned wa = L1_W + (8 * g + q4) * 128 + ((hh ^ fsw) * 32) + 8 * p4, wa2 = wa + 2 * L1_WSLOT;
            PIN(xc0); PIN(xc1); PIN(xh0); PIN(xh1); PIN(wa); PIN(wa2);
            f32x4 aq[5], ak[5], av[5];
#pragma unroll
            for (int t = 0; t < 5; ++t) { aq[t] = Z4; ak[t] = Z4; av[t] = Z4; }
            u32x4 xf[2][5], wf[2][3];
#define LOADM1(s) do { const int kq_ = (s) >> 1, kk_ = (s) & 1; const unsigned wsl_ = (kq_ % 3 == 2 ? wa2 : wa + (kq_ % 3) * L1_WSLOT) + kk_ * 4096; \
                wf[kk_][0] = tr_frag(wsl_, 512); xf[kk_][0] = lds128((kk_ ? xh1 : xh0) + kq_ * 2048); \
                _Pragma("unroll") for (int t = 0; t < 4; ++t) xf[kk_][1 + t] = lds128((kk_ ? xc1 : xc0) + kq_ * 8192 + t * 2048); \
                wf[kk_][1] = tr_frag(wsl_ + 8192, 512); wf[kk_][2] = tr_frag(wsl_ + 16384, 512); } while (0)
            LOADM1(0);
#pragma unroll
            for (int s = 0; s < 16; ++s) {
                if ((s & 1) && s < 15) { if (s == 1) WAIT_VM(1); if (s == 9) WAIT_VM(0); BAR(); }
                if (s == 9) READ_GEN();
                if (s < 15) LOADM1(s + 1);
#pragma unroll
                for (int t = 0; t < 5; ++t) aq[t] = mfma32(wf[s & 1][0], xf[s & 1][t], aq[t]);
#pragma unroll
                for (int t = 0; t < 5; ++t) ak[t] = mfma32(wf[s & 1][1], xf[s & 1][t], ak[t]);
#pragma unroll
                for (int t = 0; t < 5; ++t) av[t] = mfma32(xf[s & 1][t], wf[s & 1][2], av[t]);
#pragma unroll
                for (int i = 0; i < 11; ++i) { __builtin_amdgcn_sched_group_barrier(0x8, 1, 0); __builtin_amdgcn_sched_group_barrier(0x100, 1, 0); }
                __builtin_amdgcn_sched_group_barrier(0x8, 4, 0);
                __builtin_amdgcn_sched_barrier(0);
            }
            f32x4 csq, csk;
#pragma unroll
            for (int r = 0; r < 4; ++r) { csq[r] = bcast_row0(aq[0][r]); csk[r] = bcast_row0(ak[0][r]); }
            const float csv = swap16_lo(av[0][0]);
            u32x2 qb[5], kb[5], vb[5];
#pragma unroll
            for (int t = 0; t < 5; ++t) {
                const f32x2 sl = lds64f(fin + (t * 16 + r16) * 8);
                const f32x4 s01 = lds128f(fin + (t * 16 + 4 * g) * 8), s23 = lds128f(fin + (t * 16 + 4 * g) * 8 + 16);
                const f32x4 nm = {-sl.x, -sl.x, -sl.x, -sl.x};
                const f32x4 kv = __builtin_elementwise_fma(csk, nm, ak[t]) * sl.y;
                kb[t] = (u32x2){pk_bf16(kv[0], kv[1]), pk_bf16(kv[2], kv[3])};
                if (t > 0) { const f32x4 qv = __builtin_elementwise_fma(csq, nm, aq[t]) * sl.y; qb[t] = (u32x2){pk_bf16(qv[0], qv[1]), pk_bf16(qv[2], qv[3])}; }
                const f32x4 mn = {s01[0], s01[2], s23[0], s23[2]}, rd = {s01[1], s01[3], s23[1], s23[3]};
                const f32x4 vv = __builtin_elementwise_fma(mn, (f32x4){-csv, -csv, -csv, -csv}, av[t]) * rd;
                vb[t] = (u32x2){pk_bf16(vv[0], vv[1]), pk_bf16(vv[2], vv[3])};
            }
            f32x4 mh[2], mp, mc;
#pragma unroll
            for (int r = 0; r < 4; ++r) {
                const unsigned kap = 4 * g + r;
                mh[0][r] = (T0 > 0 && kap < 8 && kap >= r16 + 1) ? 0.f : -1e30f;
                mh[1][r] = (T0 > 0 && kap >= 8 && (kap & 7) >= r16 + 1) ? 0.f : -1e30f;
                mp[r] = (kap >= r16 + 9) ? 0.f : -1e30f;
                mc[r] = (kap <= r16 && r16 - kap <= 7) ? 0.f : -1e30f;
            }
            const float SC = 0.25f * 1.44269504088896f;
            const rsrc_t rs_ws1 = mk_rsrc(wsp, WS_BYTES);
#pragma unroll
            for (int pz = 0; pz < 2; ++pz) {
                u32x2 ob[2];
#pragma unroll
                for (int a = 0; a < 2; ++a) {
                    const int cur = 1 + 2 * pz + a, prev = a == 0 ? 0 : cur - 1;
                    f32x4 sp = mfma16(kb[prev], qb[cur], a == 0 ? mh[pz] : mp), sc = mfma16(kb[cur], qb[cur], mc);
                    float mx = max2(max2(max2(sp[0], sp[1]), max2(sp[2], sp[3])), max2(max2(sc[0], sc[1]), max2(sc[2], sc[3])));
                    mx = xmax16_32(mx);
                    const float mb = -mx * SC;
#pragma unroll
                    for (int r = 0; r < 4; ++r) { sp[r] = __builtin_amdgcn_exp2f(sp[r] * SC + mb); sc[r] = __builtin_amdgcn_exp2f(sc[r] * SC + mb); }
                    const u32x4 pf = {pk_bf16(sp[0], sp[1]), pk_bf16(sp[2], sp[3]), pk_bf16(sc[0], sc[1]), pk_bf16(sc[2], sc[3])};
                    const u32x4 vf = {vb[prev].x, vb[prev].y, vb[cur].x, vb[cur].y};
                    const f32x4 sm = mfma32(ONES, pf, Z4);
                    const f32x4 o = mfma32(vf, pf, Z4) * __builtin_amdgcn_rcpf(sm[0]);
                    ob[a] = (u32x2){pk_bf16(o[0], o[1]), pk_bf16(o[2], o[3])};
                }
                auto s0 = __builtin_amdgcn_permlane16_swap(ob[0].x, ob[1].x, false, false);
                auto s1 = __builtin_amdgcn_permlane16_swap(ob[0].y, ob[1].y, false, false);
                const u32x4 o_frag = {s0[0], s1[0], s0[1], s1[1]};
                const unsigned off = ((g & 1) * 16384) + ((2 * (hh & 1) + (g >> 1)) * 16 + r16) * 16;
                const unsigned soff = WS_O + ((4 * hf + 2 * pz) * 16 + 2 * hg + (hh >> 1)) * 1024;
                __builtin_amdgcn_raw_buffer_store_b128(o_frag, rs_ws1, off + soff, 0, AUX_SC1);
            }
            asm volatile("" ::: "memory");
        } else {
            const unsigned lw = w - 4, wk0 = lw * 1024;
            const unsigned sx0 = (T0 + 32 * ((lw >> 1) & 1) + 16 * (lw & 1)) * 1024;
            const unsigned vw0 = (8 * lw + l8) * 3072 + wsw + hg * 128, vwo0 = (8 * lw + l8) * 1024 + wsw + hg * 128;
#define X2(q) do { DMA(wk0, L1_X + (q) * 8192, vxr, rs_x, sx0 + kx[q] * 128, 0); DMA(wk0, L1_X + (q) * 8192 + 4096, vxr, rs_x, sx0 + kx[q] * 128, 1024); } while (0)
#define W6(q) do { _Pragma("unroll") for (int mat_ = 0; mat_ < 3; ++mat_) { DMA(wk0, L1_W + ((q) % 3) * L1_WSLOT + mat_ * 8192, vw0, rs_w, kx[q] * 196608, mat_ * 1024); \
                                                                            DMA(wk0, L1_W + ((q) % 3) * L1_WSLOT + mat_ * 8192 + 4096, vw0, rs_w, kx[q] * 196608, mat_ * 1024 + 98304); } } while (0)
#define O2(lds, m) do { DMA(wk0, (lds), vwo0, rs_wo, 0u, (m) * 65536); DMA(wk0, (lds) + 4096, vwo0, rs_wo, 0u, (m) * 65536 + 32768); } while (0)
            const unsigned sxa = xa ^ ((lw & 1) * 64);
            const unsigned stc = sxa + (lw >> 1) * 8192, sth = sxa + L1_H + (lw >> 1) * 2048;
            f32x4 gr[5], rs[5];
#pragma unroll
            for (int t = 0; t < 5; ++t) { gr[t] = Z4; rs[t] = Z4; }
#pragma unroll
            for (int s = 0; s < 16; ++s) {
                if ((s & 1) && s < 15) {
                    if (s == 1) WAIT_VM(9); else if (s == 3) WAIT_VM(12); else if (s == 5) WAIT_VM(14 + 2 * NTOUCH); else if (s == 7) WAIT_VM(10 + 2 * NTOUCH); else if (s < 13) WAIT_VM(6); else WAIT_VM(10);
                    BAR();
                    const int it = (s + 1) / 2;
                    if (it <= 5) W6(it + 2 > 7 ? 7 : it + 2);
                    if (it == 1) { X2(3); X2(4); X2(5); }
                    if (it == 2) { X2(6); X2(7); }
                    if (it == 6) { O2(L2_WO + 6 * 8192, 6); O2(L2_WO + 7 * 8192, 7); O2(0, 3); O2(8192, 4); O2(16384, 5); }
                    if (it == 7) { O2(L2_WO, 0); O2(L2_WO + 8192, 1); O2(L2_WO + 2 * 8192, 2); }
                    if (s == 9) READ_GEN();
                }
                if (s == 0) { X2(2); W6(2); }
                if (s == 3) {
                    const char* t1 = lane < 22 ? (const char*)P.wg + lane * 65536 : (lane < 44 ? (const char*)P.wu + (lane - 22) * 65536 : (const char*)P.wd + (lane - 44) * 65536);
                    const char* t2 = lane < 8 ? (const char*)P.wo + lane * 65536 : (lane < 32 ? (const char*)P.out + p * 131072 + (lane - 8) * 4096 : (const char*)P.wd + (20 + (lane & 1)) * 65536);
                    const char* tw = lw == 0 ? (const char*)wsp + WS_FLAG + lane * 256 : (lw == 1 ? t1 : (lw == 2 ? t2 : (const char*)wsp + WS_SPARE + lane * 16));
                    TOUCH(tw);
                    const char* tz = (const char*)wsp + 32768u + lane * 16;
                    TOUCH(tz);
#pragma unroll
                    for (int i = 0; i < 6; ++i) {
                        unsigned qq = b * 24 + lw * 6 + i; if (qq >= 4640) qq -= 4640;
                        const char* base = qq < 512 ? (const char*)P.wo + qq * 1024 : (qq < 1888 ? (const char*)P.wg + (qq - 512) * 1024 : (qq < 3264 ? (const char*)P.wu + (qq - 1888) * 1024 : (const char*)P.wd + (qq - 3264) * 1024));
                        const char* t3 = base + lane * 16;
                        TOUCH(t3);
                    }
                }
                if (s == 5 || s == 6 || s == 7 || s == 9) {
                    const int j = s == 5 ? 0 : (s == 6 ? 1 : (s == 7 ? 2 : 3));
                    u32x4 sf[5];
                    sf[0] = lds128(sth + j * 4096);
#pragma unroll
                    for (int t = 0; t < 4; ++t) sf[1 + t] = lds128(stc + j * 16384 + t * 2048);
#pragma unroll
                    for (int t = 0; t < 5; ++t) { gr[t] = mfma32(sf[t], sf[t], gr[t]); rs[t] = mfma32(ONES, sf[t], rs[t]); }
                }
                if (s == 10 && g == q4) {
#pragma unroll
                    for (int t = 0; t < 5; ++t) *(LAS f32x2*)(part + ((t * 4 + lw) * 16 + r16) * 8) = (f32x2){rs[t][0], diag4(gr[t], lane)};
                }
                if (s == 11) {
                    const unsigned row = lw * 64 + lane < 80 ? lw * 64 + lane : 79;
                    const unsigned pa = part + ((row >> 4) * 64 + (row & 15)) * 8;
                    const f32x2 sm = (lds64f(pa) + lds64f(pa + 128)) + (lds64f(pa + 256) + lds64f(pa + 384));
                    *(LAS f32x2*)(fin + row * 8) = ln_stats(sm.x, sm.y);
                }
            }
        }
    }
    const rsrc_t rs_ws = mk_rsrc(wsp, WS_BYTES);
    const u64 epoch = gen + 1;
    const unsigned n3 = w & 3, m3 = w >> 2;
    const rsrc_t rs_g = mk_rsrc(P.wg, DM * FF * 2), rs_u = mk_rsrc(P.wu, DM * FF * 2);
    const rsrc_t rs_gu = mk_rsrc(m3 ? P.wu : P.wg, DM * FF * 2);
    unsigned vg0[3], vg1[3];
#pragma unroll
    for (int i = 0; i < 3; ++i) {
        const unsigned s = (n3 * 3 + i) * 64 + lane;
        const unsigned ks2 = s >= 384 ? 1 : 0, s2 = s - ks2 * 384, row = (s2 * 43691u) >> 19, pos = s2 - row * 12;
        const int umax = (int)(c < 15 ? 171 - 12 * c : 0);
        int u0 = (int)pos - (int)((row >> 3) & 1); if (u0 < 0) u0 += 12;
        int u1 = (int)pos - (int)(((row >> 3) & 1) + 2); if (u1 < 0) u1 += 12;
        const unsigned rb = (ks2 * 32 + row) * (FF * 2) + c * 192;
        vg0[i] = rb + min(u0, umax) * 16; vg1[i] = rb + min(u1, umax) * 16;
    }
    unsigned vgu[3], vgz[3];
#pragma unroll
    for (int i = 0; i < 3; ++i) {
        vgu[i] = m3 ? vg1[i] : vg0[i];
        const unsigned s = i * 64 + lane, row = (s * 43691u) >> 19, pos = s - row * 12;
        int u0 = (int)pos - (int)((row >> 3) & 1); if (u0 < 0) u0 += 12;
        vgz[i] = row * (FF * 2) + c * 192 + min(u0, (int)(c < 15 ? 171 - 12 * c : 0)) * 16;
    }
    unsigned mg0[3], mgu[3];
#pragma unroll
    for (int i = 0; i < 3; ++i) { const unsigned pi = n3 * 3 + i; mg0[i] = L3_B + (pi >= 6 ? L3_BSLOT + (pi - 6) * 1024 : pi * 1024); mgu[i] = mg0[i] + m3 * 6144; }
    const unsigned rot3 = (2 * (jj >> 3) + (jj & 7)) & 7;
#define ISSUE_B(pr) do { const unsigned sb_ = (((pr) + rot3) & 7) * (64 * FF * 2); _Pragma("unroll") for (int i_ = 0; i_ < 3; ++i_) DMA(mgu[i_], ((pr) & 3) * 2 * L3_BSLOT, vgu[i_], rs_gu, sb_, 0); } while (0)

    u32x4 h_keep;
    {
        WAIT_VM(0);
        BAR();
        if (tid == 0) __hip_atomic_store(flags + (FL_1 + c) * 32, epoch, __ATOMIC_RELAXED, __HIP_MEMORY_SCOPE_AGENT);
        const u32x4 xr = __builtin_amdgcn_raw_buffer_load_b128(rs_x, tok_off, 0, 0);
        const unsigned wl = (8 * g + q4) * 128 + (fsw * 32) + 8 * p4;
        const unsigned wb0 = wl + (kh ? 8192 : L2_WO), wb1 = wl + (kh ? L2_WO + 49152 : L2_WO + 16384), wb2 = wl + (kh ? L2_WO + 57344 : 0);
        unsigned abase = lane * 16;
        const unsigned asoff = WS_O + ((4 * hf + rt4) * 16 + 8 * kh) * 1024;
        PIN(abase);
        u32x4 bf[8][4];
#pragma unroll
        for (int k = 0; k < 8; ++k)
#pragma unroll
            for (int ct = 0; ct < 4; ++ct) bf[k][ct] = tr_frag(((k < 4 ? wb0 : (k < 6 ? wb1 : wb2)) ^ (ct * 32)) + (k < 4 ? k : (k < 6 ? k - 4 : k - 6)) * 4096, 512);
        __builtin_amdgcn_sched_barrier(0);
        if (w == 0) wait_flags(flags + (FL_1 + 8 * hf) * 32, 8, epoch, lane);
        BAR();
        u32x4 af[8];
#pragma unroll
        for (int k = 0; k < 4; ++k) af[k] = __builtin_amdgcn_raw_buffer_load_b128(rs_ws, abase + (k & 3) * 1024, asoff + (k >> 2) * 4096, AUX_SC1);
        __builtin_amdgcn_sched_barrier(0);
        f32x4 acc[4] = {Z4, Z4, Z4, Z4};
#pragma unroll
        for (int k = 0; k < 8; ++k) {
            if (k + 4 < 8) af[k + 4] = __builtin_amdgcn_raw_buffer_load_b128(rs_ws, abase + (k & 3) * 1024, asoff + 4096, AUX_SC1);
            __builtin_amdgcn_sched_barrier(0);
#pragma unroll
            for (int ct = 0; ct < 4; ++ct) acc[ct] = mfma32(bf[k][ct], af[k], acc[ct]);
            __builtin_amdgcn_sched_barrier(0);
        }
        const unsigned red = L2_RED + w * 2048 + lane * 16;
        *(LAS f32x4*)red = kh ? acc[0] : acc[2];
        *(LAS f32x4*)(red + 1024) = kh ? acc[1] : acc[3];
        BAR();
        const f32x4 a0 = (kh ? acc[2] : acc[0]) + lds128f(red ^ 8192);
        const f32x4 a1 = (kh ? acc[3] : acc[1]) + lds128f((red ^ 8192) + 1024);
        auto s0 = __builtin_amdgcn_permlane16_swap(pk_bf16(a0[0], a0[1]), pk_bf16(a1[0], a1[1]), false, false);
        auto s1 = __builtin_amdgcn_permlane16_swap(pk_bf16(a0[2], a0[3]), pk_bf16(a1[2], a1[3]), false, false);
        const u32x4 at = {s0[0], s1[0], s0[1], s1[1]};
#pragma unroll
        for (int i = 0; i < 4; ++i) h_keep[i] = pk_bf16(bf_lo(at[i]) + bf_lo(xr[i]), bf_hi(at[i]) + bf_hi(xr[i]));
        const unsigned hoff = ((2 * (g & 1) + (g >> 1)) * 16 + r16) * 16;
        const unsigned hsoff = WS_H + ((4 * hf + rt4) * 16 + 2 * hg + kh) * 1024;
        __builtin_amdgcn_raw_buffer_store_b128(h_keep, rs_ws, hoff + hsoff, 0, AUX_SC1);
        const f32x4 gs = mfma32(h_keep, h_keep, Z4), sm = mfma32(ONES, h_keep, Z4);
        const u32x2 stv = {f2u(sm[0]), f2u(diag4(gs, lane))};
        const unsigned stoff = (g == q4) ? WS_ST + c * 1024 + w * 128 + r16 * 8 : 0xFFFFFFF0u;
        __builtin_amdgcn_raw_buffer_store_b64(stv, rs_ws, stoff, 0, AUX_SC1);
        asm volatile("" ::: "memory");
    }
    const rsrc_t rs_wd = mk_rsrc(P.wd, FF * DM * 2);
    const unsigned vwd = l8 * 1024 + wsw + hg * 128;
#define KS4(s) (11 * ((s) / 10) + (s) % 10 + 1 - ((s) >= 30 ? 1 : 0))
    const unsigned swd = ((w >> 2) * 32 + (w & 3) * 8) * 1024;
#define ISSUE_D2(s0) DMA(wk, (s0) * 4096, vwd, rs_wd, swd, KS4(s0) * 32768)
    const unsigned swd0 = (w & 3) * 8 * 1024;
#define ISSUE_S() do { DMA(wk, 30 * 4096, vwd, rs_wd, swd0 + (kh ? 11 : 0) * 32768, 0); DMA(wk, 32 * 4096, vwd, rs_wd, swd0 + (kh ? 36 : 22) * 32768, 0); } while (0)
    {
        const bool active = c < 15;
        WAIT_VM(0);
        BAR();
        if (tid == 0) __hip_atomic_store(flags + (FL_2 + c) * 32, epoch, __ATOMIC_RELAXED, __HIP_MEMORY_SCOPE_AGENT);
        if (active && w != 0) {
            ISSUE_B(0); ISSUE_B(1);
            if (w == 1 || w == 2) {
                const unsigned sb_ = ((w - 1 + rot3) & 7) * (64 * FF * 2), mz = L3_B + (w - 1) * 2 * L3_BSLOT;
                DMA(mz, 0, vgz[0], rs_g, sb_, 0); DMA(mz, 1024, vgz[1], rs_g, sb_, 0); DMA(mz, 2048, vgz[2], rs_g, sb_, 0);
            }
        }
        unsigned aa = L3_A + lane * 16;
        unsigned ba[3];
#pragma unroll
        for (int i = 0; i < 3; ++i) {
            const unsigned mat = p4 >> 1;
            unsigned pos = 3 * n3 + i + (g & 1) + 2 * mat; if (pos >= 12) pos -= 12;
            ba[i] = L3_B + mat * 6144 + ((8 * g + q4) * 12 + pos) * 16 + 8 * (p4 & 1);
            PIN(ba[i]);
        }
        unsigned vl16 = lane * 16;
        const unsigned sa3 = WS_H + w * 16384;
        PIN(aa); PIN(vl16);
        if (w == 0) wait_flags(flags + FL_2 * 32, 16, epoch, lane);
        BAR();
        if (tid == 0 && c == 0) __hip_atomic_store(flags + FL_GEN * 32, epoch, __ATOMIC_RELAXED, __HIP_MEMORY_SCOPE_AGENT);
        if (active) {
#define ISSUE_A(pr) do { const unsigned sa_ = sa3 + (((pr) + rot3) & 7) * 2048; DMA_SC1(wk, L3_A + ((pr) % 3) * 16384, vl16, rs_ws, sa_, 0); DMA_SC1(wk, L3_A + ((pr) % 3) * 16384 + 8192, vl16, rs_ws, sa_, 1024); } while (0)
            ISSUE_A(0); ISSUE_A(1);
            WAIT_VM(2);
            BAR();
            if (w < 4) {
                f32x4 acc[8][3], cs[3] = {Z4, Z4, Z4};
#pragma unroll
                for (int t = 0; t < 8; ++t)
#pragma unroll
                    for (int i = 0; i < 3; ++i) acc[t][i] = Z4;
                u32x4 af[2][8], bf[2][3];
#define LOADM(s) do { const int pr_ = (s) >> 1, k2_ = (s) & 1; \
                    bf[k2_][0] = tr_frag(ba[0] + ((pr_ & 3) * 2 + k2_) * L3_BSLOT, 768); \
                    _Pragma("unroll") for (int t = 0; t < 8; ++t) af[k2_][t] = lds128(aa + (pr_ % 3) * 16384 + k2_ * 8192 + t * 1024); \
                    bf[k2_][1] = tr_frag(ba[1] + ((pr_ & 3) * 2 + k2_) * L3_BSLOT, 768); bf[k2_][2] = tr_frag(ba[2] + ((pr_ & 3) * 2 + k2_) * L3_BSLOT, 768); } while (0)
                LOADM(0);
#pragma unroll
                for (int s = 0; s < 16; ++s) {
                    if (s & 1) { if (s == 1) WAIT_VM(0); BAR(); }
                    if (s < 15) LOADM(s + 1);
#pragma unroll
                    for (int i = 0; i < 3; ++i) {
#pragma unroll
                        for (int t = 0; t < 8; ++t) acc[t][i] = mfma32(bf[s & 1][i], af[s & 1][t], acc[t][i]);
                        cs[i] = mfma32(bf[s & 1][i], ONES, cs[i]);
                    }
#pragma unroll
                    for (int i = 0; i < 14; ++i) { __builtin_amdgcn_sched_group_barrier(0x8, 1, 0); __builtin_amdgcn_sched_group_barrier(0x100, 1, 0); }
                    __builtin_amdgcn_sched_group_barrier(0x8, 13, 0);
                    __builtin_amdgcn_sched_barrier(0);
                }
                const unsigned hi = lane >> 5;
                const unsigned tau = 2 * (g & 1) + (g >> 1);
#pragma unroll
                for (int m = 0; m < 2; ++m) {
                    f32x2 st[4];
#pragma unroll
                    for (int t = 0; t < 4; ++t) st[t] = lds64f(L3_ST + 512 + m * 8192 + t * 128 + r16 * 8);
                    const float r01 = hi ? st[1].y : st[0].y, r23 = hi ? st[3].y : st[2].y;
                    const float rr01 = r01 * r01, rr23 = r23 * r23;
#pragma unroll
                    for (int i = 0; i < 3; ++i) {
                        const f32x4 cv = cs[i];
                        u32x2 hp[2];
#pragma unroll
                        for (int pp = 0; pp < 2; ++pp) {
                            const float m0_ = -st[2 * pp].x, m1_ = -st[2 * pp + 1].x;
                            const f32x4 e0 = __builtin_elementwise_fma(cv, (f32x4){m0_, m0_, m0_, m0_}, acc[4 * m + 2 * pp][i]);
                            const f32x4 e1 = __builtin_elementwise_fma(cv, (f32x4){m1_, m1_, m1_, m1_}, acc[4 * m + 2 * pp + 1][i]);
                            f32x4 gt, up;
#pragma unroll
                            for (int r = 0; r < 4; ++r) {
                                auto sw = __builtin_amdgcn_permlane32_swap(f2u(e0[r]), f2u(e1[r]), false, false);
                                const unsigned x0 = sw[0], x1 = sw[1];
                                gt[r] = u2f((unsigned)max((int)x0, 0)); up[r] = u2f(x1);
                            }
                            const f32x4 hv = gt * (up * (pp ? rr23 : rr01));
                            hp[pp] = (u32x2){pk_bf16(hv[0], hv[1]), pk_bf16(hv[2], hv[3])};
                        }
                        auto s0 = __builtin_amdgcn_permlane16_swap(hp[0].x, hp[1].x, false, false);
                        auto s1 = __builtin_amdgcn_permlane16_swap(hp[0].y, hp[1].y, false, false);
                        const u32x4 hf4 = {s0[0], s1[0], s0[1], s1[1]};
                        const unsigned U = 12 * c + 3 * n3 + i;
                        const unsigned soff = WS_HID + ((4 * m) * NKS4 + (U >> 2)) * 1024 + (U & 3) * 256;
                        const unsigned off = U < 172 ? tau * (NKS4 * 1024) + r16 * 16 + soff : 0xFFFFFFF0u;
                        __builtin_amdgcn_raw_buffer_store_b128(hf4, rs_ws, off, 0, AUX_SC1);
                    }
                }
                asm volatile("" ::: "memory");
            } else {
                const unsigned lw = w - 4, wk0 = lw * 1024, sa30 = WS_H + lw * 16384;
#define A4(pr) do { const unsigned sa_ = sa30 + (((pr) + rot3) & 7) * 2048; const int o_ = L3_A + ((pr) % 3) * 16384; \
                    DMA_SC1(wk0, o_, vl16, rs_ws, sa_, 0); DMA_SC1(wk0, o_ + 8192, vl16, rs_ws, sa_, 1024); DMA_SC1(wk0, o_ + 4096, vl16, rs_ws, sa_, 65536); DMA_SC1(wk0, o_ + 12288, vl16, rs_ws, sa_, 65536 + 1024); } while (0)
#define B6(pr) do { const unsigned sb_ = (((pr) + rot3) & 7) * (64 * FF * 2); const int o_ = ((pr) & 3) * 2 * L3_BSLOT; \
                    _Pragma("unroll") for (int i_ = 0; i_ < 3; ++i_) DMA(mg0[i_], o_, vg0[i_], rs_g, sb_, 0); \
                    _Pragma("unroll") for (int i_ = 0; i_ < 3; ++i_) DMA(mg0[i_], o_ + 6144, vg1[i_], rs_u, sb_, 0); } while (0)
#define D4(s0) do { DMA(wk0, (s0) * 4096, vwd, rs_wd, swd0, KS4(s0) * 32768); DMA(wk0, (s0) * 4096 + 4096, vwd, rs_wd, swd0, KS4(s0) * 32768 + 32768); } while (0)
                const unsigned sa = L3_ST + (lane & 3) * 2048 + (lw * 16 + (lane >> 2)) * 8;
#pragma unroll
                for (int s = 0; s < 16; ++s) {
                    if (s & 1) {
                        if (s == 1 || s == 3) WAIT_VM(20); else if (s < 13) WAIT_VM(16); else if (s == 13) WAIT_VM(12);
                        BAR();
                        const int po = s >> 1;
                        if (po + 3 < 8) A4(po + 3 < 8 ? po + 3 : 7);
                        if (po + 4 < 8) B6(po + 4 < 8 ? po + 4 : 7);
                        if (po >= 4 && po < 7) { D4(12 + 6 * (po >= 4 ? po - 4 : 0)); D4(14 + 6 * (po >= 4 ? po - 4 : 0)); D4(16 + 6 * (po >= 4 ? po - 4 : 0)); }
                        if (s == 15) {
                            DMA(wk0, 30 * 4096, vwd, rs_wd, swd0, 0); DMA(wk0, 31 * 4096, vwd, rs_wd, swd0, 11 * 32768);
                            DMA(wk0, 32 * 4096, vwd, rs_wd, swd0, 22 * 32768); DMA(wk0, 33 * 4096, vwd, rs_wd, swd0, 36 * 32768);
                            D4(0); D4(2); D4(4);
                        }
                    }
                    if (s == 0) {
                        B6(2); A4(2); B6(3);
                        const unsigned sst = WS_ST + lw * 2048;
                        DMA_SC1(wk0 + wk0, L3_ST, vl16, rs_ws, sst, 0); DMA_SC1(wk0 + wk0, L3_ST + 1024, vl16, rs_ws, sst, 1024);
                        DMA_SC1(wk0 + wk0, L3_ST + 8192, vl16, rs_ws, sst, 8192); DMA_SC1(wk0 + wk0, L3_ST + 8192 + 1024, vl16, rs_ws, sst, 8192 + 1024);
                    }
                    if (s == 5) {
#pragma unroll
                        for (int hfz = 0; hfz < 2; ++hfz) {
                            const unsigned a = sa + hfz * 8192;
                            const f32x2 sm = (lds64f(a) + lds64f(a + 512)) + (lds64f(a + 1024) + lds64f(a + 1536));
                            float sx = sm.x, sq = sm.y;
                            sx += __builtin_amdgcn_update_dpp(0.f, sx, 0xB1, 0xf, 0xf, false); sq += __builtin_amdgcn_update_dpp(0.f, sq, 0xB1, 0xf, 0xf, false);
                            sx += __builtin_amdgcn_update_dpp(0.f, sx, 0x4E, 0xf, 0xf, false); sq += __builtin_amdgcn_update_dpp(0.f, sq, 0x4E, 0xf, 0xf, false);
                            *(LAS f32x2*)((a & ~(unsigned)6144) + 512) = ln_stats(sx, sq);
                        }
                    }
                }
            }
        }
    }
    {
        const bool active = c < 15;
        if (!active) {
            ISSUE_D2(12); ISSUE_D2(14); ISSUE_D2(16); ISSUE_D2(18); ISSUE_D2(20); ISSUE_D2(22); ISSUE_D2(24); ISSUE_D2(26); ISSUE_D2(28);
            ISSUE_S();
            ISSUE_D2(0); ISSUE_D2(2); ISSUE_D2(4);
        }
        WAIT_VM(0);
        BAR();
        if (tid == 0 && active) __hip_atomic_store(flags + (FL_3 + c) * 32, epoch, __ATOMIC_RELAXED, __HIP_MEMORY_SCOPE_AGENT);
#define REST_D() do { ISSUE_D2(6); ISSUE_D2(8); ISSUE_D2(10); ISSUE_D2(34); ISSUE_D2(36); ISSUE_D2(38); } while (0)
        if (w != 0) REST_D();
        const unsigned wb0 = (8 * g + q4) * 128 + (fsw * 32) + 8 * p4;
        u32x4 bq[11][4];
#pragma unroll
        for (int ct = 0; ct < 4; ++ct) {
            const u32x4 t0 = tr_frag((wb0 ^ (ct * 32)) + (30 + (kq4 < 3 ? kq4 : 0)) * 4096, 512);
            bq[0][ct] = kq4 < 3 ? t0 : (u32x4){0u, 0u, 0u, 0u};
        }
        BAR();
#define LAST_D() do { ISSUE_D2(30); DMA(wk, 32 * 4096, vwd, rs_wd, swd, 35 * 32768); } while (0)
        if (w != 0) LAST_D();
        unsigned wbk = wb0 + kq4 * (10 * 4096);
        unsigned abase = lane * 16;
        const unsigned asoff = WS_HID + ((4 * hf + 2 * rp4) * NKS4 + 11 * kq4 - (kq4 == 3 ? 1 : 0)) * 1024;
        PIN(wbk); PIN(abase);
        if (w == 0) wait_flags(flags + FL_3 * 32, 15, epoch, lane);
        BAR();
        u32x4 af[2][11];
#define LOADA4(k) do { af[0][k] = __builtin_amdgcn_raw_buffer_load_b128(rs_ws, abase + ((k) & 3) * 1024, asoff + ((k) >> 2) * 4096, AUX_SC1); \
                       af[1][k] = __builtin_amdgcn_raw_buffer_load_b128(rs_ws, abase + ((k) & 3) * 1024, asoff + NKS4 * 1024 + ((k) >> 2) * 4096, AUX_SC1); } while (0)
        if (w == 0) { REST_D(); LAST_D(); }
        LOADA4(0); LOADA4(1); LOADA4(2);
        WAIT_VM(6);
        BAR();
#define LOAD4(j) do { _Pragma("unroll") for (int ct = 0; ct < 4; ++ct) bq[j][ct] = tr_frag((wbk ^ (ct * 32)) + ((j) - 1) * 4096, 512); } while (0)
        LOAD4(1); LOAD4(2);
        __builtin_amdgcn_sched_barrier(0);
        f32x4 acc[2][4] = {{Z4, Z4, Z4, Z4}, {Z4, Z4, Z4, Z4}};
#pragma unroll
        for (int j = 0; j < 11; ++j) {
            if (j + 3 < 11) { LOADA4(j + 3); LOAD4(j + 3); }
#pragma unroll
            for (int rl = 0; rl < 2; ++rl)
#pragma unroll
                for (int ct = 0; ct < 4; ++ct) acc[rl][ct] = mfma32(bq[j][ct], af[rl][j], acc[rl][ct]);
#pragma unroll
            for (int i = 0; i < 8; ++i) { __builtin_amdgcn_sched_group_barrier(0x8, 1, 0); __builtin_amdgcn_sched_group_barrier(0x100, 1, 0); }
            __builtin_amdgcn_sched_barrier(0);
        }
        BAR();
        const unsigned red = w * 8192 + lane * 16;
#pragma unroll
        for (int rl = 0; rl < 2; ++rl)
#pragma unroll
            for (int ct = 0; ct < 4; ++ct) *(LAS f32x4*)(red + (rl * 4 + ct) * 1024) = acc[rl][ct];
        BAR();
        f32x4 a0 = Z4, a1 = Z4;
#pragma unroll
        for (int o = 0; o < 4; ++o) {
            const unsigned ra = (2 * rp4 + (o & 1) + 4 * (o >> 1)) * 8192 + lane * 16 + ((rt4 & 1) * 4 + 2 * kh) * 1024;
            a0 += lds128f(ra); a1 += lds128f(ra + 1024);
        }
        auto s0 = __builtin_amdgcn_permlane16_swap(pk_bf16(a0[0], a0[1]), pk_bf16(a1[0], a1[1]), false, false);
        auto s1 = __builtin_amdgcn_permlane16_swap(pk_bf16(a0[2], a0[3]), pk_bf16(a1[2], a1[3]), false, false);
        const u32x4 at = {s0[0], s1[0], s0[1], s1[1]};
        u32x4 o;
#pragma unroll
        for (int i = 0; i < 4; ++i) o[i] = pk_bf16(bf_lo(at[i]) + bf_lo(h_keep[i]), bf_hi(at[i]) + bf_hi(h_keep[i]));
        *(u32x4*)((char*)P.out + tok_off) = o;
    }
}

}

extern "C" void kernel_launch(void* const* d_in, const int* in_sizes, int n_in, void* d_out, int out_size, void* d_ws, size_t ws_size, hipStream_t stream) {
    Params P;
    P.x = (const u16*)d_in[0]; P.wqkv = (const u16*)d_in[1]; P.wo = (const u16*)d_in[2];
    P.wg = (const u16*)d_in[4]; P.wu = (const u16*)d_in[5]; P.wd = (const u16*)d_in[6];
    P.out = (u16*)d_out; P.ws = (unsigned char*)d_ws;
    (void)hipFuncSetAttribute((const void*)fused_block, hipFuncAttributeMaxDynamicSharedMemorySize, LDS_BYTES);
    hipLaunchKernelGGL(fused_block, dim3(256), dim3(512), LDS_BYTES, stream, P);
}
```

```cpp
#include <hip/hip_runtime.h>
#include <hip/hip_bf16.h>
#include <stdint.h>
#pragma clang diagnostic ignored "-Wint-to-pointer-cast"

#define LAS __attribute__((address_space(3)))
#define GAS __attribute__((address_space(1)))
typedef unsigned short u16;
typedef unsigned long long u64;
typedef short s16x4 __attribute__((ext_vector_type(4)));
typedef __bf16 bf16x8 __attribute__((ext_vector_type(8)));
typedef __bf16 bf16x2 __attribute__((ext_vector_type(2)));
typedef float f32x4 __attribute__((ext_vector_type(4)));
typedef float f32x2 __attribute__((ext_vector_type(2)));
typedef unsigned u32x4 __attribute__((ext_vector_type(4)));
typedef unsigned u32x2 __attribute__((ext_vector_type(2)));
typedef GAS u64 gu64;
typedef __amdgpu_buffer_rsrc_t rsrc_t;

namespace {

constexpr int SEQ = 2048, DM = 512, FF = 1376;
constexpr int NKS4 = FF / 32;
constexpr unsigned WS_FLAG = 0;
constexpr unsigned WS_ST = 16384;
constexpr unsigned WS_O = 40960;
constexpr unsigned WS_H = WS_O + 131072;
constexpr unsigned WS_HID = WS_H + 131072;
constexpr unsigned WS_SPARE = WS_HID + 8 * 43 * 1024;
constexpr unsigned WS_BYTES = WS_SPARE + 8192;
constexpr int FL_GEN = 0, FL_1 = 8, FL_2 = 24, FL_3 = 40;
constexpr int AUX_SC1 = 16;
constexpr unsigned kSpinCap = 1u << 16;
constexpr unsigned CODE_KB = 29;

constexpr int LDS_BYTES = 163840;
constexpr int L1_X = 0, L1_H = 65536, L1_W = 81920, L1_WSLOT = 24576, L1_SCR = 155648;
constexpr int L2_WO = 81920, L2_RED = 114688;
constexpr int L3_A = 0, L3_B = 49152, L3_BSLOT = 12288, L3_ST = 147456;

#define WAIT_VM(n) asm volatile("s_waitcnt vmcnt(%0)" :: "i"(n) : "memory")
#define BAR() do { asm volatile("s_waitcnt lgkmcnt(0)" ::: "memory"); __builtin_amdgcn_s_barrier(); asm volatile("" ::: "memory"); } while (0)
#define PIN(x) asm volatile("" : "+v"(x))
#define DMA(m0b, ldsoff, voff, rs, sbase, soffimm) do { unsigned t_; asm volatile("s_add_u32 m0, %1, %2\n\ts_add_u32 %0, %5, %6\n\tbuffer_load_dwordx4 %3, %4, %0 offen lds" \
    : "=&s"(t_) : "s"(m0b), "i"(ldsoff), "v"(voff), "s"(rs), "s"(sbase), "i"(soffimm) : "scc", "memory"); } while (0)
#define DMA_SC1(m0b, ldsoff, voff, rs, sbase, soffimm) do { unsigned t_; asm volatile("s_add_u32 m0, %1, %2\n\ts_add_u32 %0, %5, %6\n\tbuffer_load_dwordx4 %3, %4, %0 offen sc1 lds" \
    : "=&s"(t_) : "s"(m0b), "i"(ldsoff), "v"(voff), "s"(rs), "s"(sbase), "i"(soffimm) : "scc", "memory"); } while (0)

__device__ __forceinline__ rsrc_t mk_rsrc(const void* p, unsigned bytes) { return __builtin_amdgcn_make_buffer_rsrc((void*)p, 0, bytes, 0x00020000); }
__device__ __forceinline__ float u2f(unsigned u) { return __builtin_bit_cast(float, u); }
__device__ __forceinline__ unsigned f2u(float f) { return __builtin_bit_cast(unsigned, f); }
__device__ __forceinline__ unsigned pk_bf16(float a, float b) { bf16x2 v; v[0] = (__bf16)a; v[1] = (__bf16)b; return __builtin_bit_cast(unsigned, v); }
__device__ __forceinline__ float bf_lo(unsigned u) { return u2f(u << 16); }
__device__ __forceinline__ float bf_hi(unsigned u) { return u2f(u & 0xffff0000u); }
__device__ __forceinline__ float rbf(float f) { return (float)(__bf16)f; }
__device__ __forceinline__ f32x4 mfma32(u32x4 a, u32x4 b, f32x4 c) {
    return __builtin_amdgcn_mfma_f32_16x16x32_bf16(__builtin_bit_cast(bf16x8, a), __builtin_bit_cast(bf16x8, b), c, 0, 0, 0);
}
__device__ __forceinline__ f32x4 mfma16(u32x2 a, u32x2 b, f32x4 c) {
    return __builtin_amdgcn_mfma_f32_16x16x16bf16_1k(__builtin_bit_cast(s16x4, a), __builtin_bit_cast(s16x4, b), c, 0, 0, 0);
}
__device__ __forceinline__ u32x4 lds128(unsigned a) { return *(LAS u32x4*)a; }
__device__ __forceinline__ f32x4 lds128f(unsigned a) { return *(LAS f32x4*)a; }
__device__ __forceinline__ f32x2 lds64f(unsigned a) { return *(LAS f32x2*)a; }
__device__ __forceinline__ u32x2 tr_read(unsigned a) { return __builtin_bit_cast(u32x2, __builtin_amdgcn_ds_read_tr16_b64_v4i16((LAS s16x4*)a)); }
__device__ __forceinline__ u32x4 tr_frag(unsigned a, int step) { const u32x2 x = tr_read(a), y = tr_read(a + step); return (u32x4){x.x, x.y, y.x, y.y}; }
__device__ __forceinline__ float swap16_lo(float v) {
    const unsigned a = f2u(v); auto r = __builtin_amdgcn_permlane16_swap(a, a, false, false); const unsigned x = r[0]; return u2f(x);
}
__device__ __forceinline__ float max2(float a, float b) { return __builtin_amdgcn_fmed3f(a, b, __builtin_inff()); }
__device__ __forceinline__ float xmax16_32(float v) {
    unsigned a = f2u(v);
    auto r = __builtin_amdgcn_permlane16_swap(a, a, false, false);
    const unsigned r0 = r[0], r1 = r[1];
    v = max2(u2f(r0), u2f(r1));
    a = f2u(v);
    auto s = __builtin_amdgcn_permlane32_swap(a, a, false, false);
    const unsigned s0 = s[0], s1 = s[1];
    return max2(u2f(s0), u2f(s1));
}
__device__ __forceinline__ float bcast_row0(float v) { return __builtin_amdgcn_update_dpp(0.f, v, 0x150, 0xf, 0xf, false); }
__device__ __forceinline__ float diag4(f32x4 d, unsigned lane) { const float a = (lane & 1) ? d[1] : d[0], b = (lane & 1) ? d[3] : d[2]; return (lane & 2) ? b : a; }
__device__ __forceinline__ f32x2 ln_stats(float s, float q) {
    const float mean = s * (1.0f / DM);
    const float var = __builtin_fmaxf(q * (1.0f / DM) - mean * mean, 0.0f);
    const float sd = rbf(__builtin_amdgcn_sqrtf(rbf(rbf(var) + 1e-5f)));
    return (f32x2){rbf(mean), __builtin_amdgcn_rcpf(sd)};
}
__device__ __forceinline__ void wait_flags(gu64* base, unsigned n, u64 epoch, unsigned lane) {
    for (unsigned spins = 0; spins < kSpinCap; ++spins) {
        u64 v = epoch;
        if (lane < n) v = __hip_atomic_load(base + lane * 32, __ATOMIC_RELAXED, __HIP_MEMORY_SCOPE_AGENT);
        if (__all(v == epoch)) break;
    }
    __builtin_amdgcn_fence(__ATOMIC_ACQUIRE, "wavefront");
}

struct Params { const u16* x; const u16* wqkv; const u16* wo; const u16* wg; const u16* wu; const u16* wd; u16* out; unsigned char* ws; };

__global__ void __launch_bounds__(512, 2) __attribute__((amdgpu_waves_per_eu(2, 2))) fused_block(Params P) {
    const unsigned tid = threadIdx.x, lane = tid & 63;
    const unsigned w = __builtin_amdgcn_readfirstlane(tid >> 6);
    const unsigned b = blockIdx.x, xcd = b & 7, jj = b >> 3;
    const unsigned p = 4 * (xcd >> 1) + (jj >> 3);
    const unsigned hf = (jj >> 2) & 1, hg = 4 * (xcd & 1) + (jj & 3);
    const unsigned c = 8 * hf + hg;
    const unsigned r16 = lane & 15, g = lane >> 4, l8 = lane >> 3, l7 = lane & 7, q4 = r16 >> 2, p4 = lane & 3;
    {
        u64 pc; asm volatile("s_getpc_b64 %0" : "=s"(pc));
        const char* cp = (const char*)(pc & ~(u64)1023) + ((jj * 8 + w) % CODE_KB) * 1024 + lane * 16;
        asm volatile("s_mov_b32 m0, %0\n\ts_nop 0\n\tglobal_load_lds_dwordx4 %1, off" :: "s"(L1_SCR + 6144), "v"(cp) : "memory");
    }
    const unsigned wk = w * 1024;
    const rsrc_t rs_x = mk_rsrc(P.x, SEQ * DM * 2);
    const rsrc_t rs_w = mk_rsrc(P.wqkv, DM * 3 * DM * 2);
    unsigned char* const wsp = P.ws + (size_t)p * WS_BYTES;
    gu64* const flags = (gu64*)(wsp + WS_FLAG);
    const unsigned T0 = 128 * p + 64 * hf;
    if (w >= 4) {
        const char* t1 = lane < 22 ? (const char*)P.wg + lane * 65536 : (lane < 44 ? (const char*)P.wu + (lane - 22) * 65536 : (const char*)P.wd + (lane - 44) * 65536);
        const char* t2 = lane < 8 ? (const char*)P.wo + lane * 65536 : (lane < 32 ? (const char*)P.out + p * 131072 + (lane - 8) * 4096 : (const char*)P.wd + (20 + (lane & 1)) * 65536);
        const char* tw = w == 4 ? (const char*)wsp + WS_FLAG + lane * 256 : (w == 5 ? t1 : (w == 6 ? t2 : (const char*)wsp + WS_SPARE + lane * 16));
        asm volatile("s_mov_b32 m0, %0\n\ts_nop 0\n\tglobal_load_lds_dwordx4 %1, off" :: "s"(L1_SCR + 6144), "v"(tw) : "memory");
    }
    u32x4 ONES = {0x3F803F80u, 0x3F803F80u, 0x3F803F80u, 0x3F803F80u};
    PIN(ONES);
    const f32x4 Z4 = {0.f, 0.f, 0.f, 0.f};
    const unsigned fsw = ((q4 >> 1) & 1) | ((g & 1) << 1);
    const unsigned wsw = (l7 ^ (((g & 1) | ((w & 1) << 1)) << 1)) * 16;
    u64 gen = 0;
    const unsigned rt4 = w & 3, kh = w >> 2, rp4 = rt4 >> 1, kq4 = (rt4 & 1) + 2 * kh;
    const unsigned tok_off = (128 * p + 64 * hf + 32 * (rt4 & 1) + (rt4 >> 1) + 2 * r16) * 1024 + (64 * hg + 32 * kh + 16 * (g & 1) + 8 * (g >> 1)) * 2;

    {
        const unsigned rot = (2 * (jj & 3) + (jj >> 2) + (xcd >> 1)) & 7;
        const unsigned hh = w & 3;
        const unsigned vh = l8 * 2048 + ((l7 ^ g) * 16);
        const unsigned vxr = vh ^ ((w & 1) * 64);
        const unsigned sxr = (T0 + 32 * ((w >> 1) & 1) + (w >> 2) + 16 * (w & 1)) * 1024;
        const unsigned vw = (8 * w + l8) * 3072 + wsw + hg * 128;
        {
            const unsigned kap = (w + rot) & 7;
            const unsigned sh = (T0 == 0 ? 0 : T0 - 16) * 1024 + kap * 128;
            const unsigned hb = L1_H + w * 2048;
            if (lane >= 8) { DMA(hb, 0, vh, rs_x, sh, 0); const unsigned vh1 = vh ^ 64; DMA(hb, 1024, vh1, rs_x, sh, 1024); }
            else { *(LAS u32x4*)(hb + lane * 16) = ONES; *(LAS u32x4*)(hb + 1024 + lane * 16) = ONES; }
        }
        unsigned kx[8];
#pragma unroll
        for (int q = 0; q < 8; ++q) kx[q] = (q + rot) & 7;
#define TOUCH(ptr) asm volatile("s_mov_b32 m0, %0\n\ts_nop 0\n\tglobal_load_lds_dwordx4 %1, off" :: "s"(L1_SCR + 6144), "v"(ptr) : "memory")
#define ISSUE_X(q) DMA(wk, L1_X + (q) * 8192, vxr, rs_x, sxr + kx[q] * 128, 0)
#define ISSUE_W1(q, mat) DMA(wk, L1_W + ((q) % 3) * L1_WSLOT + (mat) * 8192, vw, rs_w, kx[q] * 196608, (mat) * 1024)
#define ISSUE_W(q) do { ISSUE_W1(q, 0); ISSUE_W1(q, 1); ISSUE_W1(q, 2); } while (0)
        ISSUE_X(0); ISSUE_W(0);
        __builtin_amdgcn_s_barrier();
        ISSUE_X(1); ISSUE_W(1);
        { const char* gp = (const char*)wsp + WS_FLAG + lane * 16; asm volatile("s_mov_b32 m0, %0\n\ts_nop 0\n\tglobal_load_lds_dwordx4 %1, off sc1" :: "s"(L1_SCR + 7168), "v"(gp) : "memory"); }
#define READ_GEN() do { const u32x2 gv_ = *(LAS u32x2*)(L1_SCR + 7168 + FL_GEN * 256); gen = ((u64)(unsigned)__builtin_amdgcn_readfirstlane(gv_.y) << 32) | (unsigned)__builtin_amdgcn_readfirstlane(gv_.x); } while (0)
        const unsigned part = L1_SCR, fin = L1_SCR + 2560;
        const unsigned xa = (r16 >> 3) * 1024 + (r16 & 7) * 128 + ((g ^ ((r16 >> 1) & 7)) * 16);
        const rsrc_t rs_wo = mk_rsrc(P.wo, DM * DM * 2);
        WAIT_VM(5);
        BAR();
        if (w < 4) {
            unsigned xc0 = xa, xc1 = xa ^ 64, xh0 = xa + L1_H, xh1 = xh0 ^ 64;
            unsigned wa = L1_W + (8 * g + q4) * 128 + ((hh ^ fsw) * 32) + 8 * p4, wa2 = wa + 2 * L1_WSLOT;
            PIN(xc0); PIN(xc1); PIN(xh0); PIN(xh1); PIN(wa); PIN(wa2);
            f32x4 aq[5], ak[5], av[5];
#pragma unroll
            for (int t = 0; t < 5; ++t) { aq[t] = Z4; ak[t] = Z4; av[t] = Z4; }
            u32x4 xf[2][5], wf[2][3];
#define LOADM1(s) do { const int kq_ = (s) >> 1, kk_ = (s) & 1; const unsigned wsl_ = (kq_ % 3 == 2 ? wa2 : wa + (kq_ % 3) * L1_WSLOT) + kk_ * 4096; \
                wf[kk_][0] = tr_frag(wsl_, 512); xf[kk_][0] = lds128((kk_ ? xh1 : xh0) + kq_ * 2048); \
                _Pragma("unroll") for (int t = 0; t < 4; ++t) xf[kk_][1 + t] = lds128((kk_ ? xc1 : xc0) + kq_ * 8192 + t * 2048); \
                wf[kk_][1] = tr_frag(wsl_ + 8192, 512); wf[kk_][2] = tr_frag(wsl_ + 16384, 512); } while (0)
            LOADM1(0);
#pragma unroll
            for (int s = 0; s < 16; ++s) {
                if ((s & 1) && s < 15) { if (s == 1) WAIT_VM(1); if (s == 9) WAIT_VM(0); BAR(); }
                if (s == 9) READ_GEN();
                if (s < 15) LOADM1(s + 1);
#pragma unroll
                for (int t = 0; t < 5; ++t) aq[t] = mfma32(wf[s & 1][0], xf[s & 1][t], aq[t]);
#pragma unroll
                for (int t = 0; t < 5; ++t) ak[t] = mfma32(wf[s & 1][1], xf[s & 1][t], ak[t]);
#pragma unroll
                for (int t = 0; t < 5; ++t) av[t] = mfma32(xf[s & 1][t], wf[s & 1][2], av[t]);
#pragma unroll
                for (int i = 0; i < 11; ++i) { __builtin_amdgcn_sched_group_barrier(0x8, 1, 0); __builtin_amdgcn_sched_group_barrier(0x100, 1, 0); }
                __builtin_amdgcn_sched_group_barrier(0x8, 4, 0);
                __builtin_amdgcn_sched_barrier(0);
            }
            f32x4 csq, csk;
#pragma unroll
            for (int r = 0; r < 4; ++r) { csq[r] = bcast_row0(aq[0][r]); csk[r] = bcast_row0(ak[0][r]); }
            const float csv = swap16_lo(av[0][0]);
            u32x2 qb[5], kb[5], vb[5];
#pragma unroll
            for (int t = 0; t < 5; ++t) {
                const f32x2 sl = lds64f(fin + (t * 16 + r16) * 8);
                const f32x4 s01 = lds128f(fin + (t * 16 + 4 * g) * 8), s23 = lds128f(fin + (t * 16 + 4 * g) * 8 + 16);
                const f32x4 nm = {-sl.x, -sl.x, -sl.x, -sl.x};
                const f32x4 kv = __builtin_elementwise_fma(csk, nm, ak[t]) * sl.y;
                kb[t] = (u32x2){pk_bf16(kv[0], kv[1]), pk_bf16(kv[2], kv[3])};
                if (t > 0) { const f32x4 qv = __builtin_elementwise_fma(csq, nm, aq[t]) * sl.y; qb[t] = (u32x2){pk_bf16(qv[0], qv[1]), pk_bf16(qv[2], qv[3])}; }
                const f32x4 mn = {s01[0], s01[2], s23[0], s23[2]}, rd = {s01[1], s01[3], s23[1], s23[3]};
                const f32x4 vv = __builtin_elementwise_fma(mn, (f32x4){-csv, -csv, -csv, -csv}, av[t]) * rd;
                vb[t] = (u32x2){pk_bf16(vv[0], vv[1]), pk_bf16(vv[2], vv[3])};
            }
            f32x4 mh[2], mp, mc;
#pragma unroll
            for (int r = 0; r < 4; ++r) {
                const unsigned kap = 4 * g + r;
                mh[0][r] = (T0 > 0 && kap < 8 && kap >= r16 + 1) ? 0.f : -1e30f;
                mh[1][r] = (T0 > 0 && kap >= 8 && (kap & 7) >= r16 + 1) ? 0.f : -1e30f;
                mp[r] = (kap >= r16 + 9) ? 0.f : -1e30f;
                mc[r] = (kap <= r16 && r16 - kap <= 7) ? 0.f : -1e30f;
            }
            const float SC = 0.25f * 1.44269504088896f;
            const rsrc_t rs_ws1 = mk_rsrc(wsp, WS_BYTES);
#pragma unroll
            for (int pz = 0; pz < 2; ++pz) {
                u32x2 ob[2];
#pragma unroll
                for (int a = 0; a < 2; ++a) {
                    const int cur = 1 + 2 * pz + a, prev = a == 0 ? 0 : cur - 1;
                    f32x4 sp = mfma16(kb[prev], qb[cur], a == 0 ? mh[pz] : mp), sc = mfma16(kb[cur], qb[cur], mc);
                    float mx = max2(max2(max2(sp[0], sp[1]), max2(sp[2], sp[3])), max2(max2(sc[0], sc[1]), max2(sc[2], sc[3])));
                    mx = xmax16_32(mx);
                    const float mb = -mx * SC;
#pragma unroll
                    for (int r = 0; r < 4; ++r) { sp[r] = __builtin_amdgcn_exp2f(sp[r] * SC + mb); sc[r] = __builtin_amdgcn_exp2f(sc[r] * SC + mb); }
                    const u32x4 pf = {pk_bf16(sp[0], sp[1]), pk_bf16(sp[2], sp[3]), pk_bf16(sc[0], sc[1]), pk_bf16(sc[2], sc[3])};
                    const u32x4 vf = {vb[prev].x, vb[prev].y, vb[cur].x, vb[cur].y};
                    const f32x4 sm = mfma32(ONES, pf, Z4);
                    const f32x4 o = mfma32(vf, pf, Z4) * __builtin_amdgcn_rcpf(sm[0]);
                    ob[a] = (u32x2){pk_bf16(o[0], o[1]), pk_bf16(o[2], o[3])};
                }
                auto s0 = __builtin_amdgcn_permlane16_swap(ob[0].x, ob[1].x, false, false);
                auto s1 = __builtin_amdgcn_permlane16_swap(ob[0].y, ob[1].y, false, false);
                const u32x4 o_frag = {s0[0], s1[0], s0[1], s1[1]};
                const unsigned off = ((g & 1) * 16384) + ((2 * (hh & 1) + (g >> 1)) * 16 + r16) * 16;
                const unsigned soff = WS_O + ((4 * hf + 2 * pz) * 16 + 2 * hg + (hh >> 1)) * 1024;
                __builtin_amdgcn_raw_buffer_store_b128(o_frag, rs_ws1, off + soff, 0, AUX_SC1);
            }
            asm volatile("" ::: "memory");
        } else {
            const unsigned lw = w - 4, wk0 = lw * 1024;
            const unsigned sx0 = (T0 + 32 * ((lw >> 1) & 1) + 16 * (lw & 1)) * 1024;
            const unsigned vw0 = (8 * lw + l8) * 3072 + wsw + hg * 128, vwo0 = (8 * lw + l8) * 1024 + wsw + hg * 128;
#define X2(q) do { DMA(wk0, L1_X + (q) * 8192, vxr, rs_x, sx0 + kx[q] * 128, 0); DMA(wk0, L1_X + (q) * 8192 + 4096, vxr, rs_x, sx0 + kx[q] * 128, 1024); } while (0)
#define W6(q) do { _Pragma("unroll") for (int mat_ = 0; mat_ < 3; ++mat_) { DMA(wk0, L1_W + ((q) % 3) * L1_WSLOT + mat_ * 8192, vw0, rs_w, kx[q] * 196608, mat_ * 1024); \
                                                                            DMA(wk0, L1_W + ((q) % 3) * L1_WSLOT + mat_ * 8192 + 4096, vw0, rs_w, kx[q] * 196608, mat_ * 1024 + 98304); } } while (0)
#define O2(lds, m) do { DMA(wk0, (lds), vwo0, rs_wo, 0u, (m) * 65536); DMA(wk0, (lds) + 4096, vwo0, rs_wo, 0u, (m) * 65536 + 32768); } while (0)
            const unsigned sxa = xa ^ ((lw & 1) * 64);
            const unsigned stc = sxa + (lw >> 1) * 8192, sth = sxa + L1_H + (lw >> 1) * 2048;
            f32x4 gr[5], rs[5];
#pragma unroll
            for (int t = 0; t < 5; ++t) { gr[t] = Z4; rs[t] = Z4; }
#pragma unroll
            for (int s = 0; s < 16; ++s) {
                if ((s & 1) && s < 15) {
                    if (s == 1) WAIT_VM(9); else if (s == 3) WAIT_VM(12); else if (s == 5) WAIT_VM(14 + 7); else if (s == 7) WAIT_VM(10 + 7); else if (s < 13) WAIT_VM(6); else WAIT_VM(10);
                    BAR();
                    const int it = (s + 1) / 2;
                    if (it <= 5) W6(it + 2 > 7 ? 7 : it + 2);
                    if (it == 1) { X2(3); X2(4); X2(5); }
                    if (it == 2) { X2(6); X2(7); }
                    if (it == 6) { O2(L2_WO + 6 * 8192, 6); O2(L2_WO + 7 * 8192, 7); O2(0, 3); O2(8192, 4); O2(16384, 5); }
                    if (it == 7) { O2(L2_WO, 0); O2(L2_WO + 8192, 1); O2(L2_WO + 2 * 8192, 2); }
                    if (s == 9) READ_GEN();
                }
                if (s == 0) { X2(2); W6(2); }
                if (s == 3) {
                    const char* tz = (const char*)wsp + 32768u + lane * 16;
                    TOUCH(tz);
#pragma unroll
                    for (int i = 0; i < 6; ++i) {
                        unsigned qq = b * 24 + lw * 6 + i; if (qq >= 4640) qq -= 4640;
                        const char* base = qq < 512 ? (const char*)P.wo + qq * 1024 : (qq < 1888 ? (const char*)P.wg + (qq - 512) * 1024 : (qq < 3264 ? (const char*)P.wu + (qq - 1888) * 1024 : (const char*)P.wd + (qq - 3264) * 1024));
                        const char* t3 = base + lane * 16;
                        TOUCH(t3);
                    }
                }
                if (s == 5 || s == 6 || s == 7 || s == 9) {
                    const int j = s == 5 ? 0 : (s == 6 ? 1 : (s == 7 ? 2 : 3));
                    u32x4 sf[5];
                    sf[0] = lds128(sth + j * 4096);
#pragma unroll
                    for (int t = 0; t < 4; ++t) sf[1 + t] = lds128(stc + j * 16384 + t * 2048);
#pragma unroll
                    for (int t = 0; t < 5; ++t) { gr[t] = mfma32(sf[t], sf[t], gr[t]); rs[t] = mfma32(ONES, sf[t], rs[t]); }
                }
                if (s == 10 && g == q4) {
#pragma unroll
                    for (int t = 0; t < 5; ++t) *(LAS f32x2*)(part + ((t * 4 + lw) * 16 + r16) * 8) = (f32x2){rs[t][0], diag4(gr[t], lane)};
                }
                if (s == 11) {
                    const unsigned row = lw * 64 + lane < 80 ? lw * 64 + lane : 79;
                    const unsigned pa = part + ((row >> 4) * 64 + (row & 15)) * 8;
                    const f32x2 sm = (lds64f(pa) + lds64f(pa + 128)) + (lds64f(pa + 256) + lds64f(pa + 384));
                    *(LAS f32x2*)(fin + row * 8) = ln_stats(sm.x, sm.y);
                }
            }
        }
    }
    const rsrc_t rs_ws = mk_rsrc(wsp, WS_BYTES);
    const u64 epoch = gen + 1;
    const unsigned n3 = w & 3, m3 = w >> 2;
    const rsrc_t rs_g = mk_rsrc(P.wg, DM * FF * 2), rs_u = mk_rsrc(P.wu, DM * FF * 2);
    const rsrc_t rs_gu = mk_rsrc(m3 ? P.wu : P.wg, DM * FF * 2);
    unsigned vg0[3], vg1[3];
#pragma unroll
    for (int i = 0; i < 3; ++i) {
        const unsigned s = (n3 * 3 + i) * 64 + lane;
        const unsigned ks2 = s >= 384 ? 1 : 0, s2 = s - ks2 * 384, row = (s2 * 43691u) >> 19, pos = s2 - row * 12;
        const int umax = (int)(c < 15 ? 171 - 12 * c : 0);
        int u0 = (int)pos - (int)((row >> 3) & 1); if (u0 < 0) u0 += 12;
        int u1 = (int)pos - (int)(((row >> 3) & 1) + 2); if (u1 < 0) u1 += 12;
        const unsigned rb = (ks2 * 32 + row) * (FF * 2) + c * 192;
        vg0[i] = rb + min(u0, umax) * 16; vg1[i] = rb + min(u1, umax) * 16;
    }
    unsigned vgu[3], vgz[3];
#pragma unroll
    for (int i = 0; i < 3; ++i) {
        vgu[i] = m3 ? vg1[i] : vg0[i];
        const unsigned s = i * 64 + lane, row = (s * 43691u) >> 19, pos = s - row * 12;
        int u0 = (int)pos - (int)((row >> 3) & 1); if (u0 < 0) u0 += 12;
        vgz[i] = row * (FF * 2) + c * 192 + min(u0, (int)(c < 15 ? 171 - 12 * c : 0)) * 16;
    }
    unsigned mg0[3], mgu[3];
#pragma unroll
    for (int i = 0; i < 3; ++i) { const unsigned pi = n3 * 3 + i; mg0[i] = L3_B + (pi >= 6 ? L3_BSLOT + (pi - 6) * 1024 : pi * 1024); mgu[i] = mg0[i] + m3 * 6144; }
    const unsigned rot3 = (2 * (jj >> 3) + (jj & 7)) & 7;
#define ISSUE_B(pr) do { const unsigned sb_ = (((pr) + rot3) & 7) * (64 * FF * 2); _Pragma("unroll") for (int i_ = 0; i_ < 3; ++i_) DMA(mgu[i_], ((pr) & 3) * 2 * L3_BSLOT, vgu[i_], rs_gu, sb_, 0); } while (0)

    u32x4 h_keep;
    {
        WAIT_VM(0);
        BAR();
        if (tid == 0) __hip_atomic_store(flags + (FL_1 + c) * 32, epoch, __ATOMIC_RELAXED, __HIP_MEMORY_SCOPE_AGENT);
        const u32x4 xr = __builtin_amdgcn_raw_buffer_load_b128(rs_x, tok_off, 0, 0);
        const unsigned wl = (8 * g + q4) * 128 + (fsw * 32) + 8 * p4;
        const unsigned wb0 = wl + (kh ? 8192 : L2_WO), wb1 = wl + (kh ? L2_WO + 49152 : L2_WO + 16384), wb2 = wl + (kh ? L2_WO + 57344 : 0);
        unsigned abase = lane * 16;
        const unsigned asoff = WS_O + ((4 * hf + rt4) * 16 + 8 * kh) * 1024;
        PIN(abase);
        u32x4 bf[8][4];
#pragma unroll
        for (int k = 0; k < 8; ++k)
#pragma unroll
            for (int ct = 0; ct < 4; ++ct) bf[k][ct] = tr_frag(((k < 4 ? wb0 : (k < 6 ? wb1 : wb2)) ^ (ct * 32)) + (k < 4 ? k : (k < 6 ? k - 4 : k - 6)) * 4096, 512);
        __builtin_amdgcn_sched_barrier(0);
        if (w == 0) wait_flags(flags + (FL_1 + 8 * hf) * 32, 8, epoch, lane);
        BAR();
        u32x4 af[8];
#pragma unroll
        for (int k = 0; k < 4; ++k) af[k] = __builtin_amdgcn_raw_buffer_load_b128(rs_ws, abase + (k & 3) * 1024, asoff + (k >> 2) * 4096, AUX_SC1);
        __builtin_amdgcn_sched_barrier(0);
        f32x4 acc[4] = {Z4, Z4, Z4, Z4};
#pragma unroll
        for (int k = 0; k < 8; ++k) {
            if (k + 4 < 8) af[k + 4] = __builtin_amdgcn_raw_buffer_load_b128(rs_ws, abase + (k & 3) * 1024, asoff + 4096, AUX_SC1);
            __builtin_amdgcn_sched_barrier(0);
#pragma unroll
            for (int ct = 0; ct < 4; ++ct) acc[ct] = mfma32(bf[k][ct], af[k], acc[ct]);
            __builtin_amdgcn_sched_barrier(0);
        }
        const unsigned red = L2_RED + w * 2048 + lane * 16;
        *(LAS f32x4*)red = kh ? acc[0] : acc[2];
        *(LAS f32x4*)(red + 1024) = kh ? acc[1] : acc[3];
        BAR();
        const f32x4 a0 = (kh ? acc[2] : acc[0]) + lds128f(red ^ 8192);
        const f32x4 a1 = (kh ? acc[3] : acc[1]) + lds128f((red ^ 8192) + 1024);
        auto s0 = __builtin_amdgcn_permlane16_swap(pk_bf16(a0[0], a0[1]), pk_bf16(a1[0], a1[1]), false, false);
        auto s1 = __builtin_amdgcn_permlane16_swap(pk_bf16(a0[2], a0[3]), pk_bf16(a1[2], a1[3]), false, false);
        const u32x4 at = {s0[0], s1[0], s0[1], s1[1]};
#pragma unroll
        for (int i = 0; i < 4; ++i) h_keep[i] = pk_bf16(bf_lo(at[i]) + bf_lo(xr[i]), bf_hi(at[i]) + bf_hi(xr[i]));
        const unsigned hoff = ((2 * (g & 1) + (g >> 1)) * 16 + r16) * 16;
        const unsigned hsoff = WS_H + ((4 * hf + rt4) * 16 + 2 * hg + kh) * 1024;
        __builtin_amdgcn_raw_buffer_store_b128(h_keep, rs_ws, hoff + hsoff, 0, AUX_SC1);
        const f32x4 gs = mfma32(h_keep, h_keep, Z4), sm = mfma32(ONES, h_keep, Z4);
        const u32x2 stv = {f2u(sm[0]), f2u(diag4(gs, lane))};
        const unsigned stoff = (g == q4) ? WS_ST + c * 1024 + w * 128 + r16 * 8 : 0xFFFFFFF0u;
        __builtin_amdgcn_raw_buffer_store_b64(stv, rs_ws, stoff, 0, AUX_SC1);
        asm volatile("" ::: "memory");
    }
    const rsrc_t rs_wd = mk_rsrc(P.wd, FF * DM * 2);
    const unsigned vwd = l8 * 1024 + wsw + hg * 128;
#define KS4(s) (11 * ((s) / 10) + (s) % 10 + 1 - ((s) >= 30 ? 1 : 0))
    const unsigned swd = ((w >> 2) * 32 + (w & 3) * 8) * 1024;
#define ISSUE_D2(s0) DMA(wk, (s0) * 4096, vwd, rs_wd, swd, KS4(s0) * 32768)
    const unsigned swd0 = (w & 3) * 8 * 1024;
#define ISSUE_S() do { DMA(wk, 30 * 4096, vwd, rs_wd, swd0 + (kh ? 11 : 0) * 32768, 0); DMA(wk, 32 * 4096, vwd, rs_wd, swd0 + (kh ? 36 : 22) * 32768, 0); } while (0)
    {
        const bool active = c < 15;
        WAIT_VM(0);
        BAR();
        if (tid == 0) __hip_atomic_store(flags + (FL_2 + c) * 32, epoch, __ATOMIC_RELAXED, __HIP_MEMORY_SCOPE_AGENT);
        if (active && w != 0) {
            ISSUE_B(0); ISSUE_B(1);
            if (w == 1 || w == 2) {
                const unsigned sb_ = ((w - 1 + rot3) & 7) * (64 * FF * 2), mz = L3_B + (w - 1) * 2 * L3_BSLOT;
                DMA(mz, 0, vgz[0], rs_g, sb_, 0); DMA(mz, 1024, vgz[1], rs_g, sb_, 0); DMA(mz, 2048, vgz[2], rs_g, sb_, 0);
            }
        }
        unsigned aa = L3_A + lane * 16;
        unsigned ba[3];
#pragma unroll
        for (int i = 0; i < 3; ++i) {
            const unsigned mat = p4 >> 1;
            unsigned pos = 3 * n3 + i + (g & 1) + 2 * mat; if (pos >= 12) pos -= 12;
            ba[i] = L3_B + mat * 6144 + ((8 * g + q4) * 12 + pos) * 16 + 8 * (p4 & 1);
            PIN(ba[i]);
        }
        unsigned vl16 = lane * 16;
        const unsigned sa3 = WS_H + w * 16384;
        PIN(aa); PIN(vl16);
        if (w == 0) wait_flags(flags + FL_2 * 32, 16, epoch, lane);
        BAR();
        if (tid == 0 && c == 0) __hip_atomic_store(flags + FL_GEN * 32, epoch, __ATOMIC_RELAXED, __HIP_MEMORY_SCOPE_AGENT);
        if (active) {
#define ISSUE_A(pr) do { const unsigned sa_ = sa3 + (((pr) + rot3) & 7) * 2048; DMA_SC1(wk, L3_A + ((pr) % 3) * 16384, vl16, rs_ws, sa_, 0); DMA_SC1(wk, L3_A + ((pr) % 3) * 16384 + 8192, vl16, rs_ws, sa_, 1024); } while (0)
            ISSUE_A(0); ISSUE_A(1);
            WAIT_VM(2);
            BAR();
            if (w < 4) {
                f32x4 acc[8][3], cs[3] = {Z4, Z4, Z4};
#pragma unroll
                for (int t = 0; t < 8; ++t)
#pragma unroll
                    for (int i = 0; i < 3; ++i) acc[t][i] = Z4;
                u32x4 af[2][8], bf[2][3];
#define LOADM(s) do { const int pr_ = (s) >> 1, k2_ = (s) & 1; \
                    bf[k2_][0] = tr_frag(ba[0] + ((pr_ & 3) * 2 + k2_) * L3_BSLOT, 768); \
                    _Pragma("unroll") for (int t = 0; t < 8; ++t) af[k2_][t] = lds128(aa + (pr_ % 3) * 16384 + k2_ * 8192 + t * 1024); \
                    bf[k2_][1] = tr_frag(ba[1] + ((pr_ & 3) * 2 + k2_) * L3_BSLOT, 768); bf[k2_][2] = tr_frag(ba[2] + ((pr_ & 3) * 2 + k2_) * L3_BSLOT, 768); } while (0)
                LOADM(0);
#pragma unroll
                for (int s = 0; s < 16; ++s) {
                    if (s & 1) { if (s == 1) WAIT_VM(0); BAR(); }
                    if (s < 15) LOADM(s + 1);
#pragma unroll
                    for (int i = 0; i < 3; ++i) {
#pragma unroll
                        for (int t = 0; t < 8; ++t) acc[t][i] = mfma32(bf[s & 1][i], af[s & 1][t], acc[t][i]);
                        cs[i] = mfma32(bf[s & 1][i], ONES, cs[i]);
                    }
#pragma unroll
                    for (int i = 0; i < 14; ++i) { __builtin_amdgcn_sched_group_barrier(0x8, 1, 0); __builtin_amdgcn_sched_group_barrier(0x100, 1, 0); }
                    __builtin_amdgcn_sched_group_barrier(0x8, 13, 0);
                    __builtin_amdgcn_sched_barrier(0);
                }
                const unsigned hi = lane >> 5;
                const unsigned tau = 2 * (g & 1) + (g >> 1);
#pragma unroll
                for (int m = 0; m < 2; ++m) {
                    f32x2 st[4];
#pragma unroll
                    for (int t = 0; t < 4; ++t) st[t] = lds64f(L3_ST + 512 + m * 8192 + t * 128 + r16 * 8);
                    const float r01 = hi ? st[1].y : st[0].y, r23 = hi ? st[3].y : st[2].y;
                    const float rr01 = r01 * r01, rr23 = r23 * r23;
#pragma unroll
                    for (int i = 0; i < 3; ++i) {
                        const f32x4 cv = cs[i];
                        u32x2 hp[2];
#pragma unroll
                        for (int pp = 0; pp < 2; ++pp) {
                            const float m0_ = -st[2 * pp].x, m1_ = -st[2 * pp + 1].x;
                            const f32x4 e0 = __builtin_elementwise_fma(cv, (f32x4){m0_, m0_, m0_, m0_}, acc[4 * m + 2 * pp][i]);
                            const f32x4 e1 = __builtin_elementwise_fma(cv, (f32x4){m1_, m1_, m1_, m1_}, acc[4 * m + 2 * pp + 1][i]);
                            f32x4 gt, up;
#pragma unroll
                            for (int r = 0; r < 4; ++r) {
                                auto sw = __builtin_amdgcn_permlane32_swap(f2u(e0[r]), f2u(e1[r]), false, false);
                                const unsigned x0 = sw[0], x1 = sw[1];
                                gt[r] = u2f((unsigned)max((int)x0, 0)); up[r] = u2f(x1);
                            }
                            const f32x4 hv = gt * (up * (pp ? rr23 : rr01));
                            hp[pp] = (u32x2){pk_bf16(hv[0], hv[1]), pk_bf16(hv[2], hv[3])};
                        }
                        auto s0 = __builtin_amdgcn_permlane16_swap(hp[0].x, hp[1].x, false, false);
                        auto s1 = __builtin_amdgcn_permlane16_swap(hp[0].y, hp[1].y, false, false);
                        const u32x4 hf4 = {s0[0], s1[0], s0[1], s1[1]};
                        const unsigned U = 12 * c + 3 * n3 + i;
                        const unsigned soff = WS_HID + ((4 * m) * NKS4 + (U >> 2)) * 1024 + (U & 3) * 256;
                        const unsigned off = U < 172 ? tau * (NKS4 * 1024) + r16 * 16 + soff : 0xFFFFFFF0u;
                        __builtin_amdgcn_raw_buffer_store_b128(hf4, rs_ws, off, 0, AUX_SC1);
                    }
                }
                asm volatile("" ::: "memory");
            } else {
                const unsigned lw = w - 4, wk0 = lw * 1024, sa30 = WS_H + lw * 16384;
#define A4(pr) do { const unsigned sa_ = sa30 + (((pr) + rot3) & 7) * 2048; const int o_ = L3_A + ((pr) % 3) * 16384; \
                    DMA_SC1(wk0, o_, vl16, rs_ws, sa_, 0); DMA_SC1(wk0, o_ + 8192, vl16, rs_ws, sa_, 1024); DMA_SC1(wk0, o_ + 4096, vl16, rs_ws, sa_, 65536); DMA_SC1(wk0, o_ + 12288, vl16, rs_ws, sa_, 65536 + 1024); } while (0)
#define B6(pr) do { const unsigned sb_ = (((pr) + rot3) & 7) * (64 * FF * 2); const int o_ = ((pr) & 3) * 2 * L3_BSLOT; \
                    _Pragma("unroll") for (int i_ = 0; i_ < 3; ++i_) DMA(mg0[i_], o_, vg0[i_], rs_g, sb_, 0); \
                    _Pragma("unroll") for (int i_ = 0; i_ < 3; ++i_) DMA(mg0[i_], o_ + 6144, vg1[i_], rs_u, sb_, 0); } while (0)
#define D4(s0) do { DMA(wk0, (s0) * 4096, vwd, rs_wd, swd0, KS4(s0) * 32768); DMA(wk0, (s0) * 4096 + 4096, vwd, rs_wd, swd0, KS4(s0) * 32768 + 32768); } while (0)
                const unsigned sa = L3_ST + (lane & 3) * 2048 + (lw * 16 + (lane >> 2)) * 8;
#pragma unroll
                for (int s = 0; s < 16; ++s) {
                    if (s & 1) {
                        if (s == 1 || s == 3) WAIT_VM(20); else if (s < 13) WAIT_VM(16); else if (s == 13) WAIT_VM(12);
                        BAR();
                        const int po = s >> 1;
                        if (po + 3 < 8) A4(po + 3 < 8 ? po + 3 : 7);
                        if (po + 4 < 8) B6(po + 4 < 8 ? po + 4 : 7);
                        if (po >= 4 && po < 7) { D4(12 + 6 * (po >= 4 ? po - 4 : 0)); D4(14 + 6 * (po >= 4 ? po - 4 : 0)); D4(16 + 6 * (po >= 4 ? po - 4 : 0)); }
                        if (s == 15) {
                            DMA(wk0, 30 * 4096, vwd, rs_wd, swd0, 0); DMA(wk0, 31 * 4096, vwd, rs_wd, swd0, 11 * 32768);
                            DMA(wk0, 32 * 4096, vwd, rs_wd, swd0, 22 * 32768); DMA(wk0, 33 * 4096, vwd, rs_wd, swd0, 36 * 32768);
                            D4(0); D4(2); D4(4);
                        }
                    }
                    if (s == 0) {
                        B6(2); A4(2); B6(3);
                        const unsigned sst = WS_ST + lw * 2048;
                        DMA_SC1(wk0 + wk0, L3_ST, vl16, rs_ws, sst, 0); DMA_SC1(wk0 + wk0, L3_ST + 1024, vl16, rs_ws, sst, 1024);
                        DMA_SC1(wk0 + wk0, L3_ST + 8192, vl16, rs_ws, sst, 8192); DMA_SC1(wk0 + wk0, L3_ST + 8192 + 1024, vl16, rs_ws, sst, 8192 + 1024);
                    }
                    if (s == 5) {
#pragma unroll
                        for (int hfz = 0; hfz < 2; ++hfz) {
                            const unsigned a = sa + hfz * 8192;
                            const f32x2 sm = (lds64f(a) + lds64f(a + 512)) + (lds64f(a + 1024) + lds64f(a + 1536));
                            float sx = sm.x, sq = sm.y;
                            sx += __builtin_amdgcn_update_dpp(0.f, sx, 0xB1, 0xf, 0xf, false); sq += __builtin_amdgcn_update_dpp(0.f, sq, 0xB1, 0xf, 0xf, false);
                            sx += __builtin_amdgcn_update_dpp(0.f, sx, 0x4E, 0xf, 0xf, false); sq += __builtin_amdgcn_update_dpp(0.f, sq, 0x4E, 0xf, 0xf, false);
                            *(LAS f32x2*)((a & ~(unsigned)6144) + 512) = ln_stats(sx, sq);
                        }
                    }
                }
            }
        }
    }
    {
        const bool active = c < 15;
        if (!active) {
            ISSUE_D2(12); ISSUE_D2(14); ISSUE_D2(16); ISSUE_D2(18); ISSUE_D2(20); ISSUE_D2(22); ISSUE_D2(24); ISSUE_D2(26); ISSUE_D2(28);
            ISSUE_S();
            ISSUE_D2(0); ISSUE_D2(2); ISSUE_D2(4);
        }
        WAIT_VM(0);
        BAR();
        if (tid == 0 && active) __hip_atomic_store(flags + (FL_3 + c) * 32, epoch, __ATOMIC_RELAXED, __HIP_MEMORY_SCOPE_AGENT);
#define REST_D() do { ISSUE_D2(6); ISSUE_D2(8); ISSUE_D2(10); ISSUE_D2(34); ISSUE_D2(36); ISSUE_D2(38); } while (0)
        if (w != 0) REST_D();
        const unsigned wb0 = (8 * g + q4) * 128 + (fsw * 32) + 8 * p4;
        u32x4 bq[11][4];
#pragma unroll
        for (int ct = 0; ct < 4; ++ct) {
            const u32x4 t0 = tr_frag((wb0 ^ (ct * 32)) + (30 + (kq4 < 3 ? kq4 : 0)) * 4096, 512);
            bq[0][ct] = kq4 < 3 ? t0 : (u32x4){0u, 0u, 0u, 0u};
        }
        BAR();
#define LAST_D() do { ISSUE_D2(30); DMA(wk, 32 * 4096, vwd, rs_wd, swd, 35 * 32768); } while (0)
        if (w != 0) LAST_D();
        unsigned wbk = wb0 + kq4 * (10 * 4096);
        unsigned abase = lane * 16;
        const unsigned asoff = WS_HID + ((4 * hf + 2 * rp4) * NKS4 + 11 * kq4 - (kq4 == 3 ? 1 : 0)) * 1024;
        PIN(wbk); PIN(abase);
        if (w == 0) wait_flags(flags + FL_3 * 32, 15, epoch, lane);
        BAR();
        u32x4 af[2][11];
#define LOADA4(k) do { af[0][k] = __builtin_amdgcn_raw_buffer_load_b128(rs_ws, abase + ((k) & 3) * 1024, asoff + ((k) >> 2) * 4096, AUX_SC1); \
                       af[1][k] = __builtin_amdgcn_raw_buffer_load_b128(rs_ws, abase + ((k) & 3) * 1024, asoff + NKS4 * 1024 + ((k) >> 2) * 4096, AUX_SC1); } while (0)
        if (w == 0) { REST_D(); LAST_D(); }
        LOADA4(0); LOADA4(1); LOADA4(2);
        WAIT_VM(6);
        BAR();
#define LOAD4(j) do { _Pragma("unroll") for (int ct = 0; ct < 4; ++ct) bq[j][ct] = tr_frag((wbk ^ (ct * 32)) + ((j) - 1) * 4096, 512); } while (0)
        LOAD4(1); LOAD4(2);
        __builtin_amdgcn_sched_barrier(0);
        f32x4 acc[2][4] = {{Z4, Z4, Z4, Z4}, {Z4, Z4, Z4, Z4}};
#pragma unroll
        for (int j = 0; j < 11; ++j) {
            if (j + 3 < 11) { LOADA4(j + 3); LOAD4(j + 3); }
#pragma unroll
            for (int rl = 0; rl < 2; ++rl)
#pragma unroll
                for (int ct = 0; ct < 4; ++ct) acc[rl][ct] = mfma32(bq[j][ct], af[rl][j], acc[rl][ct]);
#pragma unroll
            for (int i = 0; i < 8; ++i) { __builtin_amdgcn_sched_group_barrier(0x8, 1, 0); __builtin_amdgcn_sched_group_barrier(0x100, 1, 0); }
            __builtin_amdgcn_sched_barrier(0);
        }
        BAR();
        const unsigned red = w * 8192 + lane * 16;
#pragma unroll
        for (int rl = 0; rl < 2; ++rl)
#pragma unroll
            for (int ct = 0; ct < 4; ++ct) *(LAS f32x4*)(red + (rl * 4 + ct) * 1024) = acc[rl][ct];
        BAR();
        f32x4 a0 = Z4, a1 = Z4;
#pragma unroll
        for (int o = 0; o < 4; ++o) {
            const unsigned ra = (2 * rp4 + (o & 1) + 4 * (o >> 1)) * 8192 + lane * 16 + ((rt4 & 1) * 4 + 2 * kh) * 1024;
            a0 += lds128f(ra); a1 += lds128f(ra + 1024);
        }
        auto s0 = __builtin_amdgcn_permlane16_swap(pk_bf16(a0[0], a0[1]), pk_bf16(a1[0], a1[1]), false, false);
        auto s1 = __builtin_amdgcn_permlane16_swap(pk_bf16(a0[2], a0[3]), pk_bf16(a1[2], a1[3]), false, false);
        const u32x4 at = {s0[0], s1[0], s0[1], s1[1]};
        u32x4 o;
#pragma unroll
        for (int i = 0; i < 4; ++i) o[i] = pk_bf16(bf_lo(at[i]) + bf_lo(h_keep[i]), bf_hi(at[i]) + bf_hi(h_keep[i]));
        *(u32x4*)((char*)P.out + tok_off) = o;
    }
}

}

extern "C" void kernel_launch(void* const* d_in, const int* in_sizes, int n_in, void* d_out, int out_size, void* d_ws, size_t ws_size, hipStream_t stream) {
    Params P;
    P.x = (const u16*)d_in[0]; P.wqkv = (const u16*)d_in[1]; P.wo = (const u16*)d_in[2];
    P.wg = (const u16*)d_in[4]; P.wu = (const u16*)d_in[5]; P.wd = (const u16*)d_in[6];
    P.out = (u16*)d_out; P.ws = (unsigned char*)d_ws;
    (void)hipFuncSetAttribute((const void*)fused_block, hipFuncAttributeMaxDynamicSharedMemorySize, LDS_BYTES);
    hipLaunchKernelGGL(fused_block, dim3(256), dim3(512), LDS_BYTES, stream, P);
}
```

```cpp
#include <hip/hip_runtime.h>
#include <hip/hip_bf16.h>
#include <stdint.h>
#pragma clang diagnostic ignored "-Wint-to-pointer-cast"

#define LAS __attribute__((address_space(3)))
#define GAS __attribute__((address_space(1)))
typedef unsigned short u16;
typedef unsigned long long u64;
typedef short s16x4 __attribute__((ext_vector_type(4)));
typedef __bf16 bf16x8 __attribute__((ext_vector_type(8)));
typedef __bf16 bf16x2 __attribute__((ext_vector_type(2)));
typedef float f32x4 __attribute__((ext_vector_type(4)));
typedef float f32x2 __attribute__((ext_vector_type(2)));
typedef unsigned u32x4 __attribute__((ext_vector_type(4)));
typedef unsigned u32x2 __attribute__((ext_vector_type(2)));
typedef GAS u64 gu64;
typedef __amdgpu_buffer_rsrc_t rsrc_t;

namespace {

constexpr int SEQ = 2048, DM = 512, FF = 1376;
constexpr int NKS4 = FF / 32;
constexpr unsigned WS_FLAG = 0;
constexpr unsigned WS_ST = 16384;
constexpr unsigned WS_O = 40960;
constexpr unsigned WS_H = WS_O + 131072;
constexpr unsigned WS_HID = WS_H + 131072;
constexpr unsigned WS_SPARE = WS_HID + 8 * 43 * 1024;
constexpr unsigned WS_BYTES = WS_SPARE + 8192;
constexpr int FL_GEN = 0, FL_1 = 8, FL_2 = 24, FL_3 = 40;
constexpr int AUX_SC1 = 16;
constexpr unsigned kSpinCap = 1u << 16;
constexpr unsigned CODE_KB = 29;

constexpr int LDS_BYTES = 163840;
constexpr int L1_X = 0, L1_H = 65536, L1_W = 81920, L1_WSLOT = 24576, L1_SCR = 155648;
constexpr int L2_WO = 81920, L2_RED = 114688;
constexpr int L3_A = 0, L3_B = 49152, L3_BSLOT = 12288, L3_ST = 147456;

#define WAIT_VM(n) asm volatile("s_waitcnt vmcnt(%0)" :: "i"(n) : "memory")
#define BAR() do { asm volatile("s_waitcnt lgkmcnt(0)" ::: "memory"); __builtin_amdgcn_s_barrier(); asm volatile("" ::: "memory"); } while (0)
#define PIN(x) asm volatile("" : "+v"(x))
#define DMA(m0b, ldsoff, voff, rs, sbase, soffimm) do { unsigned t_; asm volatile("s_add_u32 m0, %1, %2\n\ts_add_u32 %0, %5, %6\n\tbuffer_load_dwordx4 %3, %4, %0 offen lds" \
    : "=&s"(t_) : "s"(m0b), "i"(ldsoff), "v"(voff), "s"(rs), "s"(sbase), "i"(soffimm) : "scc", "memory"); } while (0)
#define DMA_SC1(m0b, ldsoff, voff, rs, sbase, soffimm) do { unsigned t_; asm volatile("s_add_u32 m0, %1, %2\n\ts_add_u32 %0, %5, %6\n\tbuffer_load_dwordx4 %3, %4, %0 offen sc1 lds" \
    : "=&s"(t_) : "s"(m0b), "i"(ldsoff), "v"(voff), "s"(rs), "s"(sbase), "i"(soffimm) : "scc", "memory"); } while (0)

__device__ __forceinline__ rsrc_t mk_rsrc(const void* p, unsigned bytes) { return __builtin_amdgcn_make_buffer_rsrc((void*)p, 0, bytes, 0x00020000); }
__device__ __forceinline__ float u2f(unsigned u) { return __builtin_bit_cast(float, u); }
__device__ __forceinline__ unsigned f2u(float f) { return __builtin_bit_cast(unsigned, f); }
__device__ __forceinline__ unsigned pk_bf16(float a, float b) { bf16x2 v; v[0] = (__bf16)a; v[1] = (__bf16)b; return __builtin_bit_cast(unsigned, v); }
__device__ __forceinline__ float bf_lo(unsigned u) { return u2f(u << 16); }
__device__ __forceinline__ float bf_hi(unsigned u) { return u2f(u & 0xffff0000u); }
__device__ __forceinline__ float rbf(float f) { return (float)(__bf16)f; }
__device__ __forceinline__ f32x4 mfma32(u32x4 a, u32x4 b, f32x4 c) {
    return __builtin_amdgcn_mfma_f32_16x16x32_bf16(__builtin_bit_cast(bf16x8, a), __builtin_bit_cast(bf16x8, b), c, 0, 0, 0);
}
__device__ __forceinline__ f32x4 mfma16(u32x2 a, u32x2 b, f32x4 c) {
    return __builtin_amdgcn_mfma_f32_16x16x16bf16_1k(__builtin_bit_cast(s16x4, a), __builtin_bit_cast(s16x4, b), c, 0, 0, 0);
}
__device__ __forceinline__ u32x4 lds128(unsigned a) { return *(LAS u32x4*)a; }
__device__ __forceinline__ f32x4 lds128f(unsigned a) { return *(LAS f32x4*)a; }
__device__ __forceinline__ f32x2 lds64f(unsigned a) { return *(LAS f32x2*)a; }
__device__ __forceinline__ u32x2 tr_read(unsigned a) { return __builtin_bit_cast(u32x2, __builtin_amdgcn_ds_read_tr16_b64_v4i16((LAS s16x4*)a)); }
__device__ __forceinline__ u32x4 tr_frag(unsigned a, int step) { const u32x2 x = tr_read(a), y = tr_read(a + step); return (u32x4){x.x, x.y, y.x, y.y}; }
__device__ __forceinline__ float swap16_lo(float v) {
    const unsigned a = f2u(v); auto r = __builtin_amdgcn_permlane16_swap(a, a, false, false); const unsigned x = r[0]; return u2f(x);
}
__device__ __forceinline__ float max2(float a, float b) { return __builtin_amdgcn_fmed3f(a, b, __builtin_inff()); }
__device__ __forceinline__ float xmax16_32(float v) {
    unsigned a = f2u(v);
    auto r = __builtin_amdgcn_permlane16_swap(a, a, false, false);
    const unsigned r0 = r[0], r1 = r[1];
    v = max2(u2f(r0), u2f(r1));
    a = f2u(v);
    auto s = __builtin_amdgcn_permlane32_swap(a, a, false, false);
    const unsigned s0 = s[0], s1 = s[1];
    return max2(u2f(s0), u2f(s1));
}
__device__ __forceinline__ float bcast_row0(float v) { return __builtin_amdgcn_update_dpp(0.f, v, 0x150, 0xf, 0xf, false); }
__device__ __forceinline__ float diag4(f32x4 d, unsigned lane) { const float a = (lane & 1) ? d[1] : d[0], b = (lane & 1) ? d[3] : d[2]; return (lane & 2) ? b : a; }
__device__ __forceinline__ f32x2 ln_stats(float s, float q) {
    const float mean = s * (1.0f / DM);
    const float var = __builtin_fmaxf(q * (1.0f / DM) - mean * mean, 0.0f);
    const float sd = rbf(__builtin_amdgcn_sqrtf(rbf(rbf(var) + 1e-5f)));
    return (f32x2){rbf(mean), __builtin_amdgcn_rcpf(sd)};
}
__device__ __forceinline__ void wait_flags(gu64* base, unsigned n, u64 epoch, unsigned lane) {
    for (unsigned spins = 0; spins < kSpinCap; ++spins) {
        u64 v = epoch;
        if (lane < n) v = __hip_atomic_load(base + lane * 32, __ATOMIC_RELAXED, __HIP_MEMORY_SCOPE_AGENT);
        if (__all(v == epoch)) break;
    }
    __builtin_amdgcn_fence(__ATOMIC_ACQUIRE, "wavefront");
}

struct Params { const u16* x; const u16* wqkv; const u16* wo; const u16* wg; const u16* wu; const u16* wd; u16* out; unsigned char* ws; };

__global__ void __launch_bounds__(512, 2) __attribute__((amdgpu_waves_per_eu(2, 2))) fused_block(Params P) {
    const unsigned tid = threadIdx.x, lane = tid & 63;
    const unsigned w = __builtin_amdgcn_readfirstlane(tid >> 6);
    const unsigned b = blockIdx.x, xcd = b & 7, jj = b >> 3;
    const unsigned p = 4 * (xcd >> 1) + (jj >> 3);
    const unsigned hf = (jj >> 2) & 1, hg = 4 * (xcd & 1) + (jj & 3);
    const unsigned c = 8 * hf + hg;
    const unsigned r16 = lane & 15, g = lane >> 4, l8 = lane >> 3, l7 = lane & 7, q4 = r16 >> 2, p4 = lane & 3;
    {
        u64 pc; asm volatile("s_getpc_b64 %0" : "=s"(pc));
        const char* cp = (const char*)(pc & ~(u64)1023) + ((jj * 8 + w) % CODE_KB) * 1024 + lane * 16;
        asm volatile("s_mov_b32 m0, %0\n\ts_nop 0\n\tglobal_load_lds_dwordx4 %1, off" :: "s"(L1_SCR + 6144), "v"(cp) : "memory");
    }
    const unsigned wk = w * 1024;
    const rsrc_t rs_x = mk_rsrc(P.x, SEQ * DM * 2);
    const rsrc_t rs_w = mk_rsrc(P.wqkv, DM * 3 * DM * 2);
    unsigned char* const wsp = P.ws + (size_t)p * WS_BYTES;
    gu64* const flags = (gu64*)(wsp + WS_FLAG);
    const unsigned T0 = 128 * p + 64 * hf;
    u32x4 ONES = {0x3F803F80u, 0x3F803F80u, 0x3F803F80u, 0x3F803F80u};
    PIN(ONES);
    const f32x4 Z4 = {0.f, 0.f, 0.f, 0.f};
    const unsigned fsw = ((q4 >> 1) & 1) | ((g & 1) << 1);
    const unsigned wsw = (l7 ^ (((g & 1) | ((w & 1) << 1)) << 1)) * 16;
    u64 gen = 0;
    const unsigned rt4 = w & 3, kh = w >> 2, rp4 = rt4 >> 1, kq4 = (rt4 & 1) + 2 * kh;
    const unsigned tok_off = (128 * p + 64 * hf + 32 * (rt4 & 1) + (rt4 >> 1) + 2 * r16) * 1024 + (64 * hg + 32 * kh + 16 * (g & 1) + 8 * (g >> 1)) * 2;

    {
        const unsigned rot = (2 * (jj & 3) + (jj >> 2) + (xcd >> 1)) & 7;
        const unsigned hh = w & 3;
        const unsigned vh = l8 * 2048 + ((l7 ^ g) * 16);
        const unsigned vxr = vh ^ ((w & 1) * 64);
        const unsigned sxr = (T0 + 32 * ((w >> 1) & 1) + (w >> 2) + 16 * (w & 1)) * 1024;
        const unsigned vw = (8 * w + l8) * 3072 + wsw + hg * 128;
        {
            const unsigned kap = (w + rot) & 7;
            const unsigned sh = (T0 == 0 ? 0 : T0 - 16) * 1024 + kap * 128;
            const unsigned hb = L1_H + w * 2048;
            if (lane >= 8) { DMA(hb, 0, vh, rs_x, sh, 0); const unsigned vh1 = vh ^ 64; DMA(hb, 1024, vh1, rs_x, sh, 1024); }
            else { *(LAS u32x4*)(hb + lane * 16) = ONES; *(LAS u32x4*)(hb + 1024 + lane * 16) = ONES; }
        }
        unsigned kx[8];
#pragma unroll
        for (int q = 0; q < 8; ++q) kx[q] = (q + rot) & 7;
#define TOUCH(ptr) asm volatile("s_mov_b32 m0, %0\n\ts_nop 0\n\tglobal_load_lds_dwordx4 %1, off" :: "s"(L1_SCR + 6144), "v"(ptr) : "memory")
#define ISSUE_X(q) DMA(wk, L1_X + (q) * 8192, vxr, rs_x, sxr + kx[q] * 128, 0)
#define ISSUE_W1(q, mat) DMA(wk, L1_W + ((q) % 3) * L1_WSLOT + (mat) * 8192, vw, rs_w, kx[q] * 196608, (mat) * 1024)
#define ISSUE_W(q) do { ISSUE_W1(q, 0); ISSUE_W1(q, 1); ISSUE_W1(q, 2); } while (0)
        ISSUE_X(0); ISSUE_W(0);
        __builtin_amdgcn_s_barrier();
        ISSUE_X(1); ISSUE_W(1);
        { const char* gp = (const char*)wsp + WS_FLAG + lane * 16; asm volatile("s_mov_b32 m0, %0\n\ts_nop 0\n\tglobal_load_lds_dwordx4 %1, off sc1" :: "s"(L1_SCR + 7168), "v"(gp) : "memory"); }
#define READ_GEN() do { const u32x2 gv_ = *(LAS u32x2*)(L1_SCR + 7168 + FL_GEN * 256); gen = ((u64)(unsigned)__builtin_amdgcn_readfirstlane(gv_.y) << 32) | (unsigned)__builtin_amdgcn_readfirstlane(gv_.x); } while (0)
#define NTOUCH 4
        const unsigned part = L1_SCR, fin = L1_SCR + 2560;
        const unsigned xa = (r16 >> 3) * 1024 + (r16 & 7) * 128 + ((g ^ ((r16 >> 1) & 7)) * 16);
        const rsrc_t rs_wo = mk_rsrc(P.wo, DM * DM * 2);
        WAIT_VM(5);
        BAR();
        if (w < 4) {
            unsigned xc0 = xa, xc1 = xa ^ 64, xh0 = xa + L1_H, xh1 = xh0 ^ 64;
            unsigned wa = L1_W + (8 * g + q4) * 128 + ((hh ^ fsw) * 32) + 8 * p4, wa2 = wa + 2 * L1_WSLOT;
            PIN(xc0); PIN(xc1); PIN(xh0); PIN(xh1); PIN(wa); PIN(wa2);
            f32x4 aq[5], ak[5], av[5];
#pragma unroll
            for (int t = 0; t < 5; ++t) { aq[t] = Z4; ak[t] = Z4; av[t] = Z4; }
            u32x4 xf[2][5], wf[2][3];
#define LOADM1(s) do { const int kq_ = (s) >> 1, kk_ = (s) & 1; const unsigned wsl_ = (kq_ % 3 == 2 ? wa2 : wa + (kq_ % 3) * L1_WSLOT) + kk_ * 4096; \
                wf[kk_][0] = tr_frag(wsl_, 512); xf[kk_][0] = lds128((kk_ ? xh1 : xh0) + kq_ * 2048); \
                _Pragma("unroll") for (int t = 0; t < 4; ++t) xf[kk_][1 + t] = lds128((kk_ ? xc1 : xc0) + kq_ * 8192 + t * 2048); \
                wf[kk_][1] = tr_frag(wsl_ + 8192, 512); wf[kk_][2] = tr_frag(wsl_ + 16384, 512); } while (0)
            LOADM1(0);
#pragma unroll
            for (int s = 0; s < 16; ++s) {
                if ((s & 1) && s < 15) { if (s == 1) WAIT_VM(1); if (s == 9) WAIT_VM(0); BAR(); }
                if (s == 9) READ_GEN();
                if (s < 15) LOADM1(s + 1);
#pragma unroll
                for (int t = 0; t < 5; ++t) aq[t] = mfma32(wf[s & 1][0], xf[s & 1][t], aq[t]);
#pragma unroll
                for (int t = 0; t < 5; ++t) ak[t] = mfma32(wf[s & 1][1], xf[s & 1][t], ak[t]);
#pragma unroll
                for (int t = 0; t < 5; ++t) av[t] = mfma32(xf[s & 1][t], wf[s & 1][2], av[t]);
#pragma unroll
                for (int i = 0; i < 11; ++i) { __builtin_amdgcn_sched_group_barrier(0x8, 1, 0); __builtin_amdgcn_sched_group_barrier(0x100, 1, 0); }
                __builtin_amdgcn_sched_group_barrier(0x8, 4, 0);
                __builtin_amdgcn_sched_barrier(0);
            }
            f32x4 csq, csk;
#pragma unroll
            for (int r = 0; r < 4; ++r) { csq[r] = bcast_row0(aq[0][r]); csk[r] = bcast_row0(ak[0][r]); }
            const float csv = swap16_lo(av[0][0]);
            u32x2 qb[5], kb[5], vb[5];
#pragma unroll
            for (int t = 0; t < 5; ++t) {
                const f32x2 sl = lds64f(fin + (t * 16 + r16) * 8);
                const f32x4 s01 = lds128f(fin + (t * 16 + 4 * g) * 8), s23 = lds128f(fin + (t * 16 + 4 * g) * 8 + 16);
                const f32x4 nm = {-sl.x, -sl.x, -sl.x, -sl.x};
                const f32x4 kv = __builtin_elementwise_fma(csk, nm, ak[t]) * sl.y;
                kb[t] = (u32x2){pk_bf16(kv[0], kv[1]), pk_bf16(kv[2], kv[3])};
                if (t > 0) { const f32x4 qv = __builtin_elementwise_fma(csq, nm, aq[t]) * sl.y; qb[t] = (u32x2){pk_bf16(qv[0], qv[1]), pk_bf16(qv[2], qv[3])}; }
                const f32x4 mn = {s01[0], s01[2], s23[0], s23[2]}, rd = {s01[1], s01[3], s23[1], s23[3]};
                const f32x4 vv = __builtin_elementwise_fma(mn, (f32x4){-csv, -csv, -csv, -csv}, av[t]) * rd;
                vb[t] = (u32x2){pk_bf16(vv[0], vv[1]), pk_bf16(vv[2], vv[3])};
            }
            f32x4 mh[2], mp, mc;
#pragma unroll
            for (int r = 0; r < 4; ++r) {
                const unsigned kap = 4 * g + r;
                mh[0][r] = (T0 > 0 && kap < 8 && kap >= r16 + 1) ? 0.f : -1e30f;
                mh[1][r] = (T0 > 0 && kap >= 8 && (kap & 7) >= r16 + 1) ? 0.f : -1e30f;
                mp[r] = (kap >= r16 + 9) ? 0.f : -1e30f;
                mc[r] = (kap <= r16 && r16 - kap <= 7) ? 0.f : -1e30f;
            }
            const float SC = 0.25f * 1.44269504088896f;
            const rsrc_t rs_ws1 = mk_rsrc(wsp, WS_BYTES);
#pragma unroll
            for (int pz = 0; pz < 2; ++pz) {
                u32x2 ob[2];
#pragma unroll
                for (int a = 0; a < 2; ++a) {
                    const int cur = 1 + 2 * pz + a, prev = a == 0 ? 0 : cur - 1;
                    f32x4 sp = mfma16(kb[prev], qb[cur], a == 0 ? mh[pz] : mp), sc = mfma16(kb[cur], qb[cur], mc);
                    float mx = max2(max2(max2(sp[0], sp[1]), max2(sp[2], sp[3])), max2(max2(sc[0], sc[1]), max2(sc[2], sc[3])));
                    mx = xmax16_32(mx);
                    const float mb = -mx * SC;
#pragma unroll
                    for (int r = 0; r < 4; ++r) { sp[r] = __builtin_amdgcn_exp2f(sp[r] * SC + mb); sc[r] = __builtin_amdgcn_exp2f(sc[r] * SC + mb); }
                    const u32x4 pf = {pk_bf16(sp[0], sp[1]), pk_bf16(sp[2], sp[3]), pk_bf16(sc[0], sc[1]), pk_bf16(sc[2], sc[3])};
                    const u32x4 vf = {vb[prev].x, vb[prev].y, vb[cur].x, vb[cur].y};
                    const f32x4 sm = mfma32(ONES, pf, Z4);
                    const f32x4 o = mfma32(vf, pf, Z4) * __builtin_amdgcn_rcpf(sm[0]);
                    ob[a] = (u32x2){pk_bf16(o[0], o[1]), pk_bf16(o[2], o[3])};
                }
                auto s0 = __builtin_amdgcn_permlane16_swap(ob[0].x, ob[1].x, false, false);
                auto s1 = __builtin_amdgcn_permlane16_swap(ob[0].y, ob[1].y, false, false);
                const u32x4 o_frag = {s0[0], s1[0], s0[1], s1[1]};
                const unsigned off = ((g & 1) * 16384) + ((2 * (hh & 1) + (g >> 1)) * 16 + r16) * 16;
                const unsigned soff = WS_O + ((4 * hf + 2 * pz) * 16 + 2 * hg + (hh >> 1)) * 1024;
                __builtin_amdgcn_raw_buffer_store_b128(o_frag, rs_ws1, off + soff, 0, AUX_SC1);
            }
            asm volatile("" ::: "memory");
        } else {
            const unsigned lw = w - 4, wk0 = lw * 1024;
            const unsigned sx0 = (T0 + 32 * ((lw >> 1) & 1) + 16 * (lw & 1)) * 1024;
            const unsigned vw0 = (8 * lw + l8) * 3072 + wsw + hg * 128, vwo0 = (8 * lw + l8) * 1024 + wsw + hg * 128;
#define X2(q) do { DMA(wk0, L1_X + (q) * 8192, vxr, rs_x, sx0 + kx[q] * 128, 0); DMA(wk0, L1_X + (q) * 8192 + 4096, vxr, rs_x, sx0 + kx[q] * 128, 1024); } while (0)
#define W6(q) do { _Pragma("unroll") for (int mat_ = 0; mat_ < 3; ++mat_) { DMA(wk0, L1_W + ((q) % 3) * L1_WSLOT + mat_ * 8192, vw0, rs_w, kx[q] * 196608, mat_ * 1024); \
                                                                            DMA(wk0, L1_W + ((q) % 3) * L1_WSLOT + mat_ * 8192 + 4096, vw0, rs_w, kx[q] * 196608, mat_ * 1024 + 98304); } } while (0)
#define O2(lds, m) do { DMA(wk0, (lds), vwo0, rs_wo, 0u, (m) * 65536); DMA(wk0, (lds) + 4096, vwo0, rs_wo, 0u, (m) * 65536 + 32768); } while (0)
            const unsigned sxa = xa ^ ((lw & 1) * 64);
            const unsigned stc = sxa + (lw >> 1) * 8192, sth = sxa + L1_H + (lw >> 1) * 2048;
            f32x4 gr[5], rs[5];
#pragma unroll
            for (int t = 0; t < 5; ++t) { gr[t] = Z4; rs[t] = Z4; }
#pragma unroll
            for (int s = 0; s < 16; ++s) {
                if ((s & 1) && s < 15) {
                    if (s == 1) WAIT_VM(9); else if (s == 3) WAIT_VM(12); else if (s == 5) WAIT_VM(14 + 2 * NTOUCH); else if (s == 7) WAIT_VM(10 + 2 * NTOUCH); else if (s < 13) WAIT_VM(6); else WAIT_VM(10);
                    BAR();
                    const int it = (s + 1) / 2;
                    if (it <= 5) W6(it + 2 > 7 ? 7 : it + 2);
                    if (it == 1) { X2(3); X2(4); X2(5); }
                    if (it == 2) { X2(6); X2(7); }
                    if (it == 6) { O2(L2_WO + 6 * 8192, 6); O2(L2_WO + 7 * 8192, 7); O2(0, 3); O2(8192, 4); O2(16384, 5); }
                    if (it == 7) { O2(L2_WO, 0); O2(L2_WO + 8192, 1); O2(L2_WO + 2 * 8192, 2); }
                    if (s == 9) READ_GEN();
                }
                if (s == 0) { X2(2); W6(2); }
                if (s == 3) {
                    const char* t1 = lane < 22 ? (const char*)P.wg + lane * 65536 : (lane < 44 ? (const char*)P.wu + (lane - 22) * 65536 : (const char*)P.wd + (lane - 44) * 65536);
                    const char* t2 = lane < 8 ? (const char*)P.wo + lane * 65536 : (lane < 32 ? (const char*)P.out + p * 131072 + (lane - 8) * 4096 : (const char*)P.wd + (20 + (lane & 1)) * 65536);
                    const char* tw = lw == 0 ? (const char*)wsp + WS_FLAG + lane * 256 : (lw == 1 ? t1 : (lw == 2 ? t2 : (const char*)wsp + WS_SPARE + lane * 16));
                    asm volatile("s_mov_b32 m0, %0\n\ts_nop 0\n\tglobal_load_lds_dwordx4 %1, off sc1" :: "s"(L1_SCR + 6144), "v"(tw) : "memory");
                    const char* tz = (const char*)wsp + 32768u + lane * 16;
                    TOUCH(tz);
#pragma unroll
                    for (int i = 0; i < 6; ++i) {
                        unsigned qq = b * 24 + lw * 6 + i; if (qq >= 4640) qq -= 4640;
                        const char* base = qq < 512 ? (const char*)P.wo + qq * 1024 : (qq < 1888 ? (const char*)P.wg + (qq - 512) * 1024 : (qq < 3264 ? (const char*)P.wu + (qq - 1888) * 1024 : (const char*)P.wd + (qq - 3264) * 1024));
                        const char* t3 = base + lane * 16;
                        TOUCH(t3);
                    }
                }
                if (s == 5 || s == 6 || s == 7 || s == 9) {
                    const int j = s == 5 ? 0 : (s == 6 ? 1 : (s == 7 ? 2 : 3));
                    u32x4 sf[5];
                    sf[0] = lds128(sth + j * 4096);
#pragma unroll
                    for (int t = 0; t < 4; ++t) sf[1 + t] = lds128(stc + j * 16384 + t * 2048);
#pragma unroll
                    for (int t = 0; t < 5; ++t) { gr[t] = mfma32(sf[t], sf[t], gr[t]); rs[t] = mfma32(ONES, sf[t], rs[t]); }
                }
                if (s == 10 && g == q4) {
#pragma unroll
                    for (int t = 0; t < 5; ++t) *(LAS f32x2*)(part + ((t * 4 + lw) * 16 + r16) * 8) = (f32x2){rs[t][0], diag4(gr[t], lane)};
                }
                if (s == 11) {
                    const unsigned row = lw * 64 + lane < 80 ? lw * 64 + lane : 79;
                    const unsigned pa = part + ((row >> 4) * 64 + (row & 15)) * 8;
                    const f32x2 sm = (lds64f(pa) + lds64f(pa + 128)) + (lds64f(pa + 256) + lds64f(pa + 384));
                    *(LAS f32x2*)(fin + row * 8) = ln_stats(sm.x, sm.y);
                }
            }
        }
    }
    const rsrc_t rs_ws = mk_rsrc(wsp, WS_BYTES);
    const u64 epoch = gen + 1;
    const unsigned n3 = w & 3, m3 = w >> 2;
    const rsrc_t rs_g = mk_rsrc(P.wg, DM * FF * 2), rs_u = mk_rsrc(P.wu, DM * FF * 2);
    const rsrc_t rs_gu = mk_rsrc(m3 ? P.wu : P.wg, DM * FF * 2);
    unsigned vg0[3], vg1[3];
#pragma unroll
    for (int i = 0; i < 3; ++i) {
        const unsigned s = (n3 * 3 + i) * 64 + lane;
        const unsigned ks2 = s >= 384 ? 1 : 0, s2 = s - ks2 * 384, row = (s2 * 43691u) >> 19, pos = s2 - row * 12;
        const int umax = (int)(c < 15 ? 171 - 12 * c : 0);
        int u0 = (int)pos - (int)((row >> 3) & 1); if (u0 < 0) u0 += 12;
        int u1 = (int)pos - (int)(((row >> 3) & 1) + 2); if (u1 < 0) u1 += 12;
        const unsigned rb = (ks2 * 32 + row) * (FF * 2) + c * 192;
        vg0[i] = rb + min(u0, umax) * 16; vg1[i] = rb + min(u1, umax) * 16;
    }
    unsigned vgu[3], vgz[3];
#pragma unroll
    for (int i = 0; i < 3; ++i) {
        vgu[i] = m3 ? vg1[i] : vg0[i];
        const unsigned s = i * 64 + lane, row = (s * 43691u) >> 19, pos = s - row * 12;
        int u0 = (int)pos - (int)((row >> 3) & 1); if (u0 < 0) u0 += 12;
        vgz[i] = row * (FF * 2) + c * 192 + min(u0, (int)(c < 15 ? 171 - 12 * c : 0)) * 16;
    }
    unsigned mg0[3], mgu[3];
#pragma unroll
    for (int i = 0; i < 3; ++i) { const unsigned pi = n3 * 3 + i; mg0[i] = L3_B + (pi >= 6 ? L3_BSLOT + (pi - 6) * 1024 : pi * 1024); mgu[i] = mg0[i] + m3 * 6144; }
    const unsigned rot3 = (2 * (jj >> 3) + (jj & 7)) & 7;
#define ISSUE_B(pr) do { const unsigned sb_ = (((pr) + rot3) & 7) * (64 * FF * 2); _Pragma("unroll") for (int i_ = 0; i_ < 3; ++i_) DMA(mgu[i_], ((pr) & 3) * 2 * L3_BSLOT, vgu[i_], rs_gu, sb_, 0); } while (0)

    u32x4 h_keep;
    {
        WAIT_VM(0);
        BAR();
        if (tid == 0) __hip_atomic_store(flags + (FL_1 + c) * 32, epoch, __ATOMIC_RELAXED, __HIP_MEMORY_SCOPE_AGENT);
        const u32x4 xr = __builtin_amdgcn_raw_buffer_load_b128(rs_x, tok_off, 0, 0);
        const unsigned wl = (8 * g + q4) * 128 + (fsw * 32) + 8 * p4;
        const unsigned wb0 = wl + (kh ? 8192 : L2_WO), wb1 = wl + (kh ? L2_WO + 49152 : L2_WO + 16384), wb2 = wl + (kh ? L2_WO + 57344 : 0);
        unsigned abase = lane * 16;
        const unsigned asoff = WS_O + ((4 * hf + rt4) * 16 + 8 * kh) * 1024;
        PIN(abase);
        u32x4 bf[8][4];
#pragma unroll
        for (int k = 0; k < 8; ++k)
#pragma unroll
            for (int ct = 0; ct < 4; ++ct) bf[k][ct] = tr_frag(((k < 4 ? wb0 : (k < 6 ? wb1 : wb2)) ^ (ct * 32)) + (k < 4 ? k : (k < 6 ? k - 4 : k - 6)) * 4096, 512);
        __builtin_amdgcn_sched_barrier(0);
        if (w == 0) wait_flags(flags + (FL_1 + 8 * hf) * 32, 8, epoch, lane);
        BAR();
        u32x4 af[8];
#pragma unroll
        for (int k = 0; k < 4; ++k) af[k] = __builtin_amdgcn_raw_buffer_load_b128(rs_ws, abase + (k & 3) * 1024, asoff + (k >> 2) * 4096, AUX_SC1);
        __builtin_amdgcn_sched_barrier(0);
        f32x4 acc[4] = {Z4, Z4, Z4, Z4};
#pragma unroll
        for (int k = 0; k < 8; ++k) {
            if (k + 4 < 8) af[k + 4] = __builtin_amdgcn_raw_buffer_load_b128(rs_ws, abase + (k & 3) * 1024, asoff + 4096, AUX_SC1);
            __builtin_amdgcn_sched_barrier(0);
#pragma unroll
            for (int ct = 0; ct < 4; ++ct) acc[ct] = mfma32(bf[k][ct], af[k], acc[ct]);
            __builtin_amdgcn_sched_barrier(0);
        }
        const unsigned red = L2_RED + w * 2048 + lane * 16;
        *(LAS f32x4*)red = kh ? acc[0] : acc[2];
        *(LAS f32x4*)(red + 1024) = kh ? acc[1] : acc[3];
        BAR();
        const f32x4 a0 = (kh ? acc[2] : acc[0]) + lds128f(red ^ 8192);
        const f32x4 a1 = (kh ? acc[3] : acc[1]) + lds128f((red ^ 8192) + 1024);
        auto s0 = __builtin_amdgcn_permlane16_swap(pk_bf16(a0[0], a0[1]), pk_bf16(a1[0], a1[1]), false, false);
        auto s1 = __builtin_amdgcn_permlane16_swap(pk_bf16(a0[2], a0[3]), pk_bf16(a1[2], a1[3]), false, false);
        const u32x4 at = {s0[0], s1[0], s0[1], s1[1]};
#pragma unroll
        for (int i = 0; i < 4; ++i) h_keep[i] = pk_bf16(bf_lo(at[i]) + bf_lo(xr[i]), bf_hi(at[i]) + bf_hi(xr[i]));
        const unsigned hoff = ((2 * (g & 1) + (g >> 1)) * 16 + r16) * 16;
        const unsigned hsoff = WS_H + ((4 * hf + rt4) * 16 + 2 * hg + kh) * 1024;
        __builtin_amdgcn_raw_buffer_store_b128(h_keep, rs_ws, hoff + hsoff, 0, AUX_SC1);
        const f32x4 gs = mfma32(h_keep, h_keep, Z4), sm = mfma32(ONES, h_keep, Z4);
        const u32x2 stv = {f2u(sm[0]), f2u(diag4(gs, lane))};
        const unsigned stoff = (g == q4) ? WS_ST + c * 1024 + w * 128 + r16 * 8 : 0xFFFFFFF0u;
        __builtin_amdgcn_raw_buffer_store_b64(stv, rs_ws, stoff, 0, AUX_SC1);
        asm volatile("" ::: "memory");
    }
    const rsrc_t rs_wd = mk_rsrc(P.wd, FF * DM * 2);
    const unsigned vwd = l8 * 1024 + wsw + hg * 128;
#define KS4(s) (11 * ((s) / 10) + (s) % 10 + 1 - ((s) >= 30 ? 1 : 0))
    const unsigned swd = ((w >> 2) * 32 + (w & 3) * 8) * 1024;
#define ISSUE_D2(s0) DMA(wk, (s0) * 4096, vwd, rs_wd, swd, KS4(s0) * 32768)
    const unsigned swd0 = (w & 3) * 8 * 1024;
#define ISSUE_S() do { DMA(wk, 30 * 4096, vwd, rs_wd, swd0 + (kh ? 11 : 0) * 32768, 0); DMA(wk, 32 * 4096, vwd, rs_wd, swd0 + (kh ? 36 : 22) * 32768, 0); } while (0)
    {
        const bool active = c < 15;
        WAIT_VM(0);
        BAR();
        if (tid == 0) __hip_atomic_store(flags + (FL_2 + c) * 32, epoch, __ATOMIC_RELAXED, __HIP_MEMORY_SCOPE_AGENT);
        if (active && w != 0) {
            ISSUE_B(0); ISSUE_B(1);
            if (w == 1 || w == 2) {
                const unsigned sb_ = ((w - 1 + rot3) & 7) * (64 * FF * 2), mz = L3_B + (w - 1) * 2 * L3_BSLOT;
                DMA(mz, 0, vgz[0], rs_g, sb_, 0); DMA(mz, 1024, vgz[1], rs_g, sb_, 0); DMA(mz, 2048, vgz[2], rs_g, sb_, 0);
            }
        }
        unsigned aa = L3_A + lane * 16;
        unsigned ba[3];
#pragma unroll
        for (int i = 0; i < 3; ++i) {
            const unsigned mat = p4 >> 1;
            unsigned pos = 3 * n3 + i + (g & 1) + 2 * mat; if (pos >= 12) pos -= 12;
            ba[i] = L3_B + mat * 6144 + ((8 * g + q4) * 12 + pos) * 16 + 8 * (p4 & 1);
            PIN(ba[i]);
        }
        unsigned vl16 = lane * 16;
        const unsigned sa3 = WS_H + w * 16384;
        PIN(aa); PIN(vl16);
        if (w == 0) wait_flags(flags + FL_2 * 32, 16, epoch, lane);
        BAR();
        if (tid == 0 && c == 0) __hip_atomic_store(flags + FL_GEN * 32, epoch, __ATOMIC_RELAXED, __HIP_MEMORY_SCOPE_AGENT);
        if (active) {
#define ISSUE_A(pr) do { const unsigned sa_ = sa3 + (((pr) + rot3) & 7) * 2048; DMA_SC1(wk, L3_A + ((pr) % 3) * 16384, vl16, rs_ws, sa_, 0); DMA_SC1(wk, L3_A + ((pr) % 3) * 16384 + 8192, vl16, rs_ws, sa_, 1024); } while (0)
            ISSUE_A(0); ISSUE_A(1);
            WAIT_VM(2);
            BAR();
            if (w < 4) {
                f32x4 acc[8][3], cs[3] = {Z4, Z4, Z4};
#pragma unroll
                for (int t = 0; t < 8; ++t)
#pragma unroll
                    for (int i = 0; i < 3; ++i) acc[t][i] = Z4;
                u32x4 af[2][8], bf[2][3];
#define LOADM(s) do { const int pr_ = (s) >> 1, k2_ = (s) & 1; \
                    bf[k2_][0] = tr_frag(ba[0] + ((pr_ & 3) * 2 + k2_) * L3_BSLOT, 768); \
                    _Pragma("unroll") for (int t = 0; t < 8; ++t) af[k2_][t] = lds128(aa + (pr_ % 3) * 16384 + k2_ * 8192 + t * 1024); \
                    bf[k2_][1] = tr_frag(ba[1] + ((pr_ & 3) * 2 + k2_) * L3_BSLOT, 768); bf[k2_][2] = tr_frag(ba[2] + ((pr_ & 3) * 2 + k2_) * L3_BSLOT, 768); } while (0)
                LOADM(0);
#pragma unroll
                for (int s = 0; s < 16; ++s) {
                    if (s & 1) { if (s == 1) WAIT_VM(0); BAR(); }
                    if (s < 15) LOADM(s + 1);
#pragma unroll
                    for (int i = 0; i < 3; ++i) {
#pragma unroll
                        for (int t = 0; t < 8; ++t) acc[t][i] = mfma32(bf[s & 1][i], af[s & 1][t], acc[t][i]);
                        cs[i] = mfma32(bf[s & 1][i], ONES, cs[i]);
                    }
#pragma unroll
                    for (int i = 0; i < 14; ++i) { __builtin_amdgcn_sched_group_barrier(0x8, 1, 0); __builtin_amdgcn_sched_group_barrier(0x100, 1, 0); }
                    __builtin_amdgcn_sched_group_barrier(0x8, 13, 0);
                    __builtin_amdgcn_sched_barrier(0);
                }
                const unsigned hi = lane >> 5;
                const unsigned tau = 2 * (g & 1) + (g >> 1);
#pragma unroll
                for (int m = 0; m < 2; ++m) {
                    f32x2 st[4];
#pragma unroll
                    for (int t = 0; t < 4; ++t) st[t] = lds64f(L3_ST + 512 + m * 8192 + t * 128 + r16 * 8);
                    const float r01 = hi ? st[1].y : st[0].y, r23 = hi ? st[3].y : st[2].y;
                    const float rr01 = r01 * r01, rr23 = r23 * r23;
#pragma unroll
                    for (int i = 0; i < 3; ++i) {
                        const f32x4 cv = cs[i];
                        u32x2 hp[2];
#pragma unroll
                        for (int pp = 0; pp < 2; ++pp) {
                            const float m0_ = -st[2 * pp].x, m1_ = -st[2 * pp + 1].x;
                            const f32x4 e0 = __builtin_elementwise_fma(cv, (f32x4){m0_, m0_, m0_, m0_}, acc[4 * m + 2 * pp][i]);
                            const f32x4 e1 = __builtin_elementwise_fma(cv, (f32x4){m1_, m1_, m1_, m1_}, acc[4 * m + 2 * pp + 1][i]);
                            f32x4 gt, up;
#pragma unroll
                            for (int r = 0; r < 4; ++r) {
                                auto sw = __builtin_amdgcn_permlane32_swap(f2u(e0[r]), f2u(e1[r]), false, false);
                                const unsigned x0 = sw[0], x1 = sw[1];
                                gt[r] = u2f((unsigned)max((int)x0, 0)); up[r] = u2f(x1);
                            }
                            const f32x4 hv = gt * (up * (pp ? rr23 : rr01));
                            hp[pp] = (u32x2){pk_bf16(hv[0], hv[1]), pk_bf16(hv[2], hv[3])};
                        }
                        auto s0 = __builtin_amdgcn_permlane16_swap(hp[0].x, hp[1].x, false, false);
                        auto s1 = __builtin_amdgcn_permlane16_swap(hp[0].y, hp[1].y, false, false);
                        const u32x4 hf4 = {s0[0], s1[0], s0[1], s1[1]};
                        const unsigned U = 12 * c + 3 * n3 + i;
                        const unsigned soff = WS_HID + ((4 * m) * NKS4 + (U >> 2)) * 1024 + (U & 3) * 256;
                        const unsigned off = U < 172 ? tau * (NKS4 * 1024) + r16 * 16 + soff : 0xFFFFFFF0u;
                        __builtin_amdgcn_raw_buffer_store_b128(hf4, rs_ws, off, 0, AUX_SC1);
                    }
                }
                asm volatile("" ::: "memory");
            } else {
                const unsigned lw = w - 4, wk0 = lw * 1024, sa30 = WS_H + lw * 16384;
#define A4(pr) do { const unsigned sa_ = sa30 + (((pr) + rot3) & 7) * 2048; const int o_ = L3_A + ((pr) % 3) * 16384; \
                    DMA_SC1(wk0, o_, vl16, rs_ws, sa_, 0); DMA_SC1(wk0, o_ + 8192, vl16, rs_ws, sa_, 1024); DMA_SC1(wk0, o_ + 4096, vl16, rs_ws, sa_, 65536); DMA_SC1(wk0, o_ + 12288, vl16, rs_ws, sa_, 65536 + 1024); } while (0)
#define B6(pr) do { const unsigned sb_ = (((pr) + rot3) & 7) * (64 * FF * 2); const int o_ = ((pr) & 3) * 2 * L3_BSLOT; \
                    _Pragma("unroll") for (int i_ = 0; i_ < 3; ++i_) DMA(mg0[i_], o_, vg0[i_], rs_g, sb_, 0); \
                    _Pragma("unroll") for (int i_ = 0; i_ < 3; ++i_) DMA(mg0[i_], o_ + 6144, vg1[i_], rs_u, sb_, 0); } while (0)
#define D4(s0) do { DMA(wk0, (s0) * 4096, vwd, rs_wd, swd0, KS4(s0) * 32768); DMA(wk0, (s0) * 4096 + 4096, vwd, rs_wd, swd0, KS4(s0) * 32768 + 32768); } while (0)
                const unsigned sa = L3_ST + (lane & 3) * 2048 + (lw * 16 + (lane >> 2)) * 8;
#pragma unroll
                for (int s = 0; s < 16; ++s) {
                    if (s & 1) {
                        if (s == 1 || s == 3) WAIT_VM(20); else if (s < 13) WAIT_VM(16); else if (s == 13) WAIT_VM(12);
                        BAR();
                        const int po = s >> 1;
                        if (po + 3 < 8) A4(po + 3 < 8 ? po + 3 : 7);
                        if (po + 4 < 8) B6(po + 4 < 8 ? po + 4 : 7);
                        if (po >= 4 && po < 7) { D4(12 + 6 * (po >= 4 ? po - 4 : 0)); D4(14 + 6 * (po >= 4 ? po - 4 : 0)); D4(16 + 6 * (po >= 4 ? po - 4 : 0)); }
                        if (s == 15) {
                            DMA(wk0, 30 * 4096, vwd, rs_wd, swd0, 0); DMA(wk0, 31 * 4096, vwd, rs_wd, swd0, 11 * 32768);
                            DMA(wk0, 32 * 4096, vwd, rs_wd, swd0, 22 * 32768); DMA(wk0, 33 * 4096, vwd, rs_wd, swd0, 36 * 32768);
                            D4(0); D4(2); D4(4);
                        }
                    }
                    if (s == 0) {
                        B6(2); A4(2); B6(3);
                        const unsigned sst = WS_ST + lw * 2048;
                        DMA_SC1(wk0 + wk0, L3_ST, vl16, rs_ws, sst, 0); DMA_SC1(wk0 + wk0, L3_ST + 1024, vl16, rs_ws, sst, 1024);
                        DMA_SC1(wk0 + wk0, L3_ST + 8192, vl16, rs_ws, sst, 8192); DMA_SC1(wk0 + wk0, L3_ST + 8192 + 1024, vl16, rs_ws, sst, 8192 + 1024);
                    }
                    if (s == 5) {
#pragma unroll
                        for (int hfz = 0; hfz < 2; ++hfz) {
                            const unsigned a = sa + hfz * 8192;
                            const f32x2 sm = (lds64f(a) + lds64f(a + 512)) + (lds64f(a + 1024) + lds64f(a + 1536));
                            float sx = sm.x, sq = sm.y;
                            sx += __builtin_amdgcn_update_dpp(0.f, sx, 0xB1, 0xf, 0xf, false); sq += __builtin_amdgcn_update_dpp(0.f, sq, 0xB1, 0xf, 0xf, false);
                            sx += __builtin_amdgcn_update_dpp(0.f, sx, 0x4E, 0xf, 0xf, false); sq += __builtin_amdgcn_update_dpp(0.f, sq, 0x4E, 0xf, 0xf, false);
                            *(LAS f32x2*)((a & ~(unsigned)6144) + 512) = ln_stats(sx, sq);
                        }
                    }
                }
            }
        }
    }
    {
        const bool active = c < 15;
        if (!active) {
            ISSUE_D2(12); ISSUE_D2(14); ISSUE_D2(16); ISSUE_D2(18); ISSUE_D2(20); ISSUE_D2(22); ISSUE_D2(24); ISSUE_D2(26); ISSUE_D2(28);
            ISSUE_S();
            ISSUE_D2(0); ISSUE_D2(2); ISSUE_D2(4);
        }
        WAIT_VM(0);
        BAR();
        if (tid == 0 && active) __hip_atomic_store(flags + (FL_3 + c) * 32, epoch, __ATOMIC_RELAXED, __HIP_MEMORY_SCOPE_AGENT);
#define REST_D() do { ISSUE_D2(6); ISSUE_D2(8); ISSUE_D2(10); ISSUE_D2(34); ISSUE_D2(36); ISSUE_D2(38); } while (0)
        if (w != 0) REST_D();
        const unsigned wb0 = (8 * g + q4) * 128 + (fsw * 32) + 8 * p4;
        u32x4 bq[11][4];
#pragma unroll
        for (int ct = 0; ct < 4; ++ct) {
            const u32x4 t0 = tr_frag((wb0 ^ (ct * 32)) + (30 + (kq4 < 3 ? kq4 : 0)) * 4096, 512);
            bq[0][ct] = kq4 < 3 ? t0 : (u32x4){0u, 0u, 0u, 0u};
        }
        BAR();
#define LAST_D() do { ISSUE_D2(30); DMA(wk, 32 * 4096, vwd, rs_wd, swd, 35 * 32768); } while (0)
        if (w != 0) LAST_D();
        unsigned wbk = wb0 + kq4 * (10 * 4096);
        unsigned abase = lane * 16;
        const unsigned asoff = WS_HID + ((4 * hf + 2 * rp4) * NKS4 + 11 * kq4 - (kq4 == 3 ? 1 : 0)) * 1024;
        PIN(wbk); PIN(abase);
        if (w == 0) wait_flags(flags + FL_3 * 32, 15, epoch, lane);
        BAR();
        u32x4 af[2][11];
#define LOADA4(k) do { af[0][k] = __builtin_amdgcn_raw_buffer_load_b128(rs_ws, abase + ((k) & 3) * 1024, asoff + ((k) >> 2) * 4096, AUX_SC1); \
                       af[1][k] = __builtin_amdgcn_raw_buffer_load_b128(rs_ws, abase + ((k) & 3) * 1024, asoff + NKS4 * 1024 + ((k) >> 2) * 4096, AUX_SC1); } while (0)
        if (w == 0) { REST_D(); LAST_D(); }
        LOADA4(0); LOADA4(1); LOADA4(2);
        WAIT_VM(6);
        BAR();
#define LOAD4(j) do { _Pragma("unroll") for (int ct = 0; ct < 4; ++ct) bq[j][ct] = tr_frag((wbk ^ (ct * 32)) + ((j) - 1) * 4096, 512); } while (0)
        LOAD4(1); LOAD4(2);
        __builtin_amdgcn_sched_barrier(0);
        f32x4 acc[2][4] = {{Z4, Z4, Z4, Z4}, {Z4, Z4, Z4, Z4}};
#pragma unroll
        for (int j = 0; j < 11; ++j) {
            if (j + 3 < 11) { LOADA4(j + 3); LOAD4(j + 3); }
#pragma unroll
            for (int rl = 0; rl < 2; ++rl)
#pragma unroll
                for (int ct = 0; ct < 4; ++ct) acc[rl][ct] = mfma32(bq[j][ct], af[rl][j], acc[rl][ct]);
#pragma unroll
            for (int i = 0; i < 8; ++i) { __builtin_amdgcn_sched_group_barrier(0x8, 1, 0); __builtin_amdgcn_sched_group_barrier(0x100, 1, 0); }
            __builtin_amdgcn_sched_barrier(0);
        }
        BAR();
        const unsigned red = w * 8192 + lane * 16;
#pragma unroll
        for (int rl = 0; rl < 2; ++rl)
#pragma unroll
            for (int ct = 0; ct < 4; ++ct) *(LAS f32x4*)(red + (rl * 4 + ct) * 1024) = acc[rl][ct];
        BAR();
        f32x4 a0 = Z4, a1 = Z4;
#pragma unroll
        for (int o = 0; o < 4; ++o) {
            const unsigned ra = (2 * rp4 + (o & 1) + 4 * (o >> 1)) * 8192 + lane * 16 + ((rt4 & 1) * 4 + 2 * kh) * 1024;
            a0 += lds128f(ra); a1 += lds128f(ra + 1024);
        }
        auto s0 = __builtin_amdgcn_permlane16_swap(pk_bf16(a0[0], a0[1]), pk_bf16(a1[0], a1[1]), false, false);
        auto s1 = __builtin_amdgcn_permlane16_swap(pk_bf16(a0[2], a0[3]), pk_bf16(a1[2], a1[3]), false, false);
        const u32x4 at = {s0[0], s1[0], s0[1], s1[1]};
        u32x4 o;
#pragma unroll
        for (int i = 0; i < 4; ++i) o[i] = pk_bf16(bf_lo(at[i]) + bf_lo(h_keep[i]), bf_hi(at[i]) + bf_hi(h_keep[i]));
        *(u32x4*)((char*)P.out + tok_off) = o;
    }
}

}

extern "C" void kernel_launch(void* const* d_in, const int* in_sizes, int n_in, void* d_out, int out_size, void* d_ws, size_t ws_size, hipStream_t stream) {
    Params P;
    P.x = (const u16*)d_in[0]; P.wqkv = (const u16*)d_in[1]; P.wo = (const u16*)d_in[2];
    P.wg = (const u16*)d_in[4]; P.wu = (const u16*)d_in[5]; P.wd = (const u16*)d_in[6];
    P.out = (u16*)d_out; P.ws = (unsigned char*)d_ws;
    (void)hipFuncSetAttribute((const void*)fused_block, hipFuncAttributeMaxDynamicSharedMemorySize, LDS_BYTES);
    hipLaunchKernelGGL(fused_block, dim3(256), dim3(512), LDS_BYTES, stream, P);
}
```
